```python
import torch

def _softmax(x, axis=-1):
    return torch.softmax(x, dim=axis)
def _gelu(x):
    return torch.nn.functional.gelu(x, approximate="tanh")
def _silu(x):
    return torch.nn.functional.silu(x)
def _sigmoid(x):
    return torch.sigmoid(x)
def _layernorm(x, axis=-1, eps=1e-5):
    m = x.mean(dim=axis, keepdim=True)
    v = x.var(dim=axis, keepdim=True, unbiased=False)
    return (x - m) / torch.sqrt(v + eps)
def _scatter_add(tgt, idx, src, axis=0):
    return tgt.clone().index_add_(axis, idx, src)
def _segment_sum(src, seg, n):
    out = torch.zeros((n,) + src.shape[1:], dtype=src.dtype)
    return out.index_add_(0, seg, src)
def _pad(x, pw):
    flat = tuple(v for pair in reversed(pw) for v in pair)
    return torch.nn.functional.pad(x, flat)

kernel_name = 'parallel_sum__linear_kernel_hedgehog__dense_activation_squared_relu__prompt'

def setup_inputs():

    _w = (lambda a: (lambda t: t.to(torch.bfloat16) if t.is_floating_point() else t)(torch.as_tensor(a)))
    def _A_setup():
        B, S, D, H = 1, 2048, 640, 32
        Dh = D // H
        return {
            "x": _w((lambda *s: torch.randn(*s, dtype=torch.bfloat16) * 0.05)(B, S, D)),
            "wqkv": _w((lambda *s: torch.randn(*s, dtype=torch.bfloat16) * 0.8)(D, 3 * D)),
            "w_phi": _w((lambda *s: torch.randn(*s, dtype=torch.bfloat16) * 0.8)(H, Dh, Dh) * (Dh ** -0.5)),
            "wo": _w((lambda *s: torch.randn(*s, dtype=torch.bfloat16) * 0.2)(D, D)),
        }
    _A_in = _A_setup()

    def _B_setup():
        B, S, D, F = 1, 2048, 640, 1536
        return {
            "x": _w((lambda *s: torch.randn(*s, dtype=torch.bfloat16) * 0.05)(B, S, D)),
            "w1": _w((lambda *s: torch.randn(*s, dtype=torch.bfloat16) * 0.05)(D, F)),
            "b1": _w((lambda *s: torch.randn(*s, dtype=torch.bfloat16) * 0.8)(F)),
            "w2": _w((lambda *s: torch.randn(*s, dtype=torch.bfloat16) * 0.05)(F, D)),
            "b2": _w((lambda *s: torch.randn(*s, dtype=torch.bfloat16) * 3.2)(D)),
        }
    _B_in = _B_setup()

    return {
        "A_x": _A_in["x"],
        "A_wqkv": _A_in["wqkv"],
        "A_w_phi": _A_in["w_phi"],
        "A_wo": _A_in["wo"],
        "B_x": _B_in["x"],
        "B_w1": _B_in["w1"],
        "B_b1": _B_in["b1"],
        "B_w2": _B_in["w2"],
        "B_b2": _B_in["b2"],
    }

def reference(A_x, A_wqkv, A_w_phi, A_wo, B_x, B_w1, B_b1, B_w2, B_b2):
    def _A_ref(x, wqkv, w_phi, wo):
        B, S, D = x.shape
        H = 32; Dh = D // H
        qkv = (lambda x, *d: x.permute(*d))((x @ wqkv).reshape(B, S, 3, H, Dh), 2, 0, 3, 1, 4)
        q, k, v = qkv[0], qkv[1], qkv[2]
        pq = torch.einsum("bhsd,hdf->bhsf", q, w_phi)
        pk = torch.einsum("bhsd,hdf->bhsf", k, w_phi)
        phi_q = torch.concatenate([_softmax(pq, axis=-1), _softmax(-pq, axis=-1)], axis=-1)
        phi_k = torch.concatenate([_softmax(pk, axis=-1), _softmax(-pk, axis=-1)], axis=-1)
        kv = torch.einsum("bhsf,bhsd->bhfd", phi_k, v)
        z  = torch.einsum("bhsf->bhf", phi_k)
        num = torch.einsum("bhqf,bhfd->bhqd", phi_q, kv)
        den = torch.einsum("bhqf,bhf->bhq", phi_q, z)[..., None] + 1e-6
        o = num / den
        return (lambda x, *d: x.permute(*d))(o, 0, 2, 1, 3).reshape(B, S, D) @ wo

    def _B_ref(x, w1, b1, w2, b2):
        h = torch.einsum("bsd,df->bsf", x, w1) + b1
        zero = torch.zeros_like(h)
        r = torch.where(h > zero, h, zero)
        h = r * r
        out = torch.einsum("bsf,fd->bsd", h, w2) + b2
        return out

    x = A_x
    xn = _layernorm(x, axis=-1)
    return x + _A_ref(xn, A_wqkv, A_w_phi, A_wo) + _B_ref(xn, B_w1, B_b1, B_w2, B_b2)
```

```cpp
#include <hip/hip_runtime.h>
#include <hip/hip_bf16.h>
#define RND 1
typedef unsigned short u16;
__device__ __forceinline__ float b2f(u16 b){ return __uint_as_float(((unsigned)b)<<16); }
__device__ __forceinline__ u16 f2b(float f){ unsigned u=__float_as_uint(f); u += 0x7fffu + ((u>>16)&1u); return (u16)(u>>16); }
__device__ __forceinline__ float rb(float f){ return RND ? b2f(f2b(f)) : f; }
constexpr int S=2048,D=640,H=32,DH=20,F=1536;

__global__ void k_ln(const u16* x, float* xn){
  int r=blockIdx.x; int t=threadIdx.x;
  float v[10]; float s=0;
  for(int i=0;i<10;i++){ v[i]=b2f(x[r*D+t+64*i]); s+=v[i]; }
  for(int o=32;o>0;o>>=1) s+=__shfl_xor(s,o);
  float m=s/D; float q=0;
  for(int i=0;i<10;i++){ float d=v[i]-m; q+=d*d; }
  for(int o=32;o>0;o>>=1) q+=__shfl_xor(q,o);
  float var=rb(q/D); float mb=rb(m);
  float den=rb(sqrtf(rb(var+1e-5f)));
  for(int i=0;i<10;i++) xn[r*D+t+64*i]=rb(rb(v[i]-mb)/den);
}
__global__ void k_gemm(const float* A,const u16* B,float* C,int N,int K){
  __shared__ float As[16][65];
  int n=blockIdx.x*64+(threadIdx.x&63); int m0=blockIdx.y*16; int ty=threadIdx.x>>6;
  float acc[4]={0,0,0,0};
  for(int k0=0;k0<K;k0+=64){
    for(int i=threadIdx.x;i<16*64;i+=256) As[i>>6][i&63]=A[(size_t)(m0+(i>>6))*K+k0+(i&63)];
    __syncthreads();
    for(int k=0;k<64;k++){ float b=b2f(B[(size_t)(k0+k)*N+n]); for(int j=0;j<4;j++) acc[j]+=As[ty*4+j][k]*b; }
    __syncthreads();
  }
  for(int j=0;j<4;j++) C[(size_t)(m0+ty*4+j)*N+n]=rb(acc[j]);
}
__global__ __launch_bounds__(256) void k_phi(const float* qkv,const u16* wphi,float* phi){
  int i=blockIdx.x*blockDim.x+threadIdx.x; int w=i/(S*H); int s=(i/H)%S; int h=i%H;
  float q[DH],p[DH];
  #pragma unroll
  for(int d=0;d<DH;d++) q[d]=qkv[(size_t)s*3*D+w*D+h*DH+d];
  float mx=-1e30f,mn=1e30f;
  #pragma unroll
  for(int f=0;f<DH;f++){ float a=0;
    #pragma unroll
    for(int d=0;d<DH;d++) a+=q[d]*b2f(wphi[h*DH*DH+d*DH+f]);
    p[f]=rb(a); mx=fmaxf(mx,p[f]); mn=fminf(mn,p[f]); }
  float s1=0,s2=0;
  #pragma unroll
  for(int f=0;f<DH;f++){ s1+=expf(p[f]-mx); s2+=expf(mn-p[f]); }
  #pragma unroll
  for(int f=0;f<DH;f++){ phi[(size_t)i*40+f]=rb(expf(p[f]-mx)/s1); phi[(size_t)i*40+20+f]=rb(expf(mn-p[f])/s2); }
}
__global__ void k_kv(const float* qkv,const float* phi,float* kv){
  int h=blockIdx.x; int f=blockIdx.y; int t=threadIdx.x;
  float a[21];
  #pragma unroll
  for(int d=0;d<21;d++) a[d]=0;
  for(int s=t;s<S;s+=64){ float p=phi[((size_t)(S*H)+(size_t)s*H+h)*40+f];
    #pragma unroll
    for(int d=0;d<20;d++) a[d]+=p*qkv[(size_t)s*3*D+2*D+h*DH+d];
    a[20]+=p; }
  #pragma unroll
  for(int d=0;d<21;d++){ float v=a[d]; for(int o=32;o>0;o>>=1) v+=__shfl_xor(v,o); if(t==0) kv[(h*40+f)*21+d]=rb(v); }
}
__global__ void k_o(const float* phi,const float* kv,float* o){
  int i=blockIdx.x*blockDim.x+threadIdx.x; int s=i/H; int h=i%H;
  float a[21];
  #pragma unroll
  for(int d=0;d<21;d++) a[d]=0;
  for(int f=0;f<40;f++){ float p=phi[(size_t)i*40+f];
    #pragma unroll
    for(int d=0;d<21;d++) a[d]+=p*kv[(h*40+f)*21+d]; }
  float den=rb(rb(a[20])+1e-6f);
  #pragma unroll
  for(int d=0;d<20;d++) o[(size_t)s*D+h*DH+d]=rb(rb(a[d])/den);
}
__global__ void k_act(float* h,const u16* b1){
  int i=blockIdx.x*blockDim.x+threadIdx.x; float v=rb(h[i]+b2f(b1[i%F])); v=v>0?v:0; h[i]=rb(v*v);
}
__global__ void k_fin(const u16* x,const float* a,const float* f,const u16* b2,u16* out){
  int i=blockIdx.x*blockDim.x+threadIdx.x; float t=rb(b2f(x[i])+a[i]); float u=rb(f[i]+b2f(b2[i%D])); out[i]=f2b(t+u);
}
extern "C" void kernel_launch(void* const* d_in, const int* in_sizes, int n_in,
                              void* d_out, int out_size, void* d_ws, size_t ws_size, hipStream_t stream) {
  const u16* x=(const u16*)d_in[0]; const u16* wqkv=(const u16*)d_in[1]; const u16* wphi=(const u16*)d_in[2]; const u16* wo=(const u16*)d_in[3];
  const u16* w1=(const u16*)d_in[5]; const u16* b1=(const u16*)d_in[6]; const u16* w2=(const u16*)d_in[7]; const u16* b2=(const u16*)d_in[8];
  float* ws=(float*)d_ws;
  float* xn=ws; ws+=S*D; float* qkv=ws; ws+=S*3*D; float* phi=ws; ws+=2*S*H*40; float* kv=ws; ws+=H*40*21+64; float* o=ws; ws+=S*D;
  float* hh=ws; ws+=S*F; float* ao=ws; ws+=S*D; float* fo=ws; ws+=S*D;
  k_ln<<<S,64,0,stream>>>(x,xn);
  k_gemm<<<dim3(3*D/64,S/16),256,0,stream>>>(xn,wqkv,qkv,3*D,D);
  k_phi<<<2*S*H/256,256,0,stream>>>(qkv,wphi,phi);
  k_kv<<<dim3(H,40),64,0,stream>>>(qkv,phi,kv);
  k_o<<<S*H/256,256,0,stream>>>(phi,kv,o);
  k_gemm<<<dim3(D/64,S/16),256,0,stream>>>(o,wo,ao,D,D);
  k_gemm<<<dim3(F/64,S/16),256,0,stream>>>(xn,w1,hh,F,D);
  k_act<<<S*F/256,256,0,stream>>>(hh,b1);
  k_gemm<<<dim3(D/64,S/16),256,0,stream>>>(hh,w2,fo,D,F);
  k_fin<<<S*D/256,256,0,stream>>>(x,ao,fo,b2,(u16*)d_out);
}
```

```cpp
#include <hip/hip_runtime.h>
#include <hip/hip_bf16.h>
#define RND 1
typedef unsigned short u16;
__device__ __forceinline__ float b2f(u16 b){ return __uint_as_float(((unsigned)b)<<16); }
__device__ __forceinline__ u16 f2b(float f){ unsigned u=__float_as_uint(f); u += 0x7fffu + ((u>>16)&1u); return (u16)(u>>16); }
__device__ __forceinline__ float rb(float f){ return RND ? b2f(f2b(f)) : f; }
constexpr int S=2048,D=640,H=32,DH=20,F=1536;

__global__ void k_ln(const u16* x, float* xn){
  int r=blockIdx.x; int t=threadIdx.x;
  float v[10]; float s=0;
  for(int i=0;i<10;i++){ v[i]=b2f(x[r*D+t+64*i]); s+=v[i]; }
  for(int o=32;o>0;o>>=1) s+=__shfl_xor(s,o);
  float m=s/D; float q=0;
  for(int i=0;i<10;i++){ float d=v[i]-m; q+=d*d; }
  for(int o=32;o>0;o>>=1) q+=__shfl_xor(q,o);
  float var=rb(q/D); float mb=rb(m);
  float den=rb(sqrtf(rb(var+1e-5f)));
  for(int i=0;i<10;i++) xn[r*D+t+64*i]=rb(rb(v[i]-mb)/den);
}
__global__ void k_gemm(const float* A,const u16* B,float* C,int N,int K){
  __shared__ float As[16][65];
  int n=blockIdx.x*64+(threadIdx.x&63); int m0=blockIdx.y*16; int ty=threadIdx.x>>6;
  float acc[4]={0,0,0,0};
  for(int k0=0;k0<K;k0+=64){
    for(int i=threadIdx.x;i<16*64;i+=256) As[i>>6][i&63]=A[(size_t)(m0+(i>>6))*K+k0+(i&63)];
    __syncthreads();
    for(int k=0;k<64;k++){ float b=b2f(B[(size_t)(k0+k)*N+n]); for(int j=0;j<4;j++) acc[j]+=As[ty*4+j][k]*b; }
    __syncthreads();
  }
  for(int j=0;j<4;j++) C[(size_t)(m0+ty*4+j)*N+n]=rb(acc[j]);
}
__global__ __launch_bounds__(256) void k_phi(const float* qkv,const u16* wphi,float* phi){
  int i=blockIdx.x*blockDim.x+threadIdx.x; int w=i/(S*H); int s=(i/H)%S; int h=i%H;
  float q[DH],p[DH];
  #pragma unroll
  for(int d=0;d<DH;d++) q[d]=qkv[(size_t)s*3*D+w*D+h*DH+d];
  float mx=-1e30f,mn=1e30f;
  #pragma unroll
  for(int f=0;f<DH;f++){ float a=0;
    #pragma unroll
    for(int d=0;d<DH;d++) a+=q[d]*b2f(wphi[h*DH*DH+d*DH+f]);
    p[f]=rb(a); mx=fmaxf(mx,p[f]); mn=fminf(mn,p[f]); }
  float s1=0,s2=0;
  #pragma unroll
  for(int f=0;f<DH;f++){ s1+=expf(p[f]-mx); s2+=expf(mn-p[f]); }
  #pragma unroll
  for(int f=0;f<DH;f++){ phi[(size_t)i*40+f]=rb(expf(p[f]-mx)/s1); phi[(size_t)i*40+20+f]=rb(expf(mn-p[f])/s2); }
}
__global__ void k_kv(const float* qkv,const float* phi,float* kv){
  int h=blockIdx.x; int f=blockIdx.y; int t=threadIdx.x;
  float a[21];
  #pragma unroll
  for(int d=0;d<21;d++) a[d]=0;
  for(int s=t;s<S;s+=64){ float p=phi[((size_t)(S*H)+(size_t)s*H+h)*40+f];
    #pragma unroll
    for(int d=0;d<20;d++) a[d]+=p*qkv[(size_t)s*3*D+2*D+h*DH+d];
    a[20]+=p; }
  #pragma unroll
  for(int d=0;d<21;d++){ float v=a[d]; for(int o=32;o>0;o>>=1) v+=__shfl_xor(v,o); if(t==0) kv[(h*40+f)*21+d]=rb(v); }
}
__global__ void k_o(const float* phi,const float* kv,float* o){
  int i=blockIdx.x*blockDim.x+threadIdx.x; int s=i/H; int h=i%H;
  float a[21];
  #pragma unroll
  for(int d=0;d<21;d++) a[d]=0;
  for(int f=0;f<40;f++){ float p=phi[(size_t)i*40+f];
    #pragma unroll
    for(int d=0;d<21;d++) a[d]+=p*kv[(h*40+f)*21+d]; }
  float den=rb(rb(a[20])+1e-6f);
  #pragma unroll
  for(int d=0;d<20;d++) o[(size_t)s*D+h*DH+d]=rb(rb(a[d])/den);
}
__global__ void k_act(float* h,const u16* b1){
  int i=blockIdx.x*blockDim.x+threadIdx.x; float v=rb(h[i]+b2f(b1[i%F])); v=v>0?v:0; h[i]=rb(v*v);
}
__global__ void k_fin(const u16* x,const float* a,const float* f,const u16* b2,u16* out){
  int i=blockIdx.x*blockDim.x+threadIdx.x; float t=rb(b2f(x[i])+a[i]); float u=rb(f[i]+b2f(b2[i%D])); out[i]=f2b(t+u);
}

typedef unsigned v4u __attribute__((ext_vector_type(4)));
#define PROBE 1
__global__ __launch_bounds__(256) void k_probe(const char* x,const char* wqkv,const char* wo,const char* w1,const char* w2,char* ws){
  const int tid=threadIdx.x,b=blockIdx.x;
  unsigned long long t0=__builtin_amdgcn_s_memrealtime();
  v4u acc={0,0,0,0};
  constexpr int AUX = PROBE==2?2:0;
  __amdgpu_buffer_rsrc_t r0=__builtin_amdgcn_make_buffer_rsrc((void*)x,0,2048*1280,0x00020000);
  __amdgpu_buffer_rsrc_t r1=__builtin_amdgcn_make_buffer_rsrc((void*)wqkv,0,640*3840,0x00020000);
  __amdgpu_buffer_rsrc_t r2=__builtin_amdgcn_make_buffer_rsrc((void*)w1,0,640*3072,0x00020000);
  __amdgpu_buffer_rsrc_t r3=__builtin_amdgcn_make_buffer_rsrc((void*)w2,0,640*3072,0x00020000);
  v4u v[8];
  int o=b*8192+tid*16; int o2=b*7680+tid*16;
  v[0]=__builtin_amdgcn_raw_buffer_load_b128(r0,o,0,AUX); v[1]=__builtin_amdgcn_raw_buffer_load_b128(r0,o+4096,0,AUX);
  v[2]=__builtin_amdgcn_raw_buffer_load_b128(r1,o,0,AUX); v[3]=__builtin_amdgcn_raw_buffer_load_b128(r1,o+4096,0,AUX);
  v[4]=__builtin_amdgcn_raw_buffer_load_b128(r2,o2,0,AUX); v[5]=__builtin_amdgcn_raw_buffer_load_b128(r2,o2+3584,0,AUX);
  v[6]=__builtin_amdgcn_raw_buffer_load_b128(r3,o2,0,AUX); v[7]=__builtin_amdgcn_raw_buffer_load_b128(r3,o2+3584,0,AUX);
  for(int i=0;i<8;i++) acc^=v[i];
  asm volatile(""::"v"(acc));
  asm volatile("s_waitcnt vmcnt(0)":::"memory");
  __builtin_amdgcn_s_barrier();
  unsigned long long t1=__builtin_amdgcn_s_memrealtime();
  unsigned long long dt=t1-t0; if(dt>5000) dt=5000;
  while(__builtin_amdgcn_s_memrealtime()-t1 < 100*dt) __builtin_amdgcn_s_sleep(4);
  if((acc.x^acc.y^acc.z^acc.w)==0x12345678) ((unsigned*)ws)[tid]=1;
}

extern "C" void kernel_launch(void* const* d_in, const int* in_sizes, int n_in,
                              void* d_out, int out_size, void* d_ws, size_t ws_size, hipStream_t stream) {
  const u16* x=(const u16*)d_in[0]; const u16* wqkv=(const u16*)d_in[1]; const u16* wphi=(const u16*)d_in[2]; const u16* wo=(const u16*)d_in[3];
  const u16* w1=(const u16*)d_in[5]; const u16* b1=(const u16*)d_in[6]; const u16* w2=(const u16*)d_in[7]; const u16* b2=(const u16*)d_in[8];
  float* ws=(float*)d_ws;
  float* xn=ws; ws+=S*D; float* qkv=ws; ws+=S*3*D; float* phi=ws; ws+=2*S*H*40; float* kv=ws; ws+=H*40*21+64; float* o=ws; ws+=S*D;
  float* hh=ws; ws+=S*F; float* ao=ws; ws+=S*D; float* fo=ws; ws+=S*D;
  k_probe<<<256,256,0,stream>>>((const char*)x,(const char*)wqkv,(const char*)wo,(const char*)w1,(const char*)w2,(char*)d_ws+(128u<<20));
  k_ln<<<S,64,0,stream>>>(x,xn);
  k_gemm<<<dim3(3*D/64,S/16),256,0,stream>>>(xn,wqkv,qkv,3*D,D);
  k_phi<<<2*S*H/256,256,0,stream>>>(qkv,wphi,phi);
  k_kv<<<dim3(H,40),64,0,stream>>>(qkv,phi,kv);
  k_o<<<S*H/256,256,0,stream>>>(phi,kv,o);
  k_gemm<<<dim3(D/64,S/16),256,0,stream>>>(o,wo,ao,D,D);
  k_gemm<<<dim3(F/64,S/16),256,0,stream>>>(xn,w1,hh,F,D);
  k_act<<<S*F/256,256,0,stream>>>(hh,b1);
  k_gemm<<<dim3(D/64,S/16),256,0,stream>>>(hh,w2,fo,D,F);
  k_fin<<<S*D/256,256,0,stream>>>(x,ao,fo,b2,(u16*)d_out);
}
```

```cpp
#include <hip/hip_runtime.h>
#include <hip/hip_bf16.h>
#define RND 1
typedef unsigned short u16;
__device__ __forceinline__ float b2f(u16 b){ return __uint_as_float(((unsigned)b)<<16); }
__device__ __forceinline__ u16 f2b(float f){ unsigned u=__float_as_uint(f); u += 0x7fffu + ((u>>16)&1u); return (u16)(u>>16); }
__device__ __forceinline__ float rb(float f){ return RND ? b2f(f2b(f)) : f; }
constexpr int S=2048,D=640,H=32,DH=20,F=1536;

__global__ void k_ln(const u16* x, float* xn){
  int r=blockIdx.x; int t=threadIdx.x;
  float v[10]; float s=0;
  for(int i=0;i<10;i++){ v[i]=b2f(x[r*D+t+64*i]); s+=v[i]; }
  for(int o=32;o>0;o>>=1) s+=__shfl_xor(s,o);
  float m=s/D; float q=0;
  for(int i=0;i<10;i++){ float d=v[i]-m; q+=d*d; }
  for(int o=32;o>0;o>>=1) q+=__shfl_xor(q,o);
  float var=rb(q/D); float mb=rb(m);
  float den=rb(sqrtf(rb(var+1e-5f)));
  for(int i=0;i<10;i++) xn[r*D+t+64*i]=rb(rb(v[i]-mb)/den);
}
__global__ void k_gemm(const float* A,const u16* B,float* C,int N,int K){
  __shared__ float As[16][65];
  int n=blockIdx.x*64+(threadIdx.x&63); int m0=blockIdx.y*16; int ty=threadIdx.x>>6;
  float acc[4]={0,0,0,0};
  for(int k0=0;k0<K;k0+=64){
    for(int i=threadIdx.x;i<16*64;i+=256) As[i>>6][i&63]=A[(size_t)(m0+(i>>6))*K+k0+(i&63)];
    __syncthreads();
    for(int k=0;k<64;k++){ float b=b2f(B[(size_t)(k0+k)*N+n]); for(int j=0;j<4;j++) acc[j]+=As[ty*4+j][k]*b; }
    __syncthreads();
  }
  for(int j=0;j<4;j++) C[(size_t)(m0+ty*4+j)*N+n]=rb(acc[j]);
}
__global__ __launch_bounds__(256) void k_phi(const float* qkv,const u16* wphi,float* phi){
  int i=blockIdx.x*blockDim.x+threadIdx.x; int w=i/(S*H); int s=(i/H)%S; int h=i%H;
  float q[DH],p[DH];
  #pragma unroll
  for(int d=0;d<DH;d++) q[d]=qkv[(size_t)s*3*D+w*D+h*DH+d];
  float mx=-1e30f,mn=1e30f;
  #pragma unroll
  for(int f=0;f<DH;f++){ float a=0;
    #pragma unroll
    for(int d=0;d<DH;d++) a+=q[d]*b2f(wphi[h*DH*DH+d*DH+f]);
    p[f]=rb(a); mx=fmaxf(mx,p[f]); mn=fminf(mn,p[f]); }
  float s1=0,s2=0;
  #pragma unroll
  for(int f=0;f<DH;f++){ s1+=expf(p[f]-mx); s2+=expf(mn-p[f]); }
  #pragma unroll
  for(int f=0;f<DH;f++){ phi[(size_t)i*40+f]=rb(expf(p[f]-mx)/s1); phi[(size_t)i*40+20+f]=rb(expf(mn-p[f])/s2); }
}
__global__ void k_kv(const float* qkv,const float* phi,float* kv){
  int h=blockIdx.x; int f=blockIdx.y; int t=threadIdx.x;
  float a[21];
  #pragma unroll
  for(int d=0;d<21;d++) a[d]=0;
  for(int s=t;s<S;s+=64){ float p=phi[((size_t)(S*H)+(size_t)s*H+h)*40+f];
    #pragma unroll
    for(int d=0;d<20;d++) a[d]+=p*qkv[(size_t)s*3*D+2*D+h*DH+d];
    a[20]+=p; }
  #pragma unroll
  for(int d=0;d<21;d++){ float v=a[d]; for(int o=32;o>0;o>>=1) v+=__shfl_xor(v,o); if(t==0) kv[(h*40+f)*21+d]=rb(v); }
}
__global__ void k_o(const float* phi,const float* kv,float* o){
  int i=blockIdx.x*blockDim.x+threadIdx.x; int s=i/H; int h=i%H;
  float a[21];
  #pragma unroll
  for(int d=0;d<21;d++) a[d]=0;
  for(int f=0;f<40;f++){ float p=phi[(size_t)i*40+f];
    #pragma unroll
    for(int d=0;d<21;d++) a[d]+=p*kv[(h*40+f)*21+d]; }
  float den=rb(rb(a[20])+1e-6f);
  #pragma unroll
  for(int d=0;d<20;d++) o[(size_t)s*D+h*DH+d]=rb(rb(a[d])/den);
}
__global__ void k_act(float* h,const u16* b1){
  int i=blockIdx.x*blockDim.x+threadIdx.x; float v=rb(h[i]+b2f(b1[i%F])); v=v>0?v:0; h[i]=rb(v*v);
}
__global__ void k_fin(const u16* x,const float* a,const float* f,const u16* b2,u16* out){
  int i=blockIdx.x*blockDim.x+threadIdx.x; float t=rb(b2f(x[i])+a[i]); float u=rb(f[i]+b2f(b2[i%D])); out[i]=f2b(t+u);
}

typedef unsigned v4u __attribute__((ext_vector_type(4)));
#define PROBE 4
__global__ __launch_bounds__(256) void k_probe(const char* x,const char* wqkv,const char* wo,const char* w1,const char* w2,char* ws){
  const int tid=threadIdx.x,b=blockIdx.x;
  v4u acc={0,0,0,0};
  {
    unsigned long long pc; asm volatile("s_getpc_b64 %0" : "=s"(pc));
    pc &= ~127ull;
    if(tid<64){ acc = *(const __attribute__((address_space(1))) v4u*)(pc + (b>>3)*1024 + tid*16); }
    asm volatile("s_waitcnt vmcnt(0)" :: "v"(acc) : "memory");
  }
  { unsigned long long tw=__builtin_amdgcn_s_memrealtime(); while(__builtin_amdgcn_s_memrealtime()-tw < 300); }
  __builtin_amdgcn_s_barrier();
  unsigned long long t0=__builtin_amdgcn_s_memrealtime();
  asm volatile("s_waitcnt lgkmcnt(0)":::"memory");
  asm volatile(".rept 31\n s_branch 255\n .rept 255\n s_nop 0\n .endr\n .endr");
  __builtin_amdgcn_s_barrier();
  unsigned long long t1=__builtin_amdgcn_s_memrealtime();
  unsigned long long dt=t1-t0; if(dt>5000) dt=5000;
  while(__builtin_amdgcn_s_memrealtime()-t1 < 100*dt) __builtin_amdgcn_s_sleep(4);
  if((acc.x^acc.y^acc.z^acc.w)==0x12345678) ((unsigned*)ws)[tid]=1;
  asm volatile(".rept 300\n s_nop 0\n .endr");
}

extern "C" void kernel_launch(void* const* d_in, const int* in_sizes, int n_in,
                              void* d_out, int out_size, void* d_ws, size_t ws_size, hipStream_t stream) {
  const u16* x=(const u16*)d_in[0]; const u16* wqkv=(const u16*)d_in[1]; const u16* wphi=(const u16*)d_in[2]; const u16* wo=(const u16*)d_in[3];
  const u16* w1=(const u16*)d_in[5]; const u16* b1=(const u16*)d_in[6]; const u16* w2=(const u16*)d_in[7]; const u16* b2=(const u16*)d_in[8];
  float* ws=(float*)d_ws;
  float* xn=ws; ws+=S*D; float* qkv=ws; ws+=S*3*D; float* phi=ws; ws+=2*S*H*40; float* kv=ws; ws+=H*40*21+64; float* o=ws; ws+=S*D;
  float* hh=ws; ws+=S*F; float* ao=ws; ws+=S*D; float* fo=ws; ws+=S*D;
  k_probe<<<256,256,0,stream>>>((const char*)x,(const char*)wqkv,(const char*)wo,(const char*)w1,(const char*)w2,(char*)d_ws+(128u<<20));
  k_ln<<<S,64,0,stream>>>(x,xn);
  k_gemm<<<dim3(3*D/64,S/16),256,0,stream>>>(xn,wqkv,qkv,3*D,D);
  k_phi<<<2*S*H/256,256,0,stream>>>(qkv,wphi,phi);
  k_kv<<<dim3(H,40),64,0,stream>>>(qkv,phi,kv);
  k_o<<<S*H/256,256,0,stream>>>(phi,kv,o);
  k_gemm<<<dim3(D/64,S/16),256,0,stream>>>(o,wo,ao,D,D);
  k_gemm<<<dim3(F/64,S/16),256,0,stream>>>(xn,w1,hh,F,D);
  k_act<<<S*F/256,256,0,stream>>>(hh,b1);
  k_gemm<<<dim3(D/64,S/16),256,0,stream>>>(hh,w2,fo,D,F);
  k_fin<<<S*D/256,256,0,stream>>>(x,ao,fo,b2,(u16*)d_out);
}
```

```cpp
#include <hip/hip_runtime.h>
#include <hip/hip_bf16.h>

__global__ __launch_bounds__(256, 1) void fused_block_kernel(const void* x, const void* wqkv, const void* wphi, const void* wo,
    const void* w1, const void* b1, const void* w2, const void* b2, void* out, void* ws) {
  __shared__ __attribute__((aligned(16))) char lds[163840];
  asm volatile("" :: "v"((unsigned)(size_t)lds));
  asm volatile(
R"ASM(
.Lstart:
s_load_dwordx16 s[4:19], %0, 0
s_load_dwordx4 s[20:23], %0, 64
s_getpc_b64 s[52:53]
.Lpc:
v_mov_b32 v1, %2
s_mov_b32 s63, %1
s_and_b32 s53, s53, 65535
s_mov_b32 s54, .Lend-.Lpc
s_mov_b32 s55, 131072
v_lshrrev_b32 v2, 4, v1
v_lshlrev_b32 v2, 7, v2
s_lshr_b32 s65, s63, 3
s_lshl_b32 s66, s65, 11
buffer_load_dword v88, v2, s[52:55], s66 offen
v_lshrrev_b32 v2, 6, v1
v_and_b32 v93, 63, v1
v_readfirstlane_b32 s56, v2
v_lshlrev_b32 v0, 4, v93
s_and_b32 s58, s63, 3
s_and_b32 s59, s65, 3
s_lshr_b32 s65, s65, 2
s_bfe_u32 s66, s63, 65538
s_lshl_b32 s57, s66, 3
s_add_u32 s57, s57, s65
s_lshl_b32 s65, s65, 1
s_add_u32 s65, s65, s66
s_lshl_b32 s66, s59, 2
s_add_u32 s65, s65, s66
s_add_u32 s65, s65, s58
s_and_b32 s65, s65, 15
s_mul_i32 s65, s65, 10
s_lshr_b32 s69, s65, 4
s_mul_i32 s61, s56, 5120
v_lshrrev_b32 v2, 3, v93
v_lshrrev_b32 v3, 4, v93
v_and_b32 v4, 7, v93
v_xor_b32 v4, v4, v3
v_lshlrev_b32 v4, 4, v4
v_mul_u32_u24 v2, 1280, v2
v_add_u32 v89, v2, v4
v_xor_b32 v90, 64, v89
v_add_u32 v90, 9216, v90
s_lshl_b32 s65, s57, 2
s_add_u32 s65, s65, s56
s_mul_i32 s65, s65, 40960
s_and_b32 s66, s58, 1
s_mul_i32 s71, s66, 20480
s_sub_u32 s72, 20480, s71
s_lshl_b32 s70, s69, 7
s_mov_b32 s26, 40960
s_mov_b32 s27, 131072
s_waitcnt lgkmcnt(0)
s_add_u32 s24, s4, s65
s_addc_u32 s25, s5, 0
s_add_u32 m0, s61, 0
s_add_u32 s66, s70, s71
buffer_load_dwordx4 v89, s[24:27], s66 offen lds
buffer_load_dwordx4 v90, s[24:27], s66 offen offset:1024 lds
s_add_u32 m0, s61, 2048
s_add_u32 s66, s70, s72
buffer_load_dwordx4 v89, s[24:27], s66 offen lds
buffer_load_dwordx4 v90, s[24:27], s66 offen offset:1024 lds
s_add_u32 s70, s70, 128
s_cmp_eq_u32 s70, 1280
s_cselect_b32 s70, 0, s70
s_mov_b32 s44, s22
s_mov_b32 s45, s23
s_mov_b32 s46, 25165824
s_mov_b32 s47, 131072
v_mov_b32 v16, 0
buffer_load_dword v83, v16, s[44:47], 0 offen sc1
s_lshr_b32 s60, s59, 1
s_lshl_b32 s66, s58, 1
s_add_u32 s60, s60, s66
s_and_b32 s67, s59, 1
s_lshl_b32 s66, s60, 2
s_add_u32 s66, s66, s56
s_mul_i32 s66, s66, 800
s_add_u32 s52, s8, s66
s_addc_u32 s53, s9, 0
s_mov_b32 s54, 800
s_mov_b32 s55, 131072
s_add_u32 m0, s61, 147456
s_nop 0
buffer_load_dwordx4 v0, s[52:55], 0 offen lds
s_mul_i32 s66, s60, 160
s_mul_i32 s65, s67, 1280
s_add_u32 s66, s66, s65
s_add_u32 s28, s6, s66
s_addc_u32 s29, s7, 0
s_sub_u32 s30, 2457600, s66
s_mov_b32 s31, 131072
s_mul_i32 s66, s60, 384
s_lshl_b32 s65, s67, 7
s_sub_u32 s66, s66, s65
s_add_u32 s66, s66, 128
s_add_u32 s32, s12, s66
s_addc_u32 s33, s13, 0
s_sub_u32 s34, 1966080, s66
s_mov_b32 s35, 131072
s_add_u32 s52, s14, s66
s_addc_u32 s53, s15, 0
s_sub_u32 s54, 3072, s66
s_mov_b32 s55, 131072
v_lshrrev_b32 v16, 2, v0
buffer_load_dword v82, v16, s[52:55], 0 offen
s_lshr_b32 s66, s58, 1
s_mul_i32 s66, s66, 160
s_mul_i32 s68, s59, 320
s_add_u32 s67, s66, s68
s_add_u32 s52, s18, s67
s_addc_u32 s53, s19, 0
s_mov_b32 s54, 160
s_mov_b32 s55, 131072
v_lshrrev_b32 v16, 4, v93
v_mul_u32_u24 v16, 40, v16
buffer_load_dwordx4 a[160:163], v16, s[52:55], 0 offen
buffer_load_dwordx4 a[164:167], v16, s[52:55], 0 offen offset:16
buffer_load_dwordx2 a[168:169], v16, s[52:55], 0 offen offset:32
s_add_u32 m0, s61, 20480
s_add_u32 s66, s70, s71
buffer_load_dwordx4 v89, s[24:27], s66 offen lds
buffer_load_dwordx4 v90, s[24:27], s66 offen offset:1024 lds
s_add_u32 m0, s61, 22528
s_add_u32 s66, s70, s72
buffer_load_dwordx4 v89, s[24:27], s66 offen lds
buffer_load_dwordx4 v90, s[24:27], s66 offen offset:1024 lds
s_add_u32 s70, s70, 128
s_cmp_eq_u32 s70, 1280
s_cselect_b32 s70, 0, s70
s_add_u32 s55, s71, s67
s_lshl_b32 s65, s57, 2
s_add_u32 s65, s65, s56
s_mul_i32 s65, s65, 40960
s_add_u32 s66, s65, s55
s_add_u32 s48, s20, s66
s_addc_u32 s49, s21, 0
s_mov_b32 s50, 19360
s_mov_b32 s51, 131072
s_mul_i32 s40, s58, 12
s_add_u32 s40, s40, s57
s_mul_i32 s40, s40, 40960
s_mul_i32 s66, s56, 160
s_add_u32 s40, s40, s66
s_add_u32 s40, s40, s68
s_cmp_ge_u32 s57, 5
s_cselect_b32 s42, 1, 0
s_mul_i32 s66, s42, 5
s_sub_u32 s43, s57, s66
s_lshr_b32 s66, s43, 1
s_mul_i32 s66, s66, 80
s_and_b32 s67, s43, 1
s_lshl_b32 s67, s67, 3
s_add_u32 s66, s66, s67
s_cmp_eq_u32 s43, 4
s_cselect_b32 s66, 16, s66
s_cselect_b32 s2, 97280, 0
s_mul_i32 s41, s58, 160
s_add_u32 s41, s41, s66
s_mul_i32 s41, s41, 1280
s_mul_i32 s66, s42, 160
s_add_u32 s41, s41, s66
s_add_u32 s41, s41, s68
s_lshl_b32 s66, s59, 1
s_add_u32 s66, s66, s42
s_mul_i32 s66, s66, 5
s_add_u32 s66, s66, s43
s_lshl_b32 s66, s66, 2
s_add_u32 s66, s66, s58
s_add_u32 s42, s66, 384
s_lshl_b32 s66, s58, 2
s_add_u32 s66, s66, s59
s_lshl_b32 s66, s66, 1
s_add_u32 s66, s66, s56
s_mul_i32 s66, s66, 12
s_add_u32 s43, s66, s57
s_cmp_lt_u32 s56, 2
s_cselect_b32 s66, s40, s41
s_cselect_b32 s3, s43, s42
s_cselect_b32 s40, s16, s10
s_cselect_b32 s41, s17, s11
s_mov_b32 s42, 819200
s_cselect_b32 s42, 1966080, s42
s_cselect_b32 s43, 12, 10
s_cselect_b64 vcc, -1, 0
s_add_u32 s36, s40, s66
s_addc_u32 s37, s41, 0
s_sub_u32 s38, s42, s66
s_mov_b32 s39, 131072
s_mul_i32 s3, s3, 5120
s_add_u32 s3, s3, 21495808
s_cmp_eq_u32 s56, 3
s_cselect_b32 s43, 0, s43
s_cmp_lt_u32 s57, s43
s_cselect_b32 s38, s38, 0
s_cselect_b32 s36, s36, s40
s_cselect_b32 s37, s37, s41
s_cselect_b32 s3, s3, 1073741824
s_mul_i32 s66, s60, 2560
s_add_u32 s66, s66, s65
s_add_u32 s66, s66, 8388608
s_add_u32 s17, s66, s71
s_add_u32 s18, s66, s72
s_and_b32 s66, s56, 1
s_mul_i32 s67, s66, 10240
s_add_u32 s67, s67, s71
s_add_u32 s67, s67, 8388608
s_add_u32 s19, s65, s67
s_mul_i32 s66, s66, 81920
s_add_u32 s20, s19, 40960
s_sub_u32 s20, s20, s66
s_lshl_b32 s63, s57, 4
s_lshl_b32 s66, s58, 2
s_add_u32 s63, s63, s66
s_add_u32 s65, s63, s56
s_add_u32 s63, s63, s59
s_mul_i32 s65, s65, 24576
s_add_u32 s65, s65, 2097152
s_lshr_b32 s66, s59, 1
s_mul_i32 s66, s66, 12288
s_sub_u32 s67, 12288, s66
s_add_u32 s14, s65, s66
s_add_u32 s15, s65, s67
s_lshr_b32 s65, s59, 1
s_lshr_b32 s66, s56, 1
s_xor_b32 s65, s65, s66
s_mul_i32 s65, s65, 6
s_and_b32 s66, s56, 1
s_add_u32 s41, s65, s66
s_and_b32 s65, s63, 15
s_mul_i32 s65, s65, 24
s_add_u32 s65, s65, s41
s_lshr_b32 s66, s58, 1
s_mul_i32 s67, s66, 12
s_add_u32 s22, s65, s67
s_sub_u32 s23, s65, s67
s_add_u32 s23, s23, 12
s_mul_i32 s22, s22, 5120
s_mul_i32 s23, s23, 5120
s_lshl_b32 s65, s59, 1
s_add_u32 s65, s65, s66
s_mul_i32 s65, s65, 20
s_add_u32 s65, s65, s56
s_mul_i32 s40, s65, 5120
s_lshl_b32 s65, s60, 2
s_add_u32 s65, s65, s56
s_lshl_b32 s65, s65, 4
s_and_b32 s66, s59, 1
s_mul_i32 s66, s66, s57
s_add_u32 s65, s65, s66
s_lshl_b32 s65, s65, 11
s_add_u32 s16, s65, 1048576
s_lshl_b32 s66, s57, 2
s_add_u32 s66, s66, s59
s_lshl_b32 s66, s66, 4
s_add_u32 s66, s66, s56
s_xor_b32 s65, s58, 0
s_lshl_b32 s65, s65, 2
s_add_u32 s65, s65, s66
s_mul_i32 s65, s65, 7680
s_add_u32 s21, s65, 11534336
s_xor_b32 s65, s58, 1
s_lshl_b32 s65, s65, 2
s_add_u32 s65, s65, s66
s_mul_i32 s65, s65, 7680
s_add_u32 s52, s65, 11534336
s_xor_b32 s65, s58, 2
s_lshl_b32 s65, s65, 2
s_add_u32 s65, s65, s66
s_mul_i32 s65, s65, 7680
s_add_u32 s53, s65, 11536896
s_xor_b32 s65, s58, 3
s_lshl_b32 s65, s65, 2
s_add_u32 s65, s65, s66
s_mul_i32 s65, s65, 7680
s_add_u32 s54, s65, 11539456
s_lshl_b32 s65, s69, 1
s_add_u32 s65, s65, s56
s_add_u32 s65, s65, 0
s_sub_u32 s66, s65, 20
s_cmp_ge_u32 s65, 20
s_cselect_b32 s73, s66, s65
s_lshl_b32 s65, s69, 1
s_add_u32 s65, s65, s56
s_add_u32 s65, s65, 4
s_sub_u32 s66, s65, 20
s_cmp_ge_u32 s65, 20
s_cselect_b32 s74, s66, s65
s_lshl_b32 s65, s69, 1
s_add_u32 s65, s65, s56
s_add_u32 s65, s65, 8
s_sub_u32 s66, s65, 20
s_cmp_ge_u32 s65, 20
s_cselect_b32 s75, s66, s65
s_lshl_b32 s65, s69, 1
s_add_u32 s65, s65, s56
s_add_u32 s65, s65, 12
s_sub_u32 s66, s65, 20
s_cmp_ge_u32 s65, 20
s_cselect_b32 s76, s66, s65
s_lshl_b32 s65, s69, 1
s_add_u32 s65, s65, s56
s_add_u32 s65, s65, 16
s_sub_u32 s66, s65, 20
s_cmp_ge_u32 s65, 20
s_cselect_b32 s77, s66, s65
s_mov_b32 s4, 1048577
s_mov_b32 s5, 268435712
s_mov_b32 s6, 2097154
s_mov_b32 s7, 536871424
s_mov_b32 s8, 4194308
s_mov_b32 s9, 1073742848
s_mov_b32 s10, 8388616
s_mov_b32 s11, 2147485696
s_mov_b32 s12, 1069066811
s_mov_b32 s13, 3216550459
v_and_b32 v16, 15, v93
v_lshrrev_b32 v17, 4, v93
v_lshrrev_b32 v18, 1, v16
v_xor_b32 v18, v18, v17
v_lshlrev_b32 v18, 4, v18
v_lshl_add_u32 v18, v16, 7, v18
v_add_u32 v91, s61, v18
v_xor_b32 v18, 64, v18
v_add_u32 v92, s61, v18
v_mov_b32 v84, 1065369472
v_mov_b32 v85, 1065369472
v_mov_b32 v86, 1065369472
v_mov_b32 v87, 1065369472
v_add_u32 v16, 0, v93
v_mul_u32_u24 v17, 6554, v16
v_lshrrev_b32 v17, 16, v17
v_mul_u32_u24 v18, 10, v17
v_sub_u32 v18, v16, v18
v_lshlrev_b32 v18, 4, v18
v_bfe_u32 v19, v17, 2, 2
v_lshrrev_b32 v20, 4, v17
v_and_b32 v21, 3, v17
v_mul_u32_u24 v17, 1280, v17
v_add_u32 v17, v17, v18
v_accvgpr_write_b32 a180, v17
v_lshl_add_u32 v22, v20, 2, v21
v_lshl_add_u32 v16, v19, 3, v22
v_mul_u32_u24 v16, 3840, v16
v_add_u32 v7, v16, v18
v_mad_u32_u24 v16, v19, 20, v22
v_mul_u32_u24 v16, 1280, v16
v_mad_u32_u24 v18, v20, s2, v18
v_add_u32 v16, v16, v18
v_cndmask_b32 v16, v16, v17, vcc
v_accvgpr_write_b32 a181, v16
v_add_u32 v16, 64, v93
v_mul_u32_u24 v17, 6554, v16
v_lshrrev_b32 v17, 16, v17
v_mul_u32_u24 v18, 10, v17
v_sub_u32 v18, v16, v18
v_lshlrev_b32 v18, 4, v18
v_subrev_u32 v18, 1024, v18
v_bfe_u32 v19, v17, 2, 2
v_lshrrev_b32 v20, 4, v17
v_and_b32 v21, 3, v17
v_mul_u32_u24 v17, 1280, v17
v_add_u32 v17, v17, v18
v_accvgpr_write_b32 a182, v17
v_lshl_add_u32 v22, v20, 2, v21
v_lshl_add_u32 v16, v19, 3, v22
v_mul_u32_u24 v16, 3840, v16
v_add_u32 v8, v16, v18
v_mad_u32_u24 v16, v19, 20, v22
v_mul_u32_u24 v16, 1280, v16
v_mad_u32_u24 v18, v20, s2, v18
v_add_u32 v16, v16, v18
v_cndmask_b32 v16, v16, v17, vcc
v_accvgpr_write_b32 a183, v16
v_add_u32 v16, 128, v93
v_mul_u32_u24 v17, 6554, v16
v_lshrrev_b32 v17, 16, v17
v_mul_u32_u24 v18, 10, v17
v_sub_u32 v18, v16, v18
v_lshlrev_b32 v18, 4, v18
v_subrev_u32 v18, 2048, v18
v_bfe_u32 v19, v17, 2, 2
v_lshrrev_b32 v20, 4, v17
v_and_b32 v21, 3, v17
v_mul_u32_u24 v17, 1280, v17
v_add_u32 v17, v17, v18
v_accvgpr_write_b32 a184, v17
v_lshl_add_u32 v22, v20, 2, v21
v_lshl_add_u32 v16, v19, 3, v22
v_mul_u32_u24 v16, 3840, v16
v_add_u32 v9, v16, v18
v_mad_u32_u24 v16, v19, 20, v22
v_mul_u32_u24 v16, 1280, v16
v_mad_u32_u24 v18, v20, s2, v18
v_add_u32 v16, v16, v18
v_cndmask_b32 v16, v16, v17, vcc
v_accvgpr_write_b32 a185, v16
v_add_u32 v16, 192, v93
v_mul_u32_u24 v17, 6554, v16
)ASM"
R"ASM(
v_lshrrev_b32 v17, 16, v17
v_mul_u32_u24 v18, 10, v17
v_sub_u32 v18, v16, v18
v_lshlrev_b32 v18, 4, v18
v_subrev_u32 v18, 3072, v18
v_bfe_u32 v19, v17, 2, 2
v_lshrrev_b32 v20, 4, v17
v_and_b32 v21, 3, v17
v_mul_u32_u24 v17, 1280, v17
v_add_u32 v17, v17, v18
v_lshl_add_u32 v22, v20, 2, v21
v_lshl_add_u32 v16, v19, 3, v22
v_mul_u32_u24 v16, 3840, v16
v_add_u32 v10, v16, v18
v_mad_u32_u24 v16, v19, 20, v22
v_mul_u32_u24 v16, 1280, v16
v_mad_u32_u24 v18, v20, s2, v18
v_add_u32 v16, v16, v18
v_cndmask_b32 v16, v16, v17, vcc
v_accvgpr_write_b32 a186, v16
v_add_u32 v16, 256, v93
v_mul_u32_u24 v17, 6554, v16
v_lshrrev_b32 v17, 16, v17
v_mul_u32_u24 v18, 10, v17
v_sub_u32 v18, v16, v18
v_lshlrev_b32 v18, 4, v18
v_bfe_u32 v19, v17, 2, 2
v_lshrrev_b32 v20, 4, v17
v_and_b32 v21, 3, v17
v_mul_u32_u24 v17, 1280, v17
v_add_u32 v17, v17, v18
v_lshl_add_u32 v22, v20, 2, v21
v_lshl_add_u32 v16, v19, 3, v22
v_mul_u32_u24 v16, 3840, v16
v_add_u32 v11, v16, v18
v_mad_u32_u24 v16, v19, 20, v22
v_mul_u32_u24 v16, 1280, v16
v_mad_u32_u24 v18, v20, s2, v18
v_add_u32 v16, v16, v18
v_cndmask_b32 v16, v16, v17, vcc
v_accvgpr_write_b32 a187, v16
v_lshrrev_b32 v23, 4, v93
v_and_b32 v24, 15, v93
v_bfe_u32 v25, v93, 2, 2
v_and_b32 v26, 3, v93
v_mov_b32 v18, 0
v_mov_b32 v19, 16256
v_mov_b32 v20, 1065353216
v_cndmask_b32 v16, v18, v19, s[4:5]
v_cndmask_b32 v16, v16, v20, s[6:7]
v_accvgpr_write_b32 a222, v16
v_cndmask_b32 v16, v18, v19, s[8:9]
v_cndmask_b32 v16, v16, v20, s[10:11]
v_accvgpr_write_b32 a223, v16
v_bfe_u32 v16, v93, 3, 2
v_lshlrev_b32 v16, 3, v16
v_lshrrev_b32 v17, 5, v93
v_lshl_add_u32 v16, v17, 2, v16
v_mul_u32_u24 v16, 3072, v16
v_and_b32 v17, 7, v93
v_lshl_add_u32 v16, v17, 4, v16
v_accvgpr_write_b32 a188, v16
v_mul_u32_u24 v16, 1056, v25
v_lshl_add_u32 v16, v23, 7, v16
v_lshl_add_u32 v16, v26, 3, v16
v_accvgpr_write_b32 a189, v16
v_lshrrev_b32 v16, 2, v93
v_mul_u32_u24 v16, 160, v16
v_mad_u32_u24 v1, v26, 40, v16
v_add_u32 v2, 61440, v1
v_add_u32 v3, 122880, v1
s_add_u32 s65, s61, 145408
v_mov_b32 v21, s65
v_mul_u32_u24 v22, 5120, v23
v_add_u32 v22, 143360, v22
v_mad_u32_u24 v16, v25, 40, v22
v_add_u32 v16, 4096, v16
v_cmp_eq_u32 vcc, v26, v23
s_nop 1
v_cndmask_b32 v16, v21, v16, vcc
v_accvgpr_write_b32 a190, v16
v_cmp_eq_u32 vcc, v25, v23
v_mul_u32_u24 v16, 80, v26
v_add_u32 v16, v16, v22
v_cndmask_b32 v16, v21, v16, vcc
v_accvgpr_write_b32 a191, v16
v_add_u32 v16, 1600, v22
v_cndmask_b32 v16, v21, v16, vcc
v_accvgpr_write_b32 a192, v16
s_add_u32 s65, s61, 146944
v_lshl_add_u32 v16, v23, 4, s65
v_accvgpr_write_b32 a193, v16
v_and_b32 v17, 7, v93
v_bfe_u32 v18, v93, 3, 1
v_mul_u32_u24 v19, 5120, v23
v_lshl_add_u32 v16, v17, 1, v18
v_mul_u32_u24 v16, 80, v16
v_add_u32 v16, v16, v19
s_and_b32 s65, s56, 1
s_mul_i32 s65, s65, 2560
v_add_u32 v16, s65, v16
v_accvgpr_write_b32 a194, v16
v_lshl_add_u32 v16, v17, 2, v18
v_mul_u32_u24 v16, 40, v16
v_add_u32 v16, v16, v19
s_mul_i32 s65, s56, 1280
v_add_u32 v16, s65, v16
v_accvgpr_write_b32 a195, v16
v_bfe_u32 v17, v93, 2, 3
v_lshrrev_b32 v18, 5, v93
v_lshl_add_u32 v19, v26, 3, s61
v_lshl_add_u32 v16, v17, 1, v18
v_mul_u32_u24 v16, 80, v16
v_add_u32 v16, v16, v19
v_accvgpr_write_b32 a196, v16
v_lshl_add_u32 v16, v17, 2, v18
v_mul_u32_u24 v16, 40, v16
v_add_u32 v16, v16, v19
v_accvgpr_write_b32 a197, v16
s_add_u32 s65, s61, 143360
v_mov_b32 v20, 1024
v_lshl_add_u32 v16, v24, 2, v23
v_lshlrev_b32 v16, 4, v16
v_cmp_gt_u32 vcc, 5, v24
s_nop 1
v_cndmask_b32 v16, v20, v16, vcc
v_add_u32 v16, s65, v16
v_accvgpr_write_b32 a198, v16
v_lshl_add_u32 v17, v24, 1, v23
v_lshlrev_b32 v17, 3, v17
v_cndmask_b32 v18, v20, v17, vcc
v_cmp_gt_u32 vcc, 2, v23
s_nop 1
v_cndmask_b32 v16, v20, v17, vcc
v_add_u32 v16, s65, v16
v_accvgpr_write_b32 a199, v16
v_cndmask_b32 v16, v20, v18, vcc
v_add_u32 v16, s65, v16
v_accvgpr_write_b32 a200, v16
v_mov_b32 v20, 1073741824
v_add_u32 v17, 1024, v0
v_cmp_gt_u32 vcc, 48, v93
s_nop 1
v_cndmask_b32 v16, v20, v17, vcc
v_accvgpr_write_b32 a201, v16
v_cmp_gt_u32 vcc, 41, v93
s_add_u32 s65, s61, 143360
v_mul_u32_u24 v16, 80, v24
v_lshl_add_u32 v16, v23, 3, v16
v_add_u32 v16, s65, v16
v_accvgpr_write_b32 a202, v16
v_mul_u32_u24 v17, 80, v93
v_subrev_u32 v17, 1536, v17
v_mov_b32 v18, 1792
v_cndmask_b32 v17, v18, v17, vcc
v_lshrrev_b32 v16, 2, v93
v_mul_u32_u24 v16, 80, v16
v_lshl_add_u32 v16, v26, 3, v16
v_add_u32 v16, 1280, v16
v_cmp_gt_u32 vcc, 20, v93
s_nop 1
v_cndmask_b32 v16, v17, v16, vcc
v_add_u32 v16, s65, v16
v_accvgpr_write_b32 a203, v16
v_mov_b32 v18, 32
v_cndmask_b32 v17, 8, v18, vcc
v_add_u32 v16, v16, v17
v_accvgpr_write_b32 a204, v16
v_add_u32 v94, s65, v0
v_accvgpr_write_b32 a205, v94
v_mul_u32_u24 v16, 160, v24
v_mad_u32_u24 v16, v23, 40, v16
v_add_u32 v16, s61, v16
v_accvgpr_write_b32 a206, v16
v_mul_u32_u24 v16, 1280, v24
v_mad_u32_u24 v16, v23, 40, v16
v_accvgpr_write_b32 a207, v16
v_lshrrev_b32 v27, 2, v93
s_lshl_b32 s66, s56, 8
s_lshl_b32 s64, s63, 10
s_add_u32 s64, s64, s66
s_add_u32 s64, s64, 19398656
s_and_b32 s65, s63, 15
s_or_b32 s65, s65, 1
s_lshl_b32 s65, s65, 10
s_add_u32 s65, s65, s66
v_and_b32 v17, 15, v27
v_lshl_add_u32 v16, v17, 14, s65
v_accvgpr_write_b32 a208, v16
s_and_b32 s65, s63, 252
s_lshl_b32 s65, s65, 10
s_add_u32 s65, s65, s66
v_and_b32 v17, 1, v27
v_lshl_add_u32 v16, v17, 11, s65
v_accvgpr_write_b32 a209, v16
s_and_b32 s65, s63, 252
s_or_b32 s65, s65, 1
s_lshl_b32 s65, s65, 10
s_add_u32 s65, s65, s66
v_and_b32 v17, 1, v27
v_lshl_add_u32 v16, v17, 11, s65
v_accvgpr_write_b32 a210, v16
s_and_b32 s65, s63, 243
s_lshl_b32 s65, s65, 10
s_add_u32 s65, s65, s66
v_and_b32 v17, 3, v27
v_lshl_add_u32 v16, v17, 12, s65
v_accvgpr_write_b32 a211, v16
s_and_b32 s65, s63, 240
s_and_b32 s67, s56, 1
s_lshl_b32 s67, s67, 3
s_add_u32 s65, s65, s67
s_lshl_b32 s65, s65, 10
s_and_b32 s67, s56, 2
s_lshl_b32 s67, s67, 8
s_add_u32 s65, s65, s67
v_lshl_add_u32 v16, v25, 11, s65
v_and_b32 v17, 1, v23
v_lshl_add_u32 v16, v17, 8, v16
v_accvgpr_write_b32 a212, v16
s_and_b32 s65, s63, 15
s_lshl_b32 s66, s41, 4
s_add_u32 s65, s65, s66
s_lshl_b32 s65, s65, 10
v_lshrrev_b32 v16, 1, v27
v_lshl_add_u32 v16, v16, 15, s65
v_and_b32 v17, 1, v27
v_lshl_add_u32 v16, v17, 8, v16
s_lshr_b32 s65, s58, 1
s_mul_i32 s65, s65, 80
s_lshl_b32 s66, s56, 2
s_add_u32 s65, s65, s66
s_add_u32 s65, s65, s59
s_lshl_b32 s65, s65, 10
s_add_u32 s65, s65, 512
v_subrev_u32 v17, 6, v27
v_min_u32 v17, 4, v17
v_lshl_add_u32 v17, v17, 14, s65
v_cmp_gt_u32 vcc, 6, v27
s_nop 1
v_cndmask_b32 v16, v17, v16, vcc
v_accvgpr_write_b32 a213, v16
s_add_u32 m0, s61, 40960
s_add_u32 s66, s70, s71
buffer_load_dwordx4 v89, s[24:27], s66 offen lds
buffer_load_dwordx4 v90, s[24:27], s66 offen offset:1024 lds
s_add_u32 m0, s61, 43008
s_add_u32 s66, s70, s72
buffer_load_dwordx4 v89, s[24:27], s66 offen lds
buffer_load_dwordx4 v90, s[24:27], s66 offen offset:1024 lds
s_add_u32 s70, s70, 128
s_cmp_eq_u32 s70, 1280
s_cselect_b32 s70, 0, s70
s_waitcnt vmcnt(14)
ds_read_b128 a[0:3], v91
ds_read_b128 a[4:7], v91 offset:2048
ds_read_b128 a[8:11], v92
ds_read_b128 a[12:15], v92 offset:2048
s_add_u32 m0, s61, 61440
s_add_u32 s66, s70, s71
buffer_load_dwordx4 v89, s[24:27], s66 offen lds
buffer_load_dwordx4 v90, s[24:27], s66 offen offset:1024 lds
s_add_u32 m0, s61, 63488
s_add_u32 s66, s70, s72
buffer_load_dwordx4 v89, s[24:27], s66 offen lds
buffer_load_dwordx4 v90, s[24:27], s66 offen offset:1024 lds
s_add_u32 s70, s70, 128
s_cmp_eq_u32 s70, 1280
s_cselect_b32 s70, 0, s70
s_waitcnt vmcnt(8)
ds_read_b128 a[16:19], v91 offset:20480
ds_read_b128 a[20:23], v91 offset:22528
ds_read_b128 a[24:27], v92 offset:20480
ds_read_b128 a[28:31], v92 offset:22528
s_add_u32 m0, s61, 81920
s_add_u32 s66, s70, s71
buffer_load_dwordx4 v89, s[24:27], s66 offen lds
buffer_load_dwordx4 v90, s[24:27], s66 offen offset:1024 lds
s_add_u32 m0, s61, 83968
s_add_u32 s66, s70, s72
buffer_load_dwordx4 v89, s[24:27], s66 offen lds
buffer_load_dwordx4 v90, s[24:27], s66 offen offset:1024 lds
s_add_u32 s70, s70, 128
s_cmp_eq_u32 s70, 1280
s_cselect_b32 s70, 0, s70
s_waitcnt lgkmcnt(7)
v_mfma_f32_16x16x32_bf16 v[64:67], v[84:87], a[0:3], 0
s_waitcnt lgkmcnt(6)
v_mfma_f32_16x16x32_bf16 v[68:71], v[84:87], a[4:7], 0
s_waitcnt lgkmcnt(5)
v_mfma_f32_16x16x32_bf16 v[64:67], v[84:87], a[8:11], v[64:67]
s_waitcnt lgkmcnt(4)
v_mfma_f32_16x16x32_bf16 v[68:71], v[84:87], a[12:15], v[68:71]
v_mfma_f32_16x16x32_bf16 v[72:75], a[0:3], a[0:3], 0
v_mfma_f32_16x16x32_bf16 v[76:79], a[4:7], a[4:7], 0
v_mfma_f32_16x16x32_bf16 v[72:75], a[8:11], a[8:11], v[72:75]
v_mfma_f32_16x16x32_bf16 v[76:79], a[12:15], a[12:15], v[76:79]
s_waitcnt vmcnt(8)
ds_read_b128 a[32:35], v91 offset:40960
ds_read_b128 a[36:39], v91 offset:43008
ds_read_b128 a[40:43], v92 offset:40960
ds_read_b128 a[44:47], v92 offset:43008
s_add_u32 m0, s61, 102400
s_add_u32 s66, s70, s71
buffer_load_dwordx4 v89, s[24:27], s66 offen lds
buffer_load_dwordx4 v90, s[24:27], s66 offen offset:1024 lds
s_add_u32 m0, s61, 104448
s_add_u32 s66, s70, s72
buffer_load_dwordx4 v89, s[24:27], s66 offen lds
buffer_load_dwordx4 v90, s[24:27], s66 offen offset:1024 lds
s_add_u32 s70, s70, 128
s_cmp_eq_u32 s70, 1280
s_cselect_b32 s70, 0, s70
s_waitcnt lgkmcnt(7)
v_mfma_f32_16x16x32_bf16 v[64:67], v[84:87], a[16:19], v[64:67]
s_waitcnt lgkmcnt(6)
v_mfma_f32_16x16x32_bf16 v[68:71], v[84:87], a[20:23], v[68:71]
s_waitcnt lgkmcnt(5)
v_mfma_f32_16x16x32_bf16 v[64:67], v[84:87], a[24:27], v[64:67]
s_waitcnt lgkmcnt(4)
v_mfma_f32_16x16x32_bf16 v[68:71], v[84:87], a[28:31], v[68:71]
v_mfma_f32_16x16x32_bf16 v[72:75], a[16:19], a[16:19], v[72:75]
v_mfma_f32_16x16x32_bf16 v[76:79], a[20:23], a[20:23], v[76:79]
v_mfma_f32_16x16x32_bf16 v[72:75], a[24:27], a[24:27], v[72:75]
v_mfma_f32_16x16x32_bf16 v[76:79], a[28:31], a[28:31], v[76:79]
s_waitcnt vmcnt(8)
v_add_u32 v91, 61440, v91
v_add_u32 v92, 61440, v92
ds_read_b128 a[48:51], v91
ds_read_b128 a[52:55], v91 offset:2048
ds_read_b128 a[56:59], v92
ds_read_b128 a[60:63], v92 offset:2048
s_add_u32 m0, s61, 122880
s_add_u32 s66, s70, s71
buffer_load_dwordx4 v89, s[24:27], s66 offen lds
buffer_load_dwordx4 v90, s[24:27], s66 offen offset:1024 lds
s_add_u32 m0, s61, 124928
s_add_u32 s66, s70, s72
buffer_load_dwordx4 v89, s[24:27], s66 offen lds
buffer_load_dwordx4 v90, s[24:27], s66 offen offset:1024 lds
s_add_u32 s70, s70, 128
s_cmp_eq_u32 s70, 1280
s_cselect_b32 s70, 0, s70
s_waitcnt lgkmcnt(7)
v_mfma_f32_16x16x32_bf16 v[64:67], v[84:87], a[32:35], v[64:67]
s_waitcnt lgkmcnt(6)
v_mfma_f32_16x16x32_bf16 v[68:71], v[84:87], a[36:39], v[68:71]
s_waitcnt lgkmcnt(5)
v_mfma_f32_16x16x32_bf16 v[64:67], v[84:87], a[40:43], v[64:67]
s_waitcnt lgkmcnt(4)
v_mfma_f32_16x16x32_bf16 v[68:71], v[84:87], a[44:47], v[68:71]
v_mfma_f32_16x16x32_bf16 v[72:75], a[32:35], a[32:35], v[72:75]
v_mfma_f32_16x16x32_bf16 v[76:79], a[36:39], a[36:39], v[76:79]
v_mfma_f32_16x16x32_bf16 v[72:75], a[40:43], a[40:43], v[72:75]
v_mfma_f32_16x16x32_bf16 v[76:79], a[44:47], a[44:47], v[76:79]
s_waitcnt vmcnt(8)
ds_read_b128 a[64:67], v91 offset:20480
ds_read_b128 a[68:71], v91 offset:22528
ds_read_b128 a[72:75], v92 offset:20480
ds_read_b128 a[76:79], v92 offset:22528
s_add_u32 m0, s61, 143360
s_add_u32 s66, s70, s71
buffer_load_dwordx4 v89, s[24:27], s66 offen lds
buffer_load_dwordx4 v90, s[24:27], s66 offen offset:1024 lds
s_add_u32 m0, s61, 145408
s_add_u32 s66, s70, s72
buffer_load_dwordx4 v89, s[24:27], s66 offen lds
buffer_load_dwordx4 v90, s[24:27], s66 offen offset:1024 lds
s_add_u32 s70, s70, 128
s_cmp_eq_u32 s70, 1280
s_cselect_b32 s70, 0, s70
s_waitcnt lgkmcnt(7)
v_mfma_f32_16x16x32_bf16 v[64:67], v[84:87], a[48:51], v[64:67]
s_waitcnt lgkmcnt(6)
v_mfma_f32_16x16x32_bf16 v[68:71], v[84:87], a[52:55], v[68:71]
s_waitcnt lgkmcnt(5)
v_mfma_f32_16x16x32_bf16 v[64:67], v[84:87], a[56:59], v[64:67]
s_waitcnt lgkmcnt(4)
v_mfma_f32_16x16x32_bf16 v[68:71], v[84:87], a[60:63], v[68:71]
v_mfma_f32_16x16x32_bf16 v[72:75], a[48:51], a[48:51], v[72:75]
v_mfma_f32_16x16x32_bf16 v[76:79], a[52:55], a[52:55], v[76:79]
v_mfma_f32_16x16x32_bf16 v[72:75], a[56:59], a[56:59], v[72:75]
v_mfma_f32_16x16x32_bf16 v[76:79], a[60:63], a[60:63], v[76:79]
s_waitcnt vmcnt(8)
ds_read_b128 a[80:83], v91 offset:40960
ds_read_b128 a[84:87], v91 offset:43008
ds_read_b128 a[88:91], v92 offset:40960
ds_read_b128 a[92:95], v92 offset:43008
s_add_u32 m0, s61, 0
s_add_u32 s66, s70, s71
buffer_load_dwordx4 v89, s[24:27], s66 offen lds
buffer_load_dwordx4 v90, s[24:27], s66 offen offset:1024 lds
s_add_u32 m0, s61, 2048
s_add_u32 s66, s70, s72
buffer_load_dwordx4 v89, s[24:27], s66 offen lds
buffer_load_dwordx4 v90, s[24:27], s66 offen offset:1024 lds
s_add_u32 s70, s70, 128
s_cmp_eq_u32 s70, 1280
s_cselect_b32 s70, 0, s70
s_waitcnt lgkmcnt(7)
)ASM"
R"ASM(
v_mfma_f32_16x16x32_bf16 v[64:67], v[84:87], a[64:67], v[64:67]
s_waitcnt lgkmcnt(6)
v_mfma_f32_16x16x32_bf16 v[68:71], v[84:87], a[68:71], v[68:71]
s_waitcnt lgkmcnt(5)
v_mfma_f32_16x16x32_bf16 v[64:67], v[84:87], a[72:75], v[64:67]
s_waitcnt lgkmcnt(4)
v_mfma_f32_16x16x32_bf16 v[68:71], v[84:87], a[76:79], v[68:71]
v_mfma_f32_16x16x32_bf16 v[72:75], a[64:67], a[64:67], v[72:75]
v_mfma_f32_16x16x32_bf16 v[76:79], a[68:71], a[68:71], v[76:79]
v_mfma_f32_16x16x32_bf16 v[72:75], a[72:75], a[72:75], v[72:75]
v_mfma_f32_16x16x32_bf16 v[76:79], a[76:79], a[76:79], v[76:79]
s_waitcnt vmcnt(8)
v_add_u32 v91, 61440, v91
v_add_u32 v92, 61440, v92
ds_read_b128 a[96:99], v91
ds_read_b128 a[100:103], v91 offset:2048
ds_read_b128 a[104:107], v92
ds_read_b128 a[108:111], v92 offset:2048
s_add_u32 m0, s61, 20480
s_add_u32 s66, s70, s71
buffer_load_dwordx4 v89, s[24:27], s66 offen lds
buffer_load_dwordx4 v90, s[24:27], s66 offen offset:1024 lds
s_add_u32 m0, s61, 22528
s_add_u32 s66, s70, s72
buffer_load_dwordx4 v89, s[24:27], s66 offen lds
buffer_load_dwordx4 v90, s[24:27], s66 offen offset:1024 lds
s_add_u32 s70, s70, 128
s_cmp_eq_u32 s70, 1280
s_cselect_b32 s70, 0, s70
s_waitcnt lgkmcnt(7)
v_mfma_f32_16x16x32_bf16 v[64:67], v[84:87], a[80:83], v[64:67]
s_waitcnt lgkmcnt(6)
v_mfma_f32_16x16x32_bf16 v[68:71], v[84:87], a[84:87], v[68:71]
s_waitcnt lgkmcnt(5)
v_mfma_f32_16x16x32_bf16 v[64:67], v[84:87], a[88:91], v[64:67]
s_waitcnt lgkmcnt(4)
v_mfma_f32_16x16x32_bf16 v[68:71], v[84:87], a[92:95], v[68:71]
v_mfma_f32_16x16x32_bf16 v[72:75], a[80:83], a[80:83], v[72:75]
v_mfma_f32_16x16x32_bf16 v[76:79], a[84:87], a[84:87], v[76:79]
v_mfma_f32_16x16x32_bf16 v[72:75], a[88:91], a[88:91], v[72:75]
v_mfma_f32_16x16x32_bf16 v[76:79], a[92:95], a[92:95], v[76:79]
s_waitcnt vmcnt(8)
ds_read_b128 a[112:115], v91 offset:20480
ds_read_b128 a[116:119], v91 offset:22528
ds_read_b128 a[120:123], v92 offset:20480
ds_read_b128 a[124:127], v92 offset:22528
s_mul_i32 s68, s73, 122880
s_add_u32 m0, s61, 40960
s_nop 0
buffer_load_dwordx4 v7, s[28:31], s68 offen lds
buffer_load_dwordx4 v8, s[28:31], s68 offen offset:1024 lds
s_waitcnt lgkmcnt(7)
v_mfma_f32_16x16x32_bf16 v[64:67], v[84:87], a[96:99], v[64:67]
s_waitcnt lgkmcnt(6)
v_mfma_f32_16x16x32_bf16 v[68:71], v[84:87], a[100:103], v[68:71]
s_waitcnt lgkmcnt(5)
v_mfma_f32_16x16x32_bf16 v[64:67], v[84:87], a[104:107], v[64:67]
s_waitcnt lgkmcnt(4)
v_mfma_f32_16x16x32_bf16 v[68:71], v[84:87], a[108:111], v[68:71]
v_mfma_f32_16x16x32_bf16 v[72:75], a[96:99], a[96:99], v[72:75]
v_mfma_f32_16x16x32_bf16 v[76:79], a[100:103], a[100:103], v[76:79]
v_mfma_f32_16x16x32_bf16 v[72:75], a[104:107], a[104:107], v[72:75]
v_mfma_f32_16x16x32_bf16 v[76:79], a[108:111], a[108:111], v[76:79]
s_waitcnt vmcnt(6)
v_add_u32 v91, 4294844416, v91
v_add_u32 v92, 4294844416, v92
ds_read_b128 a[128:131], v91
ds_read_b128 a[132:135], v91 offset:2048
ds_read_b128 a[136:139], v92
ds_read_b128 a[140:143], v92 offset:2048
buffer_load_dwordx4 v9, s[28:31], s68 offen offset:2048 lds
buffer_load_dwordx4 v10, s[28:31], s68 offen offset:3072 lds
s_waitcnt lgkmcnt(7)
v_mfma_f32_16x16x32_bf16 v[64:67], v[84:87], a[112:115], v[64:67]
s_waitcnt lgkmcnt(6)
v_mfma_f32_16x16x32_bf16 v[68:71], v[84:87], a[116:119], v[68:71]
s_waitcnt lgkmcnt(5)
v_mfma_f32_16x16x32_bf16 v[64:67], v[84:87], a[120:123], v[64:67]
s_waitcnt lgkmcnt(4)
v_mfma_f32_16x16x32_bf16 v[68:71], v[84:87], a[124:127], v[68:71]
v_mfma_f32_16x16x32_bf16 v[72:75], a[112:115], a[112:115], v[72:75]
v_mfma_f32_16x16x32_bf16 v[76:79], a[116:119], a[116:119], v[76:79]
v_mfma_f32_16x16x32_bf16 v[72:75], a[120:123], a[120:123], v[72:75]
v_mfma_f32_16x16x32_bf16 v[76:79], a[124:127], a[124:127], v[76:79]
v_readfirstlane_b32 s65, v83
s_add_u32 s62, s65, 2654435769
v_lshlrev_b32 v16, 16, v82
v_and_b32 v17, 4294901760, v82
v_mov_b32 v20, 0
v_mov_b32 v21, 0
v_mov_b32 v22, 0
v_mov_b32 v23, 0
v_lshrrev_b32 v95, 1, v0
v_sub_u32 v95, v94, v95
ds_write_b128 v94, v[20:23]
ds_write_b128 v94, v[20:23] offset:2048
ds_write_b64 v95, v[20:21] offset:3072
ds_write_b64 v95, v[16:17] offset:3584
s_waitcnt vmcnt(4)
ds_read_b128 a[144:147], v91 offset:20480
ds_read_b128 a[148:151], v91 offset:22528
ds_read_b128 a[152:155], v92 offset:20480
ds_read_b128 a[156:159], v92 offset:22528
s_add_u32 m0, s61, 45056
s_nop 0
buffer_load_dwordx4 v11, s[28:31], s68 offen lds
s_waitcnt lgkmcnt(11)
v_mfma_f32_16x16x32_bf16 v[64:67], v[84:87], a[128:131], v[64:67]
s_waitcnt lgkmcnt(10)
v_mfma_f32_16x16x32_bf16 v[68:71], v[84:87], a[132:135], v[68:71]
s_waitcnt lgkmcnt(9)
v_mfma_f32_16x16x32_bf16 v[64:67], v[84:87], a[136:139], v[64:67]
s_waitcnt lgkmcnt(8)
v_mfma_f32_16x16x32_bf16 v[68:71], v[84:87], a[140:143], v[68:71]
v_mfma_f32_16x16x32_bf16 v[72:75], a[128:131], a[128:131], v[72:75]
v_mfma_f32_16x16x32_bf16 v[76:79], a[132:135], a[132:135], v[76:79]
v_mfma_f32_16x16x32_bf16 v[72:75], a[136:139], a[136:139], v[72:75]
v_mfma_f32_16x16x32_bf16 v[76:79], a[140:143], a[140:143], v[76:79]
s_waitcnt lgkmcnt(3)
v_mfma_f32_16x16x32_bf16 v[64:67], v[84:87], a[144:147], v[64:67]
s_waitcnt lgkmcnt(2)
v_mfma_f32_16x16x32_bf16 v[68:71], v[84:87], a[148:151], v[68:71]
s_waitcnt lgkmcnt(1)
v_mfma_f32_16x16x32_bf16 v[64:67], v[84:87], a[152:155], v[64:67]
s_waitcnt lgkmcnt(0)
v_mfma_f32_16x16x32_bf16 v[68:71], v[84:87], a[156:159], v[68:71]
v_mfma_f32_16x16x32_bf16 v[72:75], a[144:147], a[144:147], v[72:75]
v_mfma_f32_16x16x32_bf16 v[76:79], a[148:151], a[148:151], v[76:79]
v_mfma_f32_16x16x32_bf16 v[72:75], a[152:155], a[152:155], v[72:75]
v_mfma_f32_16x16x32_bf16 v[76:79], a[156:159], a[156:159], v[76:79]
s_nop 1
v_mul_f32 v80, 986500301, v64
s_nop 0
v_mul_f32 v81, 986500301, v68
v_cvt_pk_bf16_f32 v88, -v80, -v81
v_lshlrev_b32 v56, 16, v88
v_lshlrev_b32 v57, 16, v88
v_lshlrev_b32 v58, 16, v88
v_lshlrev_b32 v59, 16, v88
v_and_b32 v60, 4294901760, v88
v_and_b32 v61, 4294901760, v88
v_and_b32 v62, 4294901760, v88
v_and_b32 v63, 4294901760, v88
v_mfma_f32_16x16x16_bf16 v[16:19], a[222:223], a[0:1], v[56:59]
v_mfma_f32_16x16x16_bf16 v[20:23], a[222:223], a[2:3], v[56:59]
v_mfma_f32_16x16x16_bf16 v[24:27], a[222:223], a[4:5], v[60:63]
v_mfma_f32_16x16x16_bf16 v[28:31], a[222:223], a[6:7], v[60:63]
v_mfma_f32_16x16x16_bf16 v[32:35], a[222:223], a[8:9], v[56:59]
s_nop 3
v_cvt_pk_bf16_f32 v96, v16, v17
v_cvt_pk_bf16_f32 v97, v18, v19
v_mfma_f32_16x16x16_bf16 v[36:39], a[222:223], a[10:11], v[56:59]
v_cvt_pk_bf16_f32 v98, v20, v21
v_cvt_pk_bf16_f32 v99, v22, v23
v_mfma_f32_16x16x16_bf16 v[40:43], a[222:223], a[12:13], v[60:63]
v_cvt_pk_bf16_f32 v100, v24, v25
v_cvt_pk_bf16_f32 v101, v26, v27
v_mfma_f32_16x16x16_bf16 v[44:47], a[222:223], a[14:15], v[60:63]
v_cvt_pk_bf16_f32 v102, v28, v29
v_cvt_pk_bf16_f32 v103, v30, v31
v_mfma_f32_16x16x16_bf16 v[48:51], a[222:223], a[16:17], v[56:59]
v_cvt_pk_bf16_f32 v104, v32, v33
v_cvt_pk_bf16_f32 v105, v34, v35
v_mfma_f32_16x16x16_bf16 v[52:55], a[222:223], a[18:19], v[56:59]
v_cvt_pk_bf16_f32 v106, v36, v37
v_cvt_pk_bf16_f32 v107, v38, v39
v_mfma_f32_16x16x16_bf16 v[16:19], a[222:223], a[20:21], v[60:63]
v_cvt_pk_bf16_f32 v108, v40, v41
v_cvt_pk_bf16_f32 v109, v42, v43
v_mfma_f32_16x16x16_bf16 v[20:23], a[222:223], a[22:23], v[60:63]
v_cvt_pk_bf16_f32 v110, v44, v45
v_cvt_pk_bf16_f32 v111, v46, v47
v_mfma_f32_16x16x16_bf16 v[24:27], a[222:223], a[24:25], v[56:59]
v_cvt_pk_bf16_f32 v112, v48, v49
v_cvt_pk_bf16_f32 v113, v50, v51
v_mfma_f32_16x16x16_bf16 v[28:31], a[222:223], a[26:27], v[56:59]
v_cvt_pk_bf16_f32 v114, v52, v53
v_cvt_pk_bf16_f32 v115, v54, v55
v_mfma_f32_16x16x16_bf16 v[32:35], a[222:223], a[28:29], v[60:63]
v_cvt_pk_bf16_f32 v116, v16, v17
v_cvt_pk_bf16_f32 v117, v18, v19
v_mfma_f32_16x16x16_bf16 v[36:39], a[222:223], a[30:31], v[60:63]
v_cvt_pk_bf16_f32 v118, v20, v21
v_cvt_pk_bf16_f32 v119, v22, v23
v_mfma_f32_16x16x16_bf16 v[40:43], a[222:223], a[32:33], v[56:59]
v_cvt_pk_bf16_f32 v120, v24, v25
v_cvt_pk_bf16_f32 v121, v26, v27
v_mfma_f32_16x16x16_bf16 v[44:47], a[222:223], a[34:35], v[56:59]
v_cvt_pk_bf16_f32 v122, v28, v29
v_cvt_pk_bf16_f32 v123, v30, v31
v_mfma_f32_16x16x16_bf16 v[48:51], a[222:223], a[36:37], v[60:63]
v_cvt_pk_bf16_f32 v124, v32, v33
v_cvt_pk_bf16_f32 v125, v34, v35
v_mfma_f32_16x16x16_bf16 v[52:55], a[222:223], a[38:39], v[60:63]
v_cvt_pk_bf16_f32 v126, v36, v37
v_cvt_pk_bf16_f32 v127, v38, v39
s_mul_i32 s68, s74, 122880
s_add_u32 m0, s61, 61440
s_nop 0
buffer_load_dwordx4 v7, s[28:31], s68 offen lds
v_mfma_f32_16x16x16_bf16 v[16:19], a[222:223], a[40:41], v[56:59]
v_cvt_pk_bf16_f32 v128, v40, v41
v_cvt_pk_bf16_f32 v129, v42, v43
v_mfma_f32_16x16x16_bf16 v[20:23], a[222:223], a[42:43], v[56:59]
v_cvt_pk_bf16_f32 v130, v44, v45
v_cvt_pk_bf16_f32 v131, v46, v47
v_mfma_f32_16x16x16_bf16 v[24:27], a[222:223], a[44:45], v[60:63]
v_cvt_pk_bf16_f32 v132, v48, v49
v_cvt_pk_bf16_f32 v133, v50, v51
v_mfma_f32_16x16x16_bf16 v[28:31], a[222:223], a[46:47], v[60:63]
v_cvt_pk_bf16_f32 v134, v52, v53
v_cvt_pk_bf16_f32 v135, v54, v55
v_mfma_f32_16x16x16_bf16 v[32:35], a[222:223], a[48:49], v[56:59]
v_cvt_pk_bf16_f32 v136, v16, v17
v_cvt_pk_bf16_f32 v137, v18, v19
v_mfma_f32_16x16x16_bf16 v[36:39], a[222:223], a[50:51], v[56:59]
v_cvt_pk_bf16_f32 v138, v20, v21
v_cvt_pk_bf16_f32 v139, v22, v23
buffer_load_dwordx4 v8, s[28:31], s68 offen offset:1024 lds
v_mfma_f32_16x16x16_bf16 v[40:43], a[222:223], a[52:53], v[60:63]
v_cvt_pk_bf16_f32 v140, v24, v25
v_cvt_pk_bf16_f32 v141, v26, v27
v_mfma_f32_16x16x16_bf16 v[44:47], a[222:223], a[54:55], v[60:63]
v_cvt_pk_bf16_f32 v142, v28, v29
v_cvt_pk_bf16_f32 v143, v30, v31
v_mfma_f32_16x16x16_bf16 v[48:51], a[222:223], a[56:57], v[56:59]
v_cvt_pk_bf16_f32 v144, v32, v33
v_cvt_pk_bf16_f32 v145, v34, v35
v_mfma_f32_16x16x16_bf16 v[52:55], a[222:223], a[58:59], v[56:59]
v_cvt_pk_bf16_f32 v146, v36, v37
v_cvt_pk_bf16_f32 v147, v38, v39
v_mfma_f32_16x16x16_bf16 v[16:19], a[222:223], a[60:61], v[60:63]
v_cvt_pk_bf16_f32 v148, v40, v41
v_cvt_pk_bf16_f32 v149, v42, v43
v_mfma_f32_16x16x16_bf16 v[20:23], a[222:223], a[62:63], v[60:63]
v_cvt_pk_bf16_f32 v150, v44, v45
v_cvt_pk_bf16_f32 v151, v46, v47
buffer_load_dwordx4 v9, s[28:31], s68 offen offset:2048 lds
v_mfma_f32_16x16x16_bf16 v[24:27], a[222:223], a[64:65], v[56:59]
v_cvt_pk_bf16_f32 v152, v48, v49
v_cvt_pk_bf16_f32 v153, v50, v51
v_mfma_f32_16x16x16_bf16 v[28:31], a[222:223], a[66:67], v[56:59]
v_cvt_pk_bf16_f32 v154, v52, v53
v_cvt_pk_bf16_f32 v155, v54, v55
v_mfma_f32_16x16x16_bf16 v[32:35], a[222:223], a[68:69], v[60:63]
v_cvt_pk_bf16_f32 v156, v16, v17
v_cvt_pk_bf16_f32 v157, v18, v19
v_mfma_f32_16x16x16_bf16 v[36:39], a[222:223], a[70:71], v[60:63]
v_cvt_pk_bf16_f32 v158, v20, v21
v_cvt_pk_bf16_f32 v159, v22, v23
v_mfma_f32_16x16x16_bf16 v[40:43], a[222:223], a[72:73], v[56:59]
v_cvt_pk_bf16_f32 v160, v24, v25
v_cvt_pk_bf16_f32 v161, v26, v27
v_mfma_f32_16x16x16_bf16 v[44:47], a[222:223], a[74:75], v[56:59]
v_cvt_pk_bf16_f32 v162, v28, v29
v_cvt_pk_bf16_f32 v163, v30, v31
buffer_load_dwordx4 v10, s[28:31], s68 offen offset:3072 lds
v_mfma_f32_16x16x16_bf16 v[48:51], a[222:223], a[76:77], v[60:63]
v_cvt_pk_bf16_f32 v164, v32, v33
v_cvt_pk_bf16_f32 v165, v34, v35
v_mfma_f32_16x16x16_bf16 v[52:55], a[222:223], a[78:79], v[60:63]
v_cvt_pk_bf16_f32 v166, v36, v37
v_cvt_pk_bf16_f32 v167, v38, v39
v_mfma_f32_16x16x16_bf16 v[16:19], a[222:223], a[80:81], v[56:59]
v_cvt_pk_bf16_f32 v168, v40, v41
v_cvt_pk_bf16_f32 v169, v42, v43
v_mfma_f32_16x16x16_bf16 v[20:23], a[222:223], a[82:83], v[56:59]
v_cvt_pk_bf16_f32 v170, v44, v45
v_cvt_pk_bf16_f32 v171, v46, v47
v_mfma_f32_16x16x16_bf16 v[24:27], a[222:223], a[84:85], v[60:63]
v_cvt_pk_bf16_f32 v172, v48, v49
v_cvt_pk_bf16_f32 v173, v50, v51
v_mfma_f32_16x16x16_bf16 v[28:31], a[222:223], a[86:87], v[60:63]
v_cvt_pk_bf16_f32 v174, v52, v53
v_cvt_pk_bf16_f32 v175, v54, v55
s_add_u32 m0, s61, 65536
s_nop 0
buffer_load_dwordx4 v11, s[28:31], s68 offen lds
v_mfma_f32_16x16x16_bf16 v[32:35], a[222:223], a[88:89], v[56:59]
v_cvt_pk_bf16_f32 v176, v16, v17
v_cvt_pk_bf16_f32 v177, v18, v19
v_mfma_f32_16x16x16_bf16 v[36:39], a[222:223], a[90:91], v[56:59]
v_cvt_pk_bf16_f32 v178, v20, v21
v_cvt_pk_bf16_f32 v179, v22, v23
v_mfma_f32_16x16x16_bf16 v[40:43], a[222:223], a[92:93], v[60:63]
v_cvt_pk_bf16_f32 v180, v24, v25
v_cvt_pk_bf16_f32 v181, v26, v27
v_mfma_f32_16x16x16_bf16 v[44:47], a[222:223], a[94:95], v[60:63]
v_cvt_pk_bf16_f32 v182, v28, v29
v_cvt_pk_bf16_f32 v183, v30, v31
v_mfma_f32_16x16x16_bf16 v[48:51], a[222:223], a[96:97], v[56:59]
v_cvt_pk_bf16_f32 v184, v32, v33
v_cvt_pk_bf16_f32 v185, v34, v35
v_mfma_f32_16x16x16_bf16 v[52:55], a[222:223], a[98:99], v[56:59]
v_cvt_pk_bf16_f32 v186, v36, v37
v_cvt_pk_bf16_f32 v187, v38, v39
s_mul_i32 s68, s75, 122880
s_add_u32 m0, s61, 81920
s_nop 0
buffer_load_dwordx4 v7, s[28:31], s68 offen lds
v_mfma_f32_16x16x16_bf16 v[16:19], a[222:223], a[100:101], v[60:63]
v_cvt_pk_bf16_f32 v188, v40, v41
v_cvt_pk_bf16_f32 v189, v42, v43
v_mfma_f32_16x16x16_bf16 v[20:23], a[222:223], a[102:103], v[60:63]
v_cvt_pk_bf16_f32 v190, v44, v45
v_cvt_pk_bf16_f32 v191, v46, v47
v_mfma_f32_16x16x16_bf16 v[24:27], a[222:223], a[104:105], v[56:59]
v_cvt_pk_bf16_f32 v192, v48, v49
v_cvt_pk_bf16_f32 v193, v50, v51
v_mfma_f32_16x16x16_bf16 v[28:31], a[222:223], a[106:107], v[56:59]
v_cvt_pk_bf16_f32 v194, v52, v53
v_cvt_pk_bf16_f32 v195, v54, v55
buffer_load_dwordx4 v8, s[28:31], s68 offen offset:1024 lds
v_mfma_f32_16x16x16_bf16 v[32:35], a[222:223], a[108:109], v[60:63]
v_cvt_pk_bf16_f32 v196, v16, v17
v_cvt_pk_bf16_f32 v197, v18, v19
v_mfma_f32_16x16x16_bf16 v[36:39], a[222:223], a[110:111], v[60:63]
v_cvt_pk_bf16_f32 v198, v20, v21
v_cvt_pk_bf16_f32 v199, v22, v23
v_mfma_f32_16x16x16_bf16 v[40:43], a[222:223], a[112:113], v[56:59]
v_cvt_pk_bf16_f32 v200, v24, v25
v_cvt_pk_bf16_f32 v201, v26, v27
v_mfma_f32_16x16x16_bf16 v[44:47], a[222:223], a[114:115], v[56:59]
v_cvt_pk_bf16_f32 v202, v28, v29
v_cvt_pk_bf16_f32 v203, v30, v31
v_mfma_f32_16x16x16_bf16 v[48:51], a[222:223], a[116:117], v[60:63]
v_cvt_pk_bf16_f32 v204, v32, v33
v_cvt_pk_bf16_f32 v205, v34, v35
v_mfma_f32_16x16x16_bf16 v[52:55], a[222:223], a[118:119], v[60:63]
v_cvt_pk_bf16_f32 v206, v36, v37
v_cvt_pk_bf16_f32 v207, v38, v39
buffer_load_dwordx4 v9, s[28:31], s68 offen offset:2048 lds
v_mfma_f32_16x16x16_bf16 v[16:19], a[222:223], a[120:121], v[56:59]
v_cvt_pk_bf16_f32 v208, v40, v41
v_cvt_pk_bf16_f32 v209, v42, v43
v_mfma_f32_16x16x16_bf16 v[20:23], a[222:223], a[122:123], v[56:59]
v_cvt_pk_bf16_f32 v210, v44, v45
v_cvt_pk_bf16_f32 v211, v46, v47
v_mfma_f32_16x16x16_bf16 v[24:27], a[222:223], a[124:125], v[60:63]
v_cvt_pk_bf16_f32 v212, v48, v49
v_cvt_pk_bf16_f32 v213, v50, v51
v_mfma_f32_16x16x16_bf16 v[28:31], a[222:223], a[126:127], v[60:63]
v_cvt_pk_bf16_f32 v214, v52, v53
v_cvt_pk_bf16_f32 v215, v54, v55
v_mfma_f32_16x16x16_bf16 v[32:35], a[222:223], a[128:129], v[56:59]
v_cvt_pk_bf16_f32 v216, v16, v17
v_cvt_pk_bf16_f32 v217, v18, v19
v_mfma_f32_16x16x16_bf16 v[36:39], a[222:223], a[130:131], v[56:59]
v_cvt_pk_bf16_f32 v218, v20, v21
v_cvt_pk_bf16_f32 v219, v22, v23
buffer_load_dwordx4 v10, s[28:31], s68 offen offset:3072 lds
v_mfma_f32_16x16x16_bf16 v[40:43], a[222:223], a[132:133], v[60:63]
v_cvt_pk_bf16_f32 v220, v24, v25
v_cvt_pk_bf16_f32 v221, v26, v27
v_mfma_f32_16x16x16_bf16 v[44:47], a[222:223], a[134:135], v[60:63]
v_cvt_pk_bf16_f32 v222, v28, v29
v_cvt_pk_bf16_f32 v223, v30, v31
v_mfma_f32_16x16x16_bf16 v[48:51], a[222:223], a[136:137], v[56:59]
v_cvt_pk_bf16_f32 v224, v32, v33
v_cvt_pk_bf16_f32 v225, v34, v35
v_mfma_f32_16x16x16_bf16 v[52:55], a[222:223], a[138:139], v[56:59]
v_cvt_pk_bf16_f32 v226, v36, v37
v_cvt_pk_bf16_f32 v227, v38, v39
v_mfma_f32_16x16x16_bf16 v[16:19], a[222:223], a[140:141], v[60:63]
v_cvt_pk_bf16_f32 v228, v40, v41
v_cvt_pk_bf16_f32 v229, v42, v43
v_mfma_f32_16x16x16_bf16 v[20:23], a[222:223], a[142:143], v[60:63]
v_cvt_pk_bf16_f32 v230, v44, v45
v_cvt_pk_bf16_f32 v231, v46, v47
v_mfma_f32_16x16x16_bf16 v[24:27], a[222:223], a[144:145], v[56:59]
s_waitcnt vmcnt(9)
s_barrier
ds_read_b64_tr_b16 a[224:225], v1 offset:40960
ds_read_b64_tr_b16 a[226:227], v1 offset:43520
ds_read_b64_tr_b16 a[228:229], v1 offset:40968
ds_read_b64_tr_b16 a[230:231], v1 offset:43528
ds_read_b64_tr_b16 a[232:233], v1 offset:40976
ds_read_b64_tr_b16 a[234:235], v1 offset:43536
ds_read_b64_tr_b16 a[236:237], v1 offset:40984
ds_read_b64_tr_b16 a[238:239], v1 offset:43544
ds_read_b64_tr_b16 a[240:241], v1 offset:40992
ds_read_b64_tr_b16 a[242:243], v1 offset:43552
ds_read_b64_tr_b16 a[244:245], v1 offset:46080
ds_read_b64_tr_b16 a[246:247], v1 offset:48640
v_cvt_pk_bf16_f32 v232, v48, v49
v_cvt_pk_bf16_f32 v233, v50, v51
v_mfma_f32_16x16x16_bf16 v[28:31], a[222:223], a[146:147], v[56:59]
v_cvt_pk_bf16_f32 v234, v52, v53
v_cvt_pk_bf16_f32 v235, v54, v55
s_add_u32 m0, s61, 86016
s_nop 0
buffer_load_dwordx4 v11, s[28:31], s68 offen lds
v_mfma_f32_16x16x16_bf16 v[32:35], a[222:223], a[148:149], v[60:63]
v_cvt_pk_bf16_f32 v236, v16, v17
v_cvt_pk_bf16_f32 v237, v18, v19
v_mfma_f32_16x16x16_bf16 v[36:39], a[222:223], a[150:151], v[60:63]
v_cvt_pk_bf16_f32 v238, v20, v21
v_cvt_pk_bf16_f32 v239, v22, v23
s_mul_i32 s68, s76, 122880
s_add_u32 m0, s61, 102400
)ASM"
R"ASM(
s_nop 0
buffer_load_dwordx4 v7, s[28:31], s68 offen lds
v_mfma_f32_16x16x16_bf16 v[40:43], a[222:223], a[152:153], v[56:59]
v_cvt_pk_bf16_f32 v240, v24, v25
v_cvt_pk_bf16_f32 v241, v26, v27
v_mfma_f32_16x16x16_bf16 v[44:47], a[222:223], a[154:155], v[56:59]
v_cvt_pk_bf16_f32 v242, v28, v29
v_cvt_pk_bf16_f32 v243, v30, v31
v_mfma_f32_16x16x16_bf16 v[48:51], a[222:223], a[156:157], v[60:63]
v_cvt_pk_bf16_f32 v244, v32, v33
v_cvt_pk_bf16_f32 v245, v34, v35
v_mfma_f32_16x16x16_bf16 v[52:55], a[222:223], a[158:159], v[60:63]
v_cvt_pk_bf16_f32 v246, v36, v37
v_cvt_pk_bf16_f32 v247, v38, v39
v_cvt_pk_bf16_f32 v248, v40, v41
v_cvt_pk_bf16_f32 v249, v42, v43
v_cvt_pk_bf16_f32 v250, v44, v45
v_cvt_pk_bf16_f32 v251, v46, v47
v_cvt_pk_bf16_f32 v252, v48, v49
v_cvt_pk_bf16_f32 v253, v50, v51
v_cvt_pk_bf16_f32 v254, v52, v53
v_cvt_pk_bf16_f32 v255, v54, v55
buffer_load_dwordx4 v8, s[28:31], s68 offen offset:1024 lds
s_bitcmp1_b32 s59, 0
s_cbranch_scc0 .Lroleq
s_waitcnt lgkmcnt(8)
v_mfma_f32_16x16x32_bf16 v[16:19], a[224:227], v[96:99], 0
ds_read_b64_tr_b16 a[248:249], v1 offset:46088
v_cndmask_b32 v64, 0, v72, s[4:5]
v_mfma_f32_16x16x32_bf16 v[36:39], a[224:227], v[100:103], 0
ds_read_b64_tr_b16 a[250:251], v1 offset:48648
v_cndmask_b32 v64, v64, v73, s[6:7]
v_mfma_f32_16x16x32_bf16 v[20:23], a[228:231], v[96:99], 0
ds_read_b64_tr_b16 a[252:253], v1 offset:46096
v_cndmask_b32 v64, v64, v74, s[8:9]
v_mfma_f32_16x16x32_bf16 v[40:43], a[228:231], v[100:103], 0
ds_read_b64_tr_b16 a[254:255], v1 offset:48656
s_waitcnt lgkmcnt(8)
v_mfma_f32_16x16x32_bf16 v[24:27], a[232:235], v[96:99], 0
ds_read_b64_tr_b16 a[224:225], v1 offset:46104
buffer_load_dwordx4 v9, s[28:31], s68 offen offset:2048 lds
v_mfma_f32_16x16x32_bf16 v[44:47], a[232:235], v[100:103], 0
ds_read_b64_tr_b16 a[226:227], v1 offset:48664
v_cndmask_b32 v64, v64, v75, s[10:11]
v_mfma_f32_16x16x32_bf16 v[28:31], a[236:239], v[96:99], 0
ds_read_b64_tr_b16 a[228:229], v1 offset:46112
v_cndmask_b32 v65, 0, v76, s[4:5]
v_mfma_f32_16x16x32_bf16 v[48:51], a[236:239], v[100:103], 0
ds_read_b64_tr_b16 a[230:231], v1 offset:48672
s_waitcnt lgkmcnt(4)
v_mfma_f32_16x16x32_bf16 v[32:35], a[240:243], v[96:99], 0
ds_read_b64_tr_b16 a[232:233], v1 offset:51200
v_cndmask_b32 v65, v65, v77, s[6:7]
v_mfma_f32_16x16x32_bf16 v[52:55], a[240:243], v[100:103], 0
ds_read_b64_tr_b16 a[234:235], v1 offset:53760
v_cndmask_b32 v65, v65, v78, s[8:9]
v_mfma_f32_16x16x32_bf16 v[16:19], a[244:247], v[104:107], v[16:19]
ds_read_b64_tr_b16 a[236:237], v1 offset:51208
v_cndmask_b32 v65, v65, v79, s[10:11]
v_mfma_f32_16x16x32_bf16 v[36:39], a[244:247], v[108:111], v[36:39]
ds_read_b64_tr_b16 a[238:239], v1 offset:53768
buffer_load_dwordx4 v10, s[28:31], s68 offen offset:3072 lds
v_mfma_f32_16x16x32_bf16 v[20:23], a[248:251], v[104:107], v[20:23]
ds_read_b64_tr_b16 a[240:241], v1 offset:51216
v_mov_b32 v66, v64
v_mfma_f32_16x16x32_bf16 v[40:43], a[248:251], v[108:111], v[40:43]
ds_read_b64_tr_b16 a[242:243], v1 offset:53776
v_mov_b32 v67, v65
v_mfma_f32_16x16x32_bf16 v[24:27], a[252:255], v[104:107], v[24:27]
ds_read_b64_tr_b16 a[244:245], v1 offset:51224
v_permlane32_swap_b32 v64, v66
v_mfma_f32_16x16x32_bf16 v[44:47], a[252:255], v[108:111], v[44:47]
ds_read_b64_tr_b16 a[246:247], v1 offset:53784
s_waitcnt lgkmcnt(4)
v_mfma_f32_16x16x32_bf16 v[28:31], a[224:227], v[104:107], v[28:31]
ds_read_b64_tr_b16 a[248:249], v1 offset:51232
v_permlane32_swap_b32 v65, v67
v_mfma_f32_16x16x32_bf16 v[48:51], a[224:227], v[108:111], v[48:51]
ds_read_b64_tr_b16 a[250:251], v1 offset:53792
s_add_u32 m0, s61, 106496
v_mfma_f32_16x16x32_bf16 v[32:35], a[228:231], v[104:107], v[32:35]
ds_read_b64_tr_b16 a[252:253], v1 offset:56320
buffer_load_dwordx4 v11, s[28:31], s68 offen lds
v_mfma_f32_16x16x32_bf16 v[52:55], a[228:231], v[108:111], v[52:55]
ds_read_b64_tr_b16 a[254:255], v1 offset:58880
v_add_f32 v64, v64, v66
v_mfma_f32_16x16x32_bf16 v[16:19], a[232:235], v[112:115], v[16:19]
ds_read_b64_tr_b16 a[224:225], v1 offset:56328
v_add_f32 v65, v65, v67
v_mfma_f32_16x16x32_bf16 v[36:39], a[232:235], v[116:119], v[36:39]
ds_read_b64_tr_b16 a[226:227], v1 offset:58888
v_mov_b32 v66, v64
v_mfma_f32_16x16x32_bf16 v[20:23], a[236:239], v[112:115], v[20:23]
ds_read_b64_tr_b16 a[228:229], v1 offset:56336
v_mov_b32 v67, v65
v_mfma_f32_16x16x32_bf16 v[40:43], a[236:239], v[116:119], v[40:43]
ds_read_b64_tr_b16 a[230:231], v1 offset:58896
s_waitcnt lgkmcnt(4)
v_mfma_f32_16x16x32_bf16 v[24:27], a[240:243], v[112:115], v[24:27]
ds_read_b64_tr_b16 a[232:233], v1 offset:56344
s_mul_i32 s68, s77, 122880
v_mfma_f32_16x16x32_bf16 v[44:47], a[240:243], v[116:119], v[44:47]
ds_read_b64_tr_b16 a[234:235], v1 offset:58904
s_waitcnt vmcnt(10)
v_mfma_f32_16x16x32_bf16 v[28:31], a[244:247], v[112:115], v[28:31]
ds_read_b64_tr_b16 a[236:237], v1 offset:56352
s_barrier
v_mfma_f32_16x16x32_bf16 v[48:51], a[244:247], v[116:119], v[48:51]
ds_read_b64_tr_b16 a[238:239], v1 offset:58912
s_add_u32 m0, s61, 122880
v_mfma_f32_16x16x32_bf16 v[32:35], a[248:251], v[112:115], v[32:35]
ds_read_b64_tr_b16 a[240:241], v2
buffer_load_dwordx4 v7, s[28:31], s68 offen lds
v_mfma_f32_16x16x32_bf16 v[52:55], a[248:251], v[116:119], v[52:55]
ds_read_b64_tr_b16 a[242:243], v2 offset:2560
buffer_load_dwordx4 v8, s[28:31], s68 offen offset:1024 lds
v_mfma_f32_16x16x32_bf16 v[16:19], a[252:255], v[120:123], v[16:19]
ds_read_b64_tr_b16 a[244:245], v2 offset:8
v_permlane16_swap_b32 v64, v66
v_mfma_f32_16x16x32_bf16 v[36:39], a[252:255], v[124:127], v[36:39]
ds_read_b64_tr_b16 a[246:247], v2 offset:2568
s_waitcnt lgkmcnt(4)
v_mfma_f32_16x16x32_bf16 v[20:23], a[224:227], v[120:123], v[20:23]
ds_read_b64_tr_b16 a[248:249], v2 offset:16
v_permlane16_swap_b32 v65, v67
v_mfma_f32_16x16x32_bf16 v[40:43], a[224:227], v[124:127], v[40:43]
ds_read_b64_tr_b16 a[250:251], v2 offset:2576
v_add_f32 v64, v64, v66
v_mfma_f32_16x16x32_bf16 v[24:27], a[228:231], v[120:123], v[24:27]
ds_read_b64_tr_b16 a[252:253], v2 offset:24
v_add_f32 v65, v65, v67
v_mfma_f32_16x16x32_bf16 v[44:47], a[228:231], v[124:127], v[44:47]
ds_read_b64_tr_b16 a[254:255], v2 offset:2584
v_mul_f32 v64, 986500301, v64
v_mfma_f32_16x16x32_bf16 v[28:31], a[232:235], v[120:123], v[28:31]
ds_read_b64_tr_b16 a[224:225], v2 offset:32
buffer_load_dwordx4 v9, s[28:31], s68 offen offset:2048 lds
v_mfma_f32_16x16x32_bf16 v[48:51], a[232:235], v[124:127], v[48:51]
ds_read_b64_tr_b16 a[226:227], v2 offset:2592
v_fma_f32 v64, -v80, v80, v64
v_mfma_f32_16x16x32_bf16 v[32:35], a[236:239], v[120:123], v[32:35]
ds_read_b64_tr_b16 a[228:229], v2 offset:5120
v_max_f32 v64, 0, v64
v_mfma_f32_16x16x32_bf16 v[52:55], a[236:239], v[124:127], v[52:55]
ds_read_b64_tr_b16 a[230:231], v2 offset:7680
s_waitcnt lgkmcnt(4)
v_mfma_f32_16x16x32_bf16 v[16:19], a[240:243], v[128:131], v[16:19]
ds_read_b64_tr_b16 a[232:233], v2 offset:5128
v_mul_f32 v65, 986500301, v65
v_mfma_f32_16x16x32_bf16 v[36:39], a[240:243], v[132:135], v[36:39]
ds_read_b64_tr_b16 a[234:235], v2 offset:7688
v_fma_f32 v65, -v81, v81, v65
v_mfma_f32_16x16x32_bf16 v[20:23], a[244:247], v[128:131], v[20:23]
ds_read_b64_tr_b16 a[236:237], v2 offset:5136
buffer_load_dwordx4 v10, s[28:31], s68 offen offset:3072 lds
v_mfma_f32_16x16x32_bf16 v[40:43], a[244:247], v[132:135], v[40:43]
ds_read_b64_tr_b16 a[238:239], v2 offset:7696
v_max_f32 v65, 0, v65
v_mfma_f32_16x16x32_bf16 v[24:27], a[248:251], v[128:131], v[24:27]
ds_read_b64_tr_b16 a[240:241], v2 offset:5144
v_cvt_pk_bf16_f32 v68, v64, v65
v_mfma_f32_16x16x32_bf16 v[44:47], a[248:251], v[132:135], v[44:47]
ds_read_b64_tr_b16 a[242:243], v2 offset:7704
v_lshlrev_b32 v64, 16, v68
v_mfma_f32_16x16x32_bf16 v[28:31], a[252:255], v[128:131], v[28:31]
ds_read_b64_tr_b16 a[244:245], v2 offset:5152
v_and_b32 v65, 4294901760, v68
v_mfma_f32_16x16x32_bf16 v[48:51], a[252:255], v[132:135], v[48:51]
ds_read_b64_tr_b16 a[246:247], v2 offset:7712
s_waitcnt lgkmcnt(4)
v_mfma_f32_16x16x32_bf16 v[32:35], a[224:227], v[128:131], v[32:35]
ds_read_b64_tr_b16 a[248:249], v2 offset:10240
v_add_f32 v64, 925353388, v64
v_mfma_f32_16x16x32_bf16 v[52:55], a[224:227], v[132:135], v[52:55]
ds_read_b64_tr_b16 a[250:251], v2 offset:12800
s_add_u32 m0, s61, 126976
v_mfma_f32_16x16x32_bf16 v[16:19], a[228:231], v[136:139], v[16:19]
ds_read_b64_tr_b16 a[252:253], v2 offset:10248
buffer_load_dwordx4 v11, s[28:31], s68 offen lds
v_mfma_f32_16x16x32_bf16 v[36:39], a[228:231], v[140:143], v[36:39]
ds_read_b64_tr_b16 a[254:255], v2 offset:12808
v_add_f32 v65, 925353388, v65
v_mfma_f32_16x16x32_bf16 v[20:23], a[232:235], v[136:139], v[20:23]
ds_read_b64_tr_b16 a[224:225], v2 offset:10256
v_cvt_pk_bf16_f32 v68, v64, v65
v_mfma_f32_16x16x32_bf16 v[40:43], a[232:235], v[140:143], v[40:43]
ds_read_b64_tr_b16 a[226:227], v2 offset:12816
v_lshlrev_b32 v64, 16, v68
v_mfma_f32_16x16x32_bf16 v[24:27], a[236:239], v[136:139], v[24:27]
ds_read_b64_tr_b16 a[228:229], v2 offset:10264
v_and_b32 v65, 4294901760, v68
v_mfma_f32_16x16x32_bf16 v[44:47], a[236:239], v[140:143], v[44:47]
ds_read_b64_tr_b16 a[230:231], v2 offset:12824
s_waitcnt lgkmcnt(4)
v_mfma_f32_16x16x32_bf16 v[28:31], a[240:243], v[136:139], v[28:31]
ds_read_b64_tr_b16 a[232:233], v2 offset:10272
s_mul_i32 s68, s73, 122880
v_mfma_f32_16x16x32_bf16 v[48:51], a[240:243], v[140:143], v[48:51]
ds_read_b64_tr_b16 a[234:235], v2 offset:12832
s_add_u32 s68, s68, 1280
v_mfma_f32_16x16x32_bf16 v[32:35], a[244:247], v[136:139], v[32:35]
ds_read_b64_tr_b16 a[236:237], v2 offset:15360
s_add_u32 m0, s61, 0
v_mfma_f32_16x16x32_bf16 v[52:55], a[244:247], v[140:143], v[52:55]
ds_read_b64_tr_b16 a[238:239], v2 offset:17920
buffer_load_dwordx4 v7, s[28:31], s68 offen lds
v_mfma_f32_16x16x32_bf16 v[16:19], a[248:251], v[144:147], v[16:19]
ds_read_b64_tr_b16 a[240:241], v2 offset:15368
v_sqrt_f32 v64, v64
v_mfma_f32_16x16x32_bf16 v[36:39], a[248:251], v[148:151], v[36:39]
ds_read_b64_tr_b16 a[242:243], v2 offset:17928
v_accvgpr_read_b32 v12, a181
v_mfma_f32_16x16x32_bf16 v[20:23], a[252:255], v[144:147], v[20:23]
ds_read_b64_tr_b16 a[244:245], v2 offset:15376
buffer_load_dwordx4 a[100:103], v12, s[36:39], 0 offen nt
v_mfma_f32_16x16x32_bf16 v[40:43], a[252:255], v[148:151], v[40:43]
ds_read_b64_tr_b16 a[246:247], v2 offset:17936
s_waitcnt lgkmcnt(4)
v_mfma_f32_16x16x32_bf16 v[24:27], a[224:227], v[144:147], v[24:27]
ds_read_b64_tr_b16 a[248:249], v2 offset:15384
v_accvgpr_read_b32 v12, a183
v_mfma_f32_16x16x32_bf16 v[44:47], a[224:227], v[148:151], v[44:47]
ds_read_b64_tr_b16 a[250:251], v2 offset:17944
s_waitcnt vmcnt(12)
v_mfma_f32_16x16x32_bf16 v[28:31], a[228:231], v[144:147], v[28:31]
ds_read_b64_tr_b16 a[252:253], v2 offset:15392
s_barrier
v_mfma_f32_16x16x32_bf16 v[48:51], a[228:231], v[148:151], v[48:51]
ds_read_b64_tr_b16 a[254:255], v2 offset:17952
buffer_load_dwordx4 a[104:107], v12, s[36:39], 0 offen offset:1024 nt
v_mfma_f32_16x16x32_bf16 v[32:35], a[232:235], v[144:147], v[32:35]
ds_read_b64_tr_b16 a[224:225], v2 offset:20480
v_accvgpr_read_b32 v12, a185
v_mfma_f32_16x16x32_bf16 v[52:55], a[232:235], v[148:151], v[52:55]
ds_read_b64_tr_b16 a[226:227], v2 offset:23040
buffer_load_dwordx4 a[108:111], v12, s[36:39], 0 offen offset:2048 nt
v_mfma_f32_16x16x32_bf16 v[16:19], a[236:239], v[152:155], v[16:19]
ds_read_b64_tr_b16 a[228:229], v2 offset:20488
v_accvgpr_read_b32 v12, a186
v_mfma_f32_16x16x32_bf16 v[36:39], a[236:239], v[156:159], v[36:39]
ds_read_b64_tr_b16 a[230:231], v2 offset:23048
s_waitcnt lgkmcnt(4)
v_mfma_f32_16x16x32_bf16 v[20:23], a[240:243], v[152:155], v[20:23]
ds_read_b64_tr_b16 a[232:233], v2 offset:20496
buffer_load_dwordx4 a[112:115], v12, s[36:39], 0 offen offset:3072 nt
v_mfma_f32_16x16x32_bf16 v[40:43], a[240:243], v[156:159], v[40:43]
ds_read_b64_tr_b16 a[234:235], v2 offset:23056
v_accvgpr_read_b32 v12, a187
v_mfma_f32_16x16x32_bf16 v[24:27], a[244:247], v[152:155], v[24:27]
ds_read_b64_tr_b16 a[236:237], v2 offset:20504
buffer_load_dwordx4 a[116:119], v12, s[36:39], 0 offen nt
v_mfma_f32_16x16x32_bf16 v[44:47], a[244:247], v[156:159], v[44:47]
ds_read_b64_tr_b16 a[238:239], v2 offset:23064
v_accvgpr_read_b32 v12, a207
v_mfma_f32_16x16x32_bf16 v[28:31], a[248:251], v[152:155], v[28:31]
ds_read_b64_tr_b16 a[240:241], v2 offset:20512
buffer_load_dwordx4 a[170:173], v12, s[24:27], s55 offen
v_mfma_f32_16x16x32_bf16 v[48:51], a[248:251], v[156:159], v[48:51]
ds_read_b64_tr_b16 a[242:243], v2 offset:23072
v_sqrt_f32 v65, v65
v_mfma_f32_16x16x32_bf16 v[32:35], a[252:255], v[152:155], v[32:35]
ds_read_b64_tr_b16 a[244:245], v2 offset:25600
v_cvt_pk_bf16_f32 v68, v64, v65
v_mfma_f32_16x16x32_bf16 v[52:55], a[252:255], v[156:159], v[52:55]
ds_read_b64_tr_b16 a[246:247], v2 offset:28160
s_waitcnt lgkmcnt(4)
v_mfma_f32_16x16x32_bf16 v[16:19], a[224:227], v[160:163], v[16:19]
ds_read_b64_tr_b16 a[248:249], v2 offset:25608
buffer_load_dwordx4 a[174:177], v12, s[24:27], s55 offen offset:16
v_mfma_f32_16x16x32_bf16 v[36:39], a[224:227], v[164:167], v[36:39]
ds_read_b64_tr_b16 a[250:251], v2 offset:28168
v_lshlrev_b32 v64, 16, v68
v_mfma_f32_16x16x32_bf16 v[20:23], a[228:231], v[160:163], v[20:23]
ds_read_b64_tr_b16 a[252:253], v2 offset:25616
v_and_b32 v65, 4294901760, v68
v_mfma_f32_16x16x32_bf16 v[40:43], a[228:231], v[164:167], v[40:43]
ds_read_b64_tr_b16 a[254:255], v2 offset:28176
v_rcp_f32 v14, v64
v_mfma_f32_16x16x32_bf16 v[24:27], a[232:235], v[160:163], v[24:27]
ds_read_b64_tr_b16 a[224:225], v2 offset:25624
v_rcp_f32 v15, v65
v_mfma_f32_16x16x32_bf16 v[44:47], a[232:235], v[164:167], v[44:47]
ds_read_b64_tr_b16 a[226:227], v2 offset:28184
v_accvgpr_read_b32 v13, a190
v_mfma_f32_16x16x32_bf16 v[28:31], a[236:239], v[160:163], v[28:31]
ds_read_b64_tr_b16 a[228:229], v2 offset:25632
ds_read_b64_tr_b16 a[0:1], v13
v_mfma_f32_16x16x32_bf16 v[48:51], a[236:239], v[164:167], v[48:51]
ds_read_b64_tr_b16 a[230:231], v2 offset:28192
s_waitcnt lgkmcnt(5)
v_mfma_f32_16x16x32_bf16 v[32:35], a[240:243], v[160:163], v[32:35]
ds_read_b64_tr_b16 a[232:233], v2 offset:30720
buffer_load_dwordx2 a[178:179], v12, s[24:27], s55 offen offset:32
v_mfma_f32_16x16x32_bf16 v[52:55], a[240:243], v[164:167], v[52:55]
ds_read_b64_tr_b16 a[234:235], v2 offset:33280
ds_read_b64_tr_b16 a[2:3], v13 offset:160
v_mfma_f32_16x16x32_bf16 v[16:19], a[244:247], v[168:171], v[16:19]
ds_read_b64_tr_b16 a[236:237], v2 offset:30728
ds_read_b64_tr_b16 a[4:5], v13 offset:320
v_mfma_f32_16x16x32_bf16 v[36:39], a[244:247], v[172:175], v[36:39]
ds_read_b64_tr_b16 a[238:239], v2 offset:33288
ds_read_b64_tr_b16 a[6:7], v13 offset:480
v_mfma_f32_16x16x32_bf16 v[20:23], a[248:251], v[168:171], v[20:23]
ds_read_b64_tr_b16 a[240:241], v2 offset:30736
ds_read_b64_tr_b16 a[8:9], v13 offset:640
v_mfma_f32_16x16x32_bf16 v[40:43], a[248:251], v[172:175], v[40:43]
ds_read_b64_tr_b16 a[242:243], v2 offset:33296
s_waitcnt lgkmcnt(14)
ds_read_b64_tr_b16 a[10:11], v13 offset:8
v_mfma_f32_16x16x32_bf16 v[24:27], a[252:255], v[168:171], v[24:27]
s_waitcnt lgkmcnt(14)
ds_read_b64_tr_b16 a[244:245], v2 offset:30744
s_waitcnt lgkmcnt(14)
ds_read_b64_tr_b16 a[12:13], v13 offset:168
v_mfma_f32_16x16x32_bf16 v[44:47], a[252:255], v[172:175], v[44:47]
s_waitcnt lgkmcnt(14)
ds_read_b64_tr_b16 a[246:247], v2 offset:33304
s_waitcnt lgkmcnt(8)
v_mfma_f32_16x16x32_bf16 v[28:31], a[224:227], v[168:171], v[28:31]
ds_read_b64_tr_b16 a[248:249], v2 offset:30752
ds_read_b64_tr_b16 a[14:15], v13 offset:328
v_mfma_f32_16x16x32_bf16 v[48:51], a[224:227], v[172:175], v[48:51]
ds_read_b64_tr_b16 a[250:251], v2 offset:33312
ds_read_b64_tr_b16 a[16:17], v13 offset:488
v_mfma_f32_16x16x32_bf16 v[32:35], a[228:231], v[168:171], v[32:35]
ds_read_b64_tr_b16 a[252:253], v2 offset:35840
ds_read_b64_tr_b16 a[18:19], v13 offset:648
v_mfma_f32_16x16x32_bf16 v[52:55], a[228:231], v[172:175], v[52:55]
ds_read_b64_tr_b16 a[254:255], v2 offset:38400
s_waitcnt lgkmcnt(14)
ds_read_b64_tr_b16 a[20:21], v13 offset:16
v_mfma_f32_16x16x32_bf16 v[16:19], a[232:235], v[176:179], v[16:19]
s_waitcnt lgkmcnt(14)
ds_read_b64_tr_b16 a[224:225], v2 offset:35848
s_waitcnt lgkmcnt(14)
ds_read_b64_tr_b16 a[22:23], v13 offset:176
v_mfma_f32_16x16x32_bf16 v[36:39], a[232:235], v[180:183], v[36:39]
s_waitcnt lgkmcnt(14)
ds_read_b64_tr_b16 a[226:227], v2 offset:38408
s_waitcnt lgkmcnt(14)
ds_read_b64_tr_b16 a[24:25], v13 offset:336
v_mfma_f32_16x16x32_bf16 v[20:23], a[236:239], v[176:179], v[20:23]
s_waitcnt lgkmcnt(14)
ds_read_b64_tr_b16 a[228:229], v2 offset:35856
s_waitcnt lgkmcnt(14)
ds_read_b64_tr_b16 a[26:27], v13 offset:496
v_mfma_f32_16x16x32_bf16 v[40:43], a[236:239], v[180:183], v[40:43]
s_waitcnt lgkmcnt(14)
ds_read_b64_tr_b16 a[230:231], v2 offset:38416
s_waitcnt lgkmcnt(8)
v_mfma_f32_16x16x32_bf16 v[24:27], a[240:243], v[176:179], v[24:27]
ds_read_b64_tr_b16 a[232:233], v2 offset:35864
ds_read_b64_tr_b16 a[28:29], v13 offset:656
v_mfma_f32_16x16x32_bf16 v[44:47], a[240:243], v[180:183], v[44:47]
ds_read_b64_tr_b16 a[234:235], v2 offset:38424
s_waitcnt vmcnt(14)
v_mfma_f32_16x16x32_bf16 v[28:31], a[244:247], v[176:179], v[28:31]
ds_read_b64_tr_b16 a[236:237], v2 offset:35872
s_barrier
v_mfma_f32_16x16x32_bf16 v[48:51], a[244:247], v[180:183], v[48:51]
ds_read_b64_tr_b16 a[238:239], v2 offset:38432
ds_read_b64_tr_b16 a[30:31], v13 offset:24
v_mfma_f32_16x16x32_bf16 v[32:35], a[248:251], v[176:179], v[32:35]
ds_read_b64_tr_b16 a[240:241], v2 offset:40960
s_waitcnt lgkmcnt(14)
ds_read_b64_tr_b16 a[32:33], v13 offset:184
v_mfma_f32_16x16x32_bf16 v[52:55], a[248:251], v[180:183], v[52:55]
s_waitcnt lgkmcnt(14)
ds_read_b64_tr_b16 a[242:243], v2 offset:43520
s_waitcnt lgkmcnt(14)
ds_read_b64_tr_b16 a[34:35], v13 offset:344
v_mfma_f32_16x16x32_bf16 v[16:19], a[252:255], v[184:187], v[16:19]
s_waitcnt lgkmcnt(14)
ds_read_b64_tr_b16 a[244:245], v2 offset:40968
s_waitcnt lgkmcnt(14)
ds_read_b64_tr_b16 a[36:37], v13 offset:504
v_mfma_f32_16x16x32_bf16 v[36:39], a[252:255], v[188:191], v[36:39]
s_waitcnt lgkmcnt(14)
ds_read_b64_tr_b16 a[246:247], v2 offset:43528
s_waitcnt lgkmcnt(8)
v_mfma_f32_16x16x32_bf16 v[20:23], a[224:227], v[184:187], v[20:23]
ds_read_b64_tr_b16 a[248:249], v2 offset:40976
ds_read_b64_tr_b16 a[38:39], v13 offset:664
v_mfma_f32_16x16x32_bf16 v[40:43], a[224:227], v[188:191], v[40:43]
ds_read_b64_tr_b16 a[250:251], v2 offset:43536
ds_read_b64_tr_b16 a[40:41], v13 offset:32
v_mfma_f32_16x16x32_bf16 v[24:27], a[228:231], v[184:187], v[24:27]
ds_read_b64_tr_b16 a[252:253], v2 offset:40984
ds_read_b64_tr_b16 a[42:43], v13 offset:192
v_mfma_f32_16x16x32_bf16 v[44:47], a[228:231], v[188:191], v[44:47]
ds_read_b64_tr_b16 a[254:255], v2 offset:43544
s_waitcnt lgkmcnt(14)
ds_read_b64_tr_b16 a[44:45], v13 offset:352
v_mfma_f32_16x16x32_bf16 v[28:31], a[232:235], v[184:187], v[28:31]
s_waitcnt lgkmcnt(14)
ds_read_b64_tr_b16 a[224:225], v2 offset:40992
s_waitcnt lgkmcnt(14)
ds_read_b64_tr_b16 a[46:47], v13 offset:512
v_mfma_f32_16x16x32_bf16 v[48:51], a[232:235], v[188:191], v[48:51]
s_waitcnt lgkmcnt(14)
)ASM"
R"ASM(
ds_read_b64_tr_b16 a[226:227], v2 offset:43552
s_waitcnt lgkmcnt(14)
ds_read_b64_tr_b16 a[48:49], v13 offset:672
v_mfma_f32_16x16x32_bf16 v[32:35], a[236:239], v[184:187], v[32:35]
s_waitcnt lgkmcnt(14)
ds_read_b64_tr_b16 a[228:229], v2 offset:46080
s_and_b32 s70, s56, 1
v_mfma_f32_16x16x32_bf16 v[52:55], a[236:239], v[188:191], v[52:55]
s_waitcnt lgkmcnt(14)
ds_read_b64_tr_b16 a[230:231], v2 offset:48640
s_waitcnt lgkmcnt(7)
v_mfma_f32_16x16x32_bf16 v[16:19], a[240:243], v[192:195], v[16:19]
ds_read_b64_tr_b16 a[232:233], v2 offset:46088
buffer_load_dwordx4 v8, s[28:31], s68 offen offset:1024 lds
v_mfma_f32_16x16x32_bf16 v[36:39], a[240:243], v[196:199], v[36:39]
ds_read_b64_tr_b16 a[234:235], v2 offset:48648
s_mul_i32 s70, s70, 10240
v_mfma_f32_16x16x32_bf16 v[20:23], a[244:247], v[192:195], v[20:23]
ds_read_b64_tr_b16 a[236:237], v2 offset:46096
s_xor_b32 s72, s56, 1
v_mfma_f32_16x16x32_bf16 v[40:43], a[244:247], v[196:199], v[40:43]
ds_read_b64_tr_b16 a[238:239], v2 offset:48656
s_mul_i32 s72, s72, 5120
v_mfma_f32_16x16x32_bf16 v[24:27], a[248:251], v[192:195], v[24:27]
ds_read_b64_tr_b16 a[240:241], v2 offset:46104
v_mfma_f32_16x16x32_bf16 v[44:47], a[248:251], v[196:199], v[44:47]
ds_read_b64_tr_b16 a[242:243], v2 offset:48664
v_mfma_f32_16x16x32_bf16 v[28:31], a[252:255], v[192:195], v[28:31]
ds_read_b64_tr_b16 a[244:245], v2 offset:46112
buffer_load_dwordx4 v9, s[28:31], s68 offen offset:2048 lds
v_mfma_f32_16x16x32_bf16 v[48:51], a[252:255], v[196:199], v[48:51]
ds_read_b64_tr_b16 a[246:247], v2 offset:48672
s_waitcnt lgkmcnt(4)
v_mfma_f32_16x16x32_bf16 v[32:35], a[224:227], v[192:195], v[32:35]
ds_read_b64_tr_b16 a[248:249], v2 offset:51200
v_mfma_f32_16x16x32_bf16 v[52:55], a[224:227], v[196:199], v[52:55]
ds_read_b64_tr_b16 a[250:251], v2 offset:53760
v_mfma_f32_16x16x32_bf16 v[16:19], a[228:231], v[200:203], v[16:19]
ds_read_b64_tr_b16 a[252:253], v2 offset:51208
v_mfma_f32_16x16x32_bf16 v[36:39], a[228:231], v[204:207], v[36:39]
ds_read_b64_tr_b16 a[254:255], v2 offset:53768
v_mfma_f32_16x16x32_bf16 v[20:23], a[232:235], v[200:203], v[20:23]
ds_read_b64_tr_b16 a[224:225], v2 offset:51216
buffer_load_dwordx4 v10, s[28:31], s68 offen offset:3072 lds
v_mfma_f32_16x16x32_bf16 v[40:43], a[232:235], v[204:207], v[40:43]
ds_read_b64_tr_b16 a[226:227], v2 offset:53776
v_mfma_f32_16x16x32_bf16 v[24:27], a[236:239], v[200:203], v[24:27]
ds_read_b64_tr_b16 a[228:229], v2 offset:51224
v_mfma_f32_16x16x32_bf16 v[44:47], a[236:239], v[204:207], v[44:47]
ds_read_b64_tr_b16 a[230:231], v2 offset:53784
s_waitcnt lgkmcnt(4)
v_mfma_f32_16x16x32_bf16 v[28:31], a[240:243], v[200:203], v[28:31]
ds_read_b64_tr_b16 a[232:233], v2 offset:51232
v_mfma_f32_16x16x32_bf16 v[48:51], a[240:243], v[204:207], v[48:51]
ds_read_b64_tr_b16 a[234:235], v2 offset:53792
v_mfma_f32_16x16x32_bf16 v[32:35], a[244:247], v[200:203], v[32:35]
ds_read_b64_tr_b16 a[236:237], v2 offset:56320
v_mfma_f32_16x16x32_bf16 v[52:55], a[244:247], v[204:207], v[52:55]
ds_read_b64_tr_b16 a[238:239], v2 offset:58880
s_add_u32 m0, s61, 4096
v_mfma_f32_16x16x32_bf16 v[16:19], a[248:251], v[208:211], v[16:19]
ds_read_b64_tr_b16 a[240:241], v2 offset:56328
buffer_load_dwordx4 v11, s[28:31], s68 offen lds
v_mfma_f32_16x16x32_bf16 v[36:39], a[248:251], v[212:215], v[36:39]
ds_read_b64_tr_b16 a[242:243], v2 offset:58888
v_mfma_f32_16x16x32_bf16 v[20:23], a[252:255], v[208:211], v[20:23]
ds_read_b64_tr_b16 a[244:245], v2 offset:56336
v_mfma_f32_16x16x32_bf16 v[40:43], a[252:255], v[212:215], v[40:43]
ds_read_b64_tr_b16 a[246:247], v2 offset:58896
s_waitcnt lgkmcnt(4)
v_mfma_f32_16x16x32_bf16 v[24:27], a[224:227], v[208:211], v[24:27]
ds_read_b64_tr_b16 a[248:249], v2 offset:56344
v_mfma_f32_16x16x32_bf16 v[44:47], a[224:227], v[212:215], v[44:47]
ds_read_b64_tr_b16 a[250:251], v2 offset:58904
s_waitcnt vmcnt(13)
v_mfma_f32_16x16x32_bf16 v[28:31], a[228:231], v[208:211], v[28:31]
ds_read_b64_tr_b16 a[252:253], v2 offset:56352
s_barrier
v_mfma_f32_16x16x32_bf16 v[48:51], a[228:231], v[212:215], v[48:51]
ds_read_b64_tr_b16 a[254:255], v2 offset:58912
v_mfma_f32_16x16x32_bf16 v[32:35], a[232:235], v[208:211], v[32:35]
ds_read_b64_tr_b16 a[224:225], v3
v_mfma_f32_16x16x32_bf16 v[52:55], a[232:235], v[212:215], v[52:55]
ds_read_b64_tr_b16 a[226:227], v3 offset:2560
v_mfma_f32_16x16x32_bf16 v[16:19], a[236:239], v[216:219], v[16:19]
ds_read_b64_tr_b16 a[228:229], v3 offset:8
v_mfma_f32_16x16x32_bf16 v[36:39], a[236:239], v[220:223], v[36:39]
ds_read_b64_tr_b16 a[230:231], v3 offset:2568
s_waitcnt lgkmcnt(4)
v_mfma_f32_16x16x32_bf16 v[20:23], a[240:243], v[216:219], v[20:23]
ds_read_b64_tr_b16 a[232:233], v3 offset:16
v_mfma_f32_16x16x32_bf16 v[40:43], a[240:243], v[220:223], v[40:43]
ds_read_b64_tr_b16 a[234:235], v3 offset:2576
v_mfma_f32_16x16x32_bf16 v[24:27], a[244:247], v[216:219], v[24:27]
ds_read_b64_tr_b16 a[236:237], v3 offset:24
v_mfma_f32_16x16x32_bf16 v[44:47], a[244:247], v[220:223], v[44:47]
ds_read_b64_tr_b16 a[238:239], v3 offset:2584
v_mfma_f32_16x16x32_bf16 v[28:31], a[248:251], v[216:219], v[28:31]
ds_read_b64_tr_b16 a[240:241], v3 offset:32
v_mfma_f32_16x16x32_bf16 v[48:51], a[248:251], v[220:223], v[48:51]
ds_read_b64_tr_b16 a[242:243], v3 offset:2592
v_mfma_f32_16x16x32_bf16 v[32:35], a[252:255], v[216:219], v[32:35]
ds_read_b64_tr_b16 a[244:245], v3 offset:5120
v_mfma_f32_16x16x32_bf16 v[52:55], a[252:255], v[220:223], v[52:55]
ds_read_b64_tr_b16 a[246:247], v3 offset:7680
s_waitcnt lgkmcnt(4)
v_mfma_f32_16x16x32_bf16 v[16:19], a[224:227], v[224:227], v[16:19]
ds_read_b64_tr_b16 a[248:249], v3 offset:5128
v_mfma_f32_16x16x32_bf16 v[36:39], a[224:227], v[228:231], v[36:39]
ds_read_b64_tr_b16 a[250:251], v3 offset:7688
v_mfma_f32_16x16x32_bf16 v[20:23], a[228:231], v[224:227], v[20:23]
ds_read_b64_tr_b16 a[252:253], v3 offset:5136
v_mfma_f32_16x16x32_bf16 v[40:43], a[228:231], v[228:231], v[40:43]
ds_read_b64_tr_b16 a[254:255], v3 offset:7696
v_mfma_f32_16x16x32_bf16 v[24:27], a[232:235], v[224:227], v[24:27]
ds_read_b64_tr_b16 a[224:225], v3 offset:5144
v_mfma_f32_16x16x32_bf16 v[44:47], a[232:235], v[228:231], v[44:47]
ds_read_b64_tr_b16 a[226:227], v3 offset:7704
v_mfma_f32_16x16x32_bf16 v[28:31], a[236:239], v[224:227], v[28:31]
ds_read_b64_tr_b16 a[228:229], v3 offset:5152
v_mfma_f32_16x16x32_bf16 v[48:51], a[236:239], v[228:231], v[48:51]
ds_read_b64_tr_b16 a[230:231], v3 offset:7712
s_waitcnt lgkmcnt(4)
v_mfma_f32_16x16x32_bf16 v[32:35], a[240:243], v[224:227], v[32:35]
ds_read_b64_tr_b16 a[232:233], v3 offset:10240
v_mfma_f32_16x16x32_bf16 v[52:55], a[240:243], v[228:231], v[52:55]
ds_read_b64_tr_b16 a[234:235], v3 offset:12800
v_mfma_f32_16x16x32_bf16 v[16:19], a[244:247], v[232:235], v[16:19]
ds_read_b64_tr_b16 a[236:237], v3 offset:10248
v_mfma_f32_16x16x32_bf16 v[36:39], a[244:247], v[236:239], v[36:39]
ds_read_b64_tr_b16 a[238:239], v3 offset:12808
v_mfma_f32_16x16x32_bf16 v[20:23], a[248:251], v[232:235], v[20:23]
ds_read_b64_tr_b16 a[240:241], v3 offset:10256
v_mfma_f32_16x16x32_bf16 v[40:43], a[248:251], v[236:239], v[40:43]
ds_read_b64_tr_b16 a[242:243], v3 offset:12816
v_mfma_f32_16x16x32_bf16 v[24:27], a[252:255], v[232:235], v[24:27]
ds_read_b64_tr_b16 a[244:245], v3 offset:10264
v_mfma_f32_16x16x32_bf16 v[44:47], a[252:255], v[236:239], v[44:47]
ds_read_b64_tr_b16 a[246:247], v3 offset:12824
s_waitcnt lgkmcnt(4)
v_mfma_f32_16x16x32_bf16 v[28:31], a[224:227], v[232:235], v[28:31]
ds_read_b64_tr_b16 a[248:249], v3 offset:10272
v_mfma_f32_16x16x32_bf16 v[48:51], a[224:227], v[236:239], v[48:51]
ds_read_b64_tr_b16 a[250:251], v3 offset:12832
v_mfma_f32_16x16x32_bf16 v[32:35], a[228:231], v[232:235], v[32:35]
ds_read_b64_tr_b16 a[252:253], v3 offset:15360
v_mfma_f32_16x16x32_bf16 v[52:55], a[228:231], v[236:239], v[52:55]
ds_read_b64_tr_b16 a[254:255], v3 offset:17920
v_mfma_f32_16x16x32_bf16 v[16:19], a[232:235], v[240:243], v[16:19]
ds_read_b64_tr_b16 a[224:225], v3 offset:15368
s_add_u32 s66, s3, 4096
v_mfma_f32_16x16x32_bf16 v[36:39], a[232:235], v[244:247], v[36:39]
ds_read_b64_tr_b16 a[226:227], v3 offset:17928
s_waitcnt vmcnt(7)
v_mfma_f32_16x16x32_bf16 v[20:23], a[236:239], v[240:243], v[20:23]
ds_read_b64_tr_b16 a[228:229], v3 offset:15376
buffer_store_dwordx4 a[100:103], v0, s[44:47], s3 offen sc1
v_mfma_f32_16x16x32_bf16 v[40:43], a[236:239], v[244:247], v[40:43]
ds_read_b64_tr_b16 a[230:231], v3 offset:17936
s_waitcnt lgkmcnt(4)
v_mfma_f32_16x16x32_bf16 v[24:27], a[240:243], v[240:243], v[24:27]
ds_read_b64_tr_b16 a[232:233], v3 offset:15384
buffer_store_dwordx4 a[104:107], v0, s[44:47], s3 offen offset:1024 sc1
v_mfma_f32_16x16x32_bf16 v[44:47], a[240:243], v[244:247], v[44:47]
ds_read_b64_tr_b16 a[234:235], v3 offset:17944
buffer_store_dwordx4 a[108:111], v0, s[44:47], s3 offen offset:2048 sc1
v_mfma_f32_16x16x32_bf16 v[28:31], a[244:247], v[240:243], v[28:31]
ds_read_b64_tr_b16 a[236:237], v3 offset:15392
buffer_store_dwordx4 a[112:115], v0, s[44:47], s3 offen offset:3072 sc1
v_mfma_f32_16x16x32_bf16 v[48:51], a[244:247], v[244:247], v[48:51]
ds_read_b64_tr_b16 a[238:239], v3 offset:17952
buffer_store_dwordx4 a[116:119], v0, s[44:47], s66 offen sc1
v_mfma_f32_16x16x32_bf16 v[32:35], a[248:251], v[240:243], v[32:35]
s_mul_i32 s68, s74, 122880
s_add_u32 s68, s68, 1280
v_mfma_f32_16x16x32_bf16 v[52:55], a[248:251], v[244:247], v[52:55]
s_add_u32 m0, s61, 20480
s_nop 0
buffer_load_dwordx4 v7, s[28:31], s68 offen lds
v_mfma_f32_16x16x32_bf16 v[16:19], a[252:255], v[248:251], v[16:19]
v_mfma_f32_16x16x32_bf16 v[36:39], a[252:255], v[252:255], v[36:39]
s_waitcnt lgkmcnt(0)
buffer_load_dwordx4 v8, s[28:31], s68 offen offset:1024 lds
v_mfma_f32_16x16x32_bf16 v[20:23], a[224:227], v[248:251], v[20:23]
v_mfma_f32_16x16x32_bf16 v[40:43], a[224:227], v[252:255], v[40:43]
v_mfma_f32_16x16x32_bf16 v[24:27], a[228:231], v[248:251], v[24:27]
v_mfma_f32_16x16x32_bf16 v[44:47], a[228:231], v[252:255], v[44:47]
v_mfma_f32_16x16x32_bf16 v[28:31], a[232:235], v[248:251], v[28:31]
v_mfma_f32_16x16x32_bf16 v[48:51], a[232:235], v[252:255], v[48:51]
buffer_load_dwordx4 v9, s[28:31], s68 offen offset:2048 lds
v_mfma_f32_16x16x32_bf16 v[32:35], a[236:239], v[248:251], v[32:35]
v_mfma_f32_16x16x32_bf16 v[52:55], a[236:239], v[252:255], v[52:55]
s_barrier
v_pk_mul_f32 v[16:17], v[16:17], v[14:15] op_sel:[0,0] op_sel_hi:[1,0]
v_pk_mul_f32 v[18:19], v[18:19], v[14:15] op_sel:[0,0] op_sel_hi:[1,0]
v_pk_mul_f32 v[20:21], v[20:21], v[14:15] op_sel:[0,0] op_sel_hi:[1,0]
v_pk_mul_f32 v[22:23], v[22:23], v[14:15] op_sel:[0,0] op_sel_hi:[1,0]
v_pk_mul_f32 v[24:25], v[24:25], v[14:15] op_sel:[0,0] op_sel_hi:[1,0]
v_pk_mul_f32 v[26:27], v[26:27], v[14:15] op_sel:[0,0] op_sel_hi:[1,0]
v_pk_mul_f32 v[28:29], v[28:29], v[14:15] op_sel:[0,0] op_sel_hi:[1,0]
v_pk_mul_f32 v[30:31], v[30:31], v[14:15] op_sel:[0,0] op_sel_hi:[1,0]
v_pk_mul_f32 v[32:33], v[32:33], v[14:15] op_sel:[0,0] op_sel_hi:[1,0]
v_pk_mul_f32 v[34:35], v[34:35], v[14:15] op_sel:[0,0] op_sel_hi:[1,0]
v_cvt_pk_bf16_f32 v76, v16, v17
v_cvt_pk_bf16_f32 v77, v18, v19
v_cvt_pk_bf16_f32 v78, v20, v21
v_cvt_pk_bf16_f32 v79, v22, v23
v_cvt_pk_bf16_f32 v80, v24, v25
v_cvt_pk_bf16_f32 v81, v26, v27
v_cvt_pk_bf16_f32 v82, v28, v29
v_cvt_pk_bf16_f32 v83, v30, v31
v_cvt_pk_bf16_f32 v84, v32, v33
v_cvt_pk_bf16_f32 v85, v34, v35
v_mfma_f32_16x16x32_bf16 v[16:19], a[0:3], v[76:79], 0
v_mul_f32 v36, v15, v36
v_mul_f32 v37, v15, v37
v_mfma_f32_16x16x32_bf16 v[20:23], a[10:13], v[76:79], 0
v_cvt_pk_bf16_f32 v86, v36, v37
v_mul_f32 v38, v15, v38
v_mfma_f32_16x16x32_bf16 v[24:27], a[20:23], v[76:79], 0
v_mul_f32 v39, v15, v39
v_cvt_pk_bf16_f32 v87, v38, v39
v_mfma_f32_16x16x32_bf16 v[28:31], a[30:33], v[76:79], 0
v_mul_f32 v40, v15, v40
v_mul_f32 v41, v15, v41
v_mfma_f32_16x16x32_bf16 v[32:35], a[40:43], v[76:79], 0
v_cvt_pk_bf16_f32 v88, v40, v41
v_mul_f32 v42, v15, v42
v_mfma_f32_16x16x32_bf16 v[16:19], a[4:7], v[80:83], v[16:19]
v_mul_f32 v43, v15, v43
v_cvt_pk_bf16_f32 v89, v42, v43
v_mfma_f32_16x16x32_bf16 v[20:23], a[14:17], v[80:83], v[20:23]
v_mul_f32 v44, v15, v44
v_mul_f32 v45, v15, v45
v_mfma_f32_16x16x32_bf16 v[24:27], a[24:27], v[80:83], v[24:27]
v_cvt_pk_bf16_f32 v90, v44, v45
v_mul_f32 v46, v15, v46
v_mfma_f32_16x16x32_bf16 v[28:31], a[34:37], v[80:83], v[28:31]
v_mul_f32 v47, v15, v47
v_cvt_pk_bf16_f32 v91, v46, v47
v_mfma_f32_16x16x32_bf16 v[32:35], a[44:47], v[80:83], v[32:35]
v_mul_f32 v48, v15, v48
v_mul_f32 v49, v15, v49
v_mfma_f32_16x16x16_bf16 v[16:19], a[8:9], v[84:85], v[16:19]
v_cvt_pk_bf16_f32 v92, v48, v49
v_mul_f32 v50, v15, v50
v_mfma_f32_16x16x16_bf16 v[20:23], a[18:19], v[84:85], v[20:23]
v_mul_f32 v51, v15, v51
v_cvt_pk_bf16_f32 v93, v50, v51
v_mfma_f32_16x16x16_bf16 v[24:27], a[28:29], v[84:85], v[24:27]
v_mul_f32 v52, v15, v52
v_mul_f32 v53, v15, v53
v_mfma_f32_16x16x16_bf16 v[28:31], a[38:39], v[84:85], v[28:31]
v_cvt_pk_bf16_f32 v94, v52, v53
v_mul_f32 v54, v15, v54
v_mfma_f32_16x16x16_bf16 v[32:35], a[48:49], v[84:85], v[32:35]
v_mul_f32 v55, v15, v55
v_cvt_pk_bf16_f32 v95, v54, v55
v_mfma_f32_16x16x32_bf16 v[36:39], a[0:3], v[86:89], 0
v_mfma_f32_16x16x32_bf16 v[40:43], a[10:13], v[86:89], 0
v_mfma_f32_16x16x32_bf16 v[44:47], a[20:23], v[86:89], 0
v_mfma_f32_16x16x32_bf16 v[48:51], a[30:33], v[86:89], 0
v_max3_f32 v4, v16, v17, v18
v_min3_f32 v5, v16, v17, v18
v_mfma_f32_16x16x32_bf16 v[52:55], a[40:43], v[86:89], 0
v_max3_f32 v4, v4, v19, v20
v_min3_f32 v5, v5, v19, v20
v_mfma_f32_16x16x32_bf16 v[36:39], a[4:7], v[90:93], v[36:39]
v_max3_f32 v4, v4, v21, v22
v_min3_f32 v5, v5, v21, v22
v_mfma_f32_16x16x32_bf16 v[40:43], a[14:17], v[90:93], v[40:43]
v_max3_f32 v4, v4, v23, v24
v_min3_f32 v5, v5, v23, v24
v_mfma_f32_16x16x32_bf16 v[44:47], a[24:27], v[90:93], v[44:47]
v_max3_f32 v4, v4, v25, v26
v_min3_f32 v5, v5, v25, v26
v_mfma_f32_16x16x32_bf16 v[48:51], a[34:37], v[90:93], v[48:51]
v_max3_f32 v4, v4, v27, v28
v_min3_f32 v5, v5, v27, v28
v_mfma_f32_16x16x32_bf16 v[52:55], a[44:47], v[90:93], v[52:55]
v_max3_f32 v4, v4, v29, v30
v_min3_f32 v5, v5, v29, v30
v_mfma_f32_16x16x16_bf16 v[36:39], a[8:9], v[94:95], v[36:39]
v_max3_f32 v4, v4, v31, v32
v_min3_f32 v5, v5, v31, v32
v_mfma_f32_16x16x16_bf16 v[40:43], a[18:19], v[94:95], v[40:43]
v_max3_f32 v4, v4, v33, v34
v_min3_f32 v5, v5, v33, v34
v_mfma_f32_16x16x16_bf16 v[44:47], a[28:29], v[94:95], v[44:47]
v_max_f32 v4, v4, v35
v_min_f32 v5, v5, v35
v_mfma_f32_16x16x16_bf16 v[48:51], a[38:39], v[94:95], v[48:51]
v_mul_f32 v4, s13, v4
v_mul_f32 v5, s12, v5
v_mfma_f32_16x16x16_bf16 v[52:55], a[48:49], v[94:95], v[52:55]
v_pk_fma_f32 v[56:57], v[16:17], s[12:13], v[4:5] op_sel:[0,0,0] op_sel_hi:[1,0,0]
v_pk_fma_f32 v[16:17], v[16:17], s[12:13], v[4:5] op_sel:[0,1,1] op_sel_hi:[1,1,1]
v_pk_fma_f32 v[58:59], v[18:19], s[12:13], v[4:5] op_sel:[0,0,0] op_sel_hi:[1,0,0]
v_pk_fma_f32 v[18:19], v[18:19], s[12:13], v[4:5] op_sel:[0,1,1] op_sel_hi:[1,1,1]
v_pk_fma_f32 v[60:61], v[20:21], s[12:13], v[4:5] op_sel:[0,0,0] op_sel_hi:[1,0,0]
v_pk_fma_f32 v[20:21], v[20:21], s[12:13], v[4:5] op_sel:[0,1,1] op_sel_hi:[1,1,1]
v_pk_fma_f32 v[62:63], v[22:23], s[12:13], v[4:5] op_sel:[0,0,0] op_sel_hi:[1,0,0]
v_pk_fma_f32 v[22:23], v[22:23], s[12:13], v[4:5] op_sel:[0,1,1] op_sel_hi:[1,1,1]
v_pk_fma_f32 v[64:65], v[24:25], s[12:13], v[4:5] op_sel:[0,0,0] op_sel_hi:[1,0,0]
v_pk_fma_f32 v[24:25], v[24:25], s[12:13], v[4:5] op_sel:[0,1,1] op_sel_hi:[1,1,1]
v_pk_fma_f32 v[66:67], v[26:27], s[12:13], v[4:5] op_sel:[0,0,0] op_sel_hi:[1,0,0]
v_pk_fma_f32 v[26:27], v[26:27], s[12:13], v[4:5] op_sel:[0,1,1] op_sel_hi:[1,1,1]
v_pk_fma_f32 v[68:69], v[28:29], s[12:13], v[4:5] op_sel:[0,0,0] op_sel_hi:[1,0,0]
v_pk_fma_f32 v[28:29], v[28:29], s[12:13], v[4:5] op_sel:[0,1,1] op_sel_hi:[1,1,1]
v_pk_fma_f32 v[70:71], v[30:31], s[12:13], v[4:5] op_sel:[0,0,0] op_sel_hi:[1,0,0]
v_pk_fma_f32 v[30:31], v[30:31], s[12:13], v[4:5] op_sel:[0,1,1] op_sel_hi:[1,1,1]
v_pk_fma_f32 v[72:73], v[32:33], s[12:13], v[4:5] op_sel:[0,0,0] op_sel_hi:[1,0,0]
v_pk_fma_f32 v[32:33], v[32:33], s[12:13], v[4:5] op_sel:[0,1,1] op_sel_hi:[1,1,1]
v_pk_fma_f32 v[74:75], v[34:35], s[12:13], v[4:5] op_sel:[0,0,0] op_sel_hi:[1,0,0]
v_pk_fma_f32 v[34:35], v[34:35], s[12:13], v[4:5] op_sel:[0,1,1] op_sel_hi:[1,1,1]
buffer_load_dwordx4 v10, s[28:31], s68 offen offset:3072 lds
v_exp_f32 v56, v56
v_exp_f32 v16, v16
v_exp_f32 v57, v57
v_exp_f32 v17, v17
v_exp_f32 v58, v58
v_exp_f32 v18, v18
s_add_u32 m0, s61, 24576
s_nop 0
buffer_load_dwordx4 v11, s[28:31], s68 offen lds
v_exp_f32 v59, v59
v_exp_f32 v19, v19
v_exp_f32 v60, v60
v_exp_f32 v20, v20
v_exp_f32 v61, v61
v_exp_f32 v21, v21
v_exp_f32 v62, v62
v_exp_f32 v22, v22
v_exp_f32 v63, v63
v_exp_f32 v23, v23
s_mul_i32 s68, s75, 122880
s_add_u32 s68, s68, 1280
s_add_u32 m0, s61, 81920
s_nop 0
buffer_load_dwordx4 v7, s[28:31], s68 offen lds
v_exp_f32 v64, v64
v_exp_f32 v24, v24
v_exp_f32 v65, v65
v_exp_f32 v25, v25
v_exp_f32 v66, v66
v_exp_f32 v26, v26
v_exp_f32 v67, v67
v_exp_f32 v27, v27
v_exp_f32 v68, v68
v_exp_f32 v28, v28
v_exp_f32 v69, v69
v_exp_f32 v29, v29
buffer_load_dwordx4 v8, s[28:31], s68 offen offset:1024 lds
v_exp_f32 v70, v70
v_exp_f32 v30, v30
v_exp_f32 v71, v71
v_exp_f32 v31, v31
v_exp_f32 v72, v72
v_exp_f32 v32, v32
v_exp_f32 v73, v73
v_exp_f32 v33, v33
v_exp_f32 v74, v74
v_exp_f32 v34, v34
v_exp_f32 v75, v75
v_exp_f32 v35, v35
buffer_load_dwordx4 v9, s[28:31], s68 offen offset:2048 lds
v_pk_add_f32 v[76:77], v[56:57], v[58:59]
v_pk_add_f32 v[78:79], v[16:17], v[18:19]
v_pk_add_f32 v[76:77], v[76:77], v[60:61]
v_pk_add_f32 v[78:79], v[78:79], v[20:21]
v_pk_add_f32 v[76:77], v[76:77], v[62:63]
v_pk_add_f32 v[78:79], v[78:79], v[22:23]
v_pk_add_f32 v[76:77], v[76:77], v[64:65]
v_pk_add_f32 v[78:79], v[78:79], v[24:25]
v_pk_add_f32 v[76:77], v[76:77], v[66:67]
v_pk_add_f32 v[78:79], v[78:79], v[26:27]
v_pk_add_f32 v[76:77], v[76:77], v[68:69]
v_pk_add_f32 v[78:79], v[78:79], v[28:29]
v_pk_add_f32 v[76:77], v[76:77], v[70:71]
v_pk_add_f32 v[78:79], v[78:79], v[30:31]
v_pk_add_f32 v[76:77], v[76:77], v[72:73]
v_pk_add_f32 v[78:79], v[78:79], v[32:33]
v_pk_add_f32 v[76:77], v[76:77], v[74:75]
v_pk_add_f32 v[78:79], v[78:79], v[34:35]
v_add_f32 v4, v76, v77
v_add_f32 v5, v78, v79
v_rcp_f32 v4, v4
v_rcp_f32 v5, v5
buffer_load_dwordx4 v10, s[28:31], s68 offen offset:3072 lds
v_pk_mul_f32 v[56:57], v[56:57], v[4:5] op_sel:[0,0] op_sel_hi:[1,0]
v_pk_mul_f32 v[16:17], v[16:17], v[4:5] op_sel:[0,1] op_sel_hi:[1,1]
v_pk_mul_f32 v[58:59], v[58:59], v[4:5] op_sel:[0,0] op_sel_hi:[1,0]
v_pk_mul_f32 v[18:19], v[18:19], v[4:5] op_sel:[0,1] op_sel_hi:[1,1]
v_pk_mul_f32 v[60:61], v[60:61], v[4:5] op_sel:[0,0] op_sel_hi:[1,0]
v_pk_mul_f32 v[20:21], v[20:21], v[4:5] op_sel:[0,1] op_sel_hi:[1,1]
v_pk_mul_f32 v[62:63], v[62:63], v[4:5] op_sel:[0,0] op_sel_hi:[1,0]
v_pk_mul_f32 v[22:23], v[22:23], v[4:5] op_sel:[0,1] op_sel_hi:[1,1]
v_pk_mul_f32 v[64:65], v[64:65], v[4:5] op_sel:[0,0] op_sel_hi:[1,0]
v_pk_mul_f32 v[24:25], v[24:25], v[4:5] op_sel:[0,1] op_sel_hi:[1,1]
v_pk_mul_f32 v[66:67], v[66:67], v[4:5] op_sel:[0,0] op_sel_hi:[1,0]
)ASM"
R"ASM(
v_pk_mul_f32 v[26:27], v[26:27], v[4:5] op_sel:[0,1] op_sel_hi:[1,1]
v_pk_mul_f32 v[68:69], v[68:69], v[4:5] op_sel:[0,0] op_sel_hi:[1,0]
v_pk_mul_f32 v[28:29], v[28:29], v[4:5] op_sel:[0,1] op_sel_hi:[1,1]
v_pk_mul_f32 v[70:71], v[70:71], v[4:5] op_sel:[0,0] op_sel_hi:[1,0]
v_pk_mul_f32 v[30:31], v[30:31], v[4:5] op_sel:[0,1] op_sel_hi:[1,1]
v_pk_mul_f32 v[72:73], v[72:73], v[4:5] op_sel:[0,0] op_sel_hi:[1,0]
v_pk_mul_f32 v[32:33], v[32:33], v[4:5] op_sel:[0,1] op_sel_hi:[1,1]
v_pk_mul_f32 v[74:75], v[74:75], v[4:5] op_sel:[0,0] op_sel_hi:[1,0]
v_pk_mul_f32 v[34:35], v[34:35], v[4:5] op_sel:[0,1] op_sel_hi:[1,1]
v_cvt_pk_bf16_f32 v76, v56, v57
v_cvt_pk_bf16_f32 v77, v58, v59
v_cvt_pk_bf16_f32 v78, v60, v61
v_cvt_pk_bf16_f32 v79, v62, v63
v_cvt_pk_bf16_f32 v80, v64, v65
v_cvt_pk_bf16_f32 v81, v66, v67
v_cvt_pk_bf16_f32 v82, v68, v69
v_cvt_pk_bf16_f32 v83, v70, v71
v_cvt_pk_bf16_f32 v84, v72, v73
v_cvt_pk_bf16_f32 v85, v74, v75
v_cvt_pk_bf16_f32 v86, v16, v17
v_cvt_pk_bf16_f32 v87, v18, v19
v_cvt_pk_bf16_f32 v88, v20, v21
v_cvt_pk_bf16_f32 v89, v22, v23
v_cvt_pk_bf16_f32 v90, v24, v25
v_cvt_pk_bf16_f32 v91, v26, v27
v_cvt_pk_bf16_f32 v92, v28, v29
v_cvt_pk_bf16_f32 v93, v30, v31
v_cvt_pk_bf16_f32 v94, v32, v33
v_cvt_pk_bf16_f32 v95, v34, v35
s_add_u32 m0, s61, 86016
s_nop 0
buffer_load_dwordx4 v11, s[28:31], s68 offen lds
v_mov_b32 v12, s62
v_lshrrev_b32 v13, 2, v0
s_add_u32 s65, s64, 1310720
s_waitcnt vmcnt(10)
buffer_store_dword v12, v13, s[44:47], s65 offen sc1
v_accvgpr_read_b32 v6, a194
s_lshr_b32 s65, s56, 1
s_mul_i32 s65, s65, 20480
s_add_u32 s65, s65, 40960
v_add_u32 v6, s65, v6
ds_write_b128 v6, v[76:79]
ds_write_b128 v6, v[80:83] offset:16
ds_write_b128 v6, v[84:87] offset:32
ds_write_b128 v6, v[88:91] offset:48
ds_write_b128 v6, v[92:95] offset:64
v_max3_f32 v12, v36, v37, v38
v_min3_f32 v13, v36, v37, v38
v_max3_f32 v12, v12, v39, v40
v_min3_f32 v13, v13, v39, v40
v_max3_f32 v12, v12, v41, v42
v_min3_f32 v13, v13, v41, v42
v_max3_f32 v12, v12, v43, v44
v_min3_f32 v13, v13, v43, v44
v_max3_f32 v12, v12, v45, v46
v_min3_f32 v13, v13, v45, v46
v_max3_f32 v12, v12, v47, v48
v_min3_f32 v13, v13, v47, v48
v_max3_f32 v12, v12, v49, v50
v_min3_f32 v13, v13, v49, v50
v_max3_f32 v12, v12, v51, v52
v_min3_f32 v13, v13, v51, v52
v_max3_f32 v12, v12, v53, v54
v_min3_f32 v13, v13, v53, v54
v_max_f32 v12, v12, v55
v_min_f32 v13, v13, v55
v_mul_f32 v12, s13, v12
v_mul_f32 v13, s12, v13
s_barrier
ds_read_b64_tr_b16 a[224:225], v1
ds_read_b64_tr_b16 a[226:227], v1 offset:2560
ds_read_b64_tr_b16 a[228:229], v1 offset:8
ds_read_b64_tr_b16 a[230:231], v1 offset:2568
ds_read_b64_tr_b16 a[232:233], v1 offset:16
ds_read_b64_tr_b16 a[234:235], v1 offset:2576
ds_read_b64_tr_b16 a[236:237], v1 offset:24
ds_read_b64_tr_b16 a[238:239], v1 offset:2584
ds_read_b64_tr_b16 a[240:241], v1 offset:32
ds_read_b64_tr_b16 a[242:243], v1 offset:2592
s_waitcnt lgkmcnt(14)
ds_read_b64_tr_b16 a[244:245], v1 offset:5120
s_waitcnt lgkmcnt(14)
ds_read_b64_tr_b16 a[246:247], v1 offset:7680
v_pk_fma_f32 v[56:57], v[36:37], s[12:13], v[12:13] op_sel:[0,0,0] op_sel_hi:[1,0,0]
v_pk_fma_f32 v[36:37], v[36:37], s[12:13], v[12:13] op_sel:[0,1,1] op_sel_hi:[1,1,1]
v_pk_fma_f32 v[58:59], v[38:39], s[12:13], v[12:13] op_sel:[0,0,0] op_sel_hi:[1,0,0]
v_pk_fma_f32 v[38:39], v[38:39], s[12:13], v[12:13] op_sel:[0,1,1] op_sel_hi:[1,1,1]
v_pk_fma_f32 v[60:61], v[40:41], s[12:13], v[12:13] op_sel:[0,0,0] op_sel_hi:[1,0,0]
v_pk_fma_f32 v[40:41], v[40:41], s[12:13], v[12:13] op_sel:[0,1,1] op_sel_hi:[1,1,1]
v_pk_fma_f32 v[62:63], v[42:43], s[12:13], v[12:13] op_sel:[0,0,0] op_sel_hi:[1,0,0]
v_pk_fma_f32 v[42:43], v[42:43], s[12:13], v[12:13] op_sel:[0,1,1] op_sel_hi:[1,1,1]
v_pk_fma_f32 v[64:65], v[44:45], s[12:13], v[12:13] op_sel:[0,0,0] op_sel_hi:[1,0,0]
v_pk_fma_f32 v[44:45], v[44:45], s[12:13], v[12:13] op_sel:[0,1,1] op_sel_hi:[1,1,1]
v_pk_fma_f32 v[66:67], v[46:47], s[12:13], v[12:13] op_sel:[0,0,0] op_sel_hi:[1,0,0]
v_pk_fma_f32 v[46:47], v[46:47], s[12:13], v[12:13] op_sel:[0,1,1] op_sel_hi:[1,1,1]
v_pk_fma_f32 v[68:69], v[48:49], s[12:13], v[12:13] op_sel:[0,0,0] op_sel_hi:[1,0,0]
v_pk_fma_f32 v[48:49], v[48:49], s[12:13], v[12:13] op_sel:[0,1,1] op_sel_hi:[1,1,1]
v_pk_fma_f32 v[70:71], v[50:51], s[12:13], v[12:13] op_sel:[0,0,0] op_sel_hi:[1,0,0]
v_pk_fma_f32 v[50:51], v[50:51], s[12:13], v[12:13] op_sel:[0,1,1] op_sel_hi:[1,1,1]
v_pk_fma_f32 v[72:73], v[52:53], s[12:13], v[12:13] op_sel:[0,0,0] op_sel_hi:[1,0,0]
v_pk_fma_f32 v[52:53], v[52:53], s[12:13], v[12:13] op_sel:[0,1,1] op_sel_hi:[1,1,1]
v_pk_fma_f32 v[74:75], v[54:55], s[12:13], v[12:13] op_sel:[0,0,0] op_sel_hi:[1,0,0]
v_pk_fma_f32 v[54:55], v[54:55], s[12:13], v[12:13] op_sel:[0,1,1] op_sel_hi:[1,1,1]
s_mul_i32 s68, s76, 122880
s_add_u32 s68, s68, 1280
s_add_u32 m0, s61, 102400
s_nop 0
buffer_load_dwordx4 v7, s[28:31], s68 offen lds
s_waitcnt lgkmcnt(8)
v_mfma_f32_16x16x32_bf16 v[16:19], a[224:227], v[96:99], 0
ds_read_b64_tr_b16 a[248:249], v1 offset:5128
v_exp_f32 v56, v56
v_mfma_f32_16x16x32_bf16 v[76:79], a[224:227], v[100:103], 0
ds_read_b64_tr_b16 a[250:251], v1 offset:7688
buffer_load_dwordx4 v8, s[28:31], s68 offen offset:1024 lds
v_mfma_f32_16x16x32_bf16 v[20:23], a[228:231], v[96:99], 0
ds_read_b64_tr_b16 a[252:253], v1 offset:5136
v_exp_f32 v57, v57
v_mfma_f32_16x16x32_bf16 v[80:83], a[228:231], v[100:103], 0
ds_read_b64_tr_b16 a[254:255], v1 offset:7696
s_waitcnt lgkmcnt(8)
v_mfma_f32_16x16x32_bf16 v[24:27], a[232:235], v[96:99], 0
ds_read_b64_tr_b16 a[224:225], v1 offset:5144
v_add_f32 v12, v56, v57
v_mfma_f32_16x16x32_bf16 v[84:87], a[232:235], v[100:103], 0
ds_read_b64_tr_b16 a[226:227], v1 offset:7704
v_exp_f32 v58, v58
v_mfma_f32_16x16x32_bf16 v[28:31], a[236:239], v[96:99], 0
ds_read_b64_tr_b16 a[228:229], v1 offset:5152
buffer_load_dwordx4 v9, s[28:31], s68 offen offset:2048 lds
v_mfma_f32_16x16x32_bf16 v[88:91], a[236:239], v[100:103], 0
ds_read_b64_tr_b16 a[230:231], v1 offset:7712
s_waitcnt lgkmcnt(4)
v_mfma_f32_16x16x32_bf16 v[32:35], a[240:243], v[96:99], 0
ds_read_b64_tr_b16 a[232:233], v1 offset:10240
v_add_f32 v12, v12, v58
v_mfma_f32_16x16x32_bf16 v[92:95], a[240:243], v[100:103], 0
ds_read_b64_tr_b16 a[234:235], v1 offset:12800
v_exp_f32 v59, v59
v_mfma_f32_16x16x32_bf16 v[16:19], a[244:247], v[104:107], v[16:19]
ds_read_b64_tr_b16 a[236:237], v1 offset:10248
v_add_f32 v12, v12, v59
v_mfma_f32_16x16x32_bf16 v[76:79], a[244:247], v[108:111], v[76:79]
ds_read_b64_tr_b16 a[238:239], v1 offset:12808
v_exp_f32 v60, v60
v_mfma_f32_16x16x32_bf16 v[20:23], a[248:251], v[104:107], v[20:23]
ds_read_b64_tr_b16 a[240:241], v1 offset:10256
buffer_load_dwordx4 v10, s[28:31], s68 offen offset:3072 lds
v_mfma_f32_16x16x32_bf16 v[80:83], a[248:251], v[108:111], v[80:83]
ds_read_b64_tr_b16 a[242:243], v1 offset:12816
v_add_f32 v12, v12, v60
v_mfma_f32_16x16x32_bf16 v[24:27], a[252:255], v[104:107], v[24:27]
ds_read_b64_tr_b16 a[244:245], v1 offset:10264
v_exp_f32 v61, v61
v_mfma_f32_16x16x32_bf16 v[84:87], a[252:255], v[108:111], v[84:87]
ds_read_b64_tr_b16 a[246:247], v1 offset:12824
s_waitcnt lgkmcnt(4)
v_mfma_f32_16x16x32_bf16 v[28:31], a[224:227], v[104:107], v[28:31]
ds_read_b64_tr_b16 a[248:249], v1 offset:10272
v_add_f32 v12, v12, v61
v_mfma_f32_16x16x32_bf16 v[88:91], a[224:227], v[108:111], v[88:91]
ds_read_b64_tr_b16 a[250:251], v1 offset:12832
v_exp_f32 v62, v62
v_mfma_f32_16x16x32_bf16 v[32:35], a[228:231], v[104:107], v[32:35]
ds_read_b64_tr_b16 a[252:253], v1 offset:15360
s_add_u32 m0, s61, 106496
v_mfma_f32_16x16x32_bf16 v[92:95], a[228:231], v[108:111], v[92:95]
ds_read_b64_tr_b16 a[254:255], v1 offset:17920
buffer_load_dwordx4 v11, s[28:31], s68 offen lds
v_mfma_f32_16x16x32_bf16 v[16:19], a[232:235], v[112:115], v[16:19]
ds_read_b64_tr_b16 a[224:225], v1 offset:15368
v_add_f32 v12, v12, v62
v_mfma_f32_16x16x32_bf16 v[76:79], a[232:235], v[116:119], v[76:79]
ds_read_b64_tr_b16 a[226:227], v1 offset:17928
v_exp_f32 v63, v63
v_mfma_f32_16x16x32_bf16 v[20:23], a[236:239], v[112:115], v[20:23]
ds_read_b64_tr_b16 a[228:229], v1 offset:15376
v_add_f32 v12, v12, v63
v_mfma_f32_16x16x32_bf16 v[80:83], a[236:239], v[116:119], v[80:83]
ds_read_b64_tr_b16 a[230:231], v1 offset:17936
s_waitcnt lgkmcnt(4)
v_mfma_f32_16x16x32_bf16 v[24:27], a[240:243], v[112:115], v[24:27]
ds_read_b64_tr_b16 a[232:233], v1 offset:15384
s_mul_i32 s68, s77, 122880
v_mfma_f32_16x16x32_bf16 v[84:87], a[240:243], v[116:119], v[84:87]
ds_read_b64_tr_b16 a[234:235], v1 offset:17944
s_waitcnt vmcnt(11)
v_mfma_f32_16x16x32_bf16 v[28:31], a[244:247], v[112:115], v[28:31]
ds_read_b64_tr_b16 a[236:237], v1 offset:15392
s_barrier
v_mfma_f32_16x16x32_bf16 v[88:91], a[244:247], v[116:119], v[88:91]
ds_read_b64_tr_b16 a[238:239], v1 offset:17952
s_add_u32 s68, s68, 1280
v_mfma_f32_16x16x32_bf16 v[32:35], a[248:251], v[112:115], v[32:35]
ds_read_b64_tr_b16 a[240:241], v1 offset:20480
s_add_u32 m0, s61, 122880
v_mfma_f32_16x16x32_bf16 v[92:95], a[248:251], v[116:119], v[92:95]
ds_read_b64_tr_b16 a[242:243], v1 offset:23040
buffer_load_dwordx4 v7, s[28:31], s68 offen lds
v_mfma_f32_16x16x32_bf16 v[16:19], a[252:255], v[120:123], v[16:19]
ds_read_b64_tr_b16 a[244:245], v1 offset:20488
buffer_load_dwordx4 v8, s[28:31], s68 offen offset:1024 lds
v_mfma_f32_16x16x32_bf16 v[76:79], a[252:255], v[124:127], v[76:79]
ds_read_b64_tr_b16 a[246:247], v1 offset:23048
s_waitcnt lgkmcnt(4)
v_mfma_f32_16x16x32_bf16 v[20:23], a[224:227], v[120:123], v[20:23]
ds_read_b64_tr_b16 a[248:249], v1 offset:20496
v_exp_f32 v64, v64
v_mfma_f32_16x16x32_bf16 v[80:83], a[224:227], v[124:127], v[80:83]
ds_read_b64_tr_b16 a[250:251], v1 offset:23056
v_add_f32 v12, v12, v64
v_mfma_f32_16x16x32_bf16 v[24:27], a[228:231], v[120:123], v[24:27]
ds_read_b64_tr_b16 a[252:253], v1 offset:20504
v_exp_f32 v65, v65
v_mfma_f32_16x16x32_bf16 v[84:87], a[228:231], v[124:127], v[84:87]
ds_read_b64_tr_b16 a[254:255], v1 offset:23064
v_add_f32 v12, v12, v65
v_mfma_f32_16x16x32_bf16 v[28:31], a[232:235], v[120:123], v[28:31]
ds_read_b64_tr_b16 a[224:225], v1 offset:20512
buffer_load_dwordx4 v9, s[28:31], s68 offen offset:2048 lds
v_mfma_f32_16x16x32_bf16 v[88:91], a[232:235], v[124:127], v[88:91]
ds_read_b64_tr_b16 a[226:227], v1 offset:23072
v_exp_f32 v66, v66
v_mfma_f32_16x16x32_bf16 v[32:35], a[236:239], v[120:123], v[32:35]
ds_read_b64_tr_b16 a[228:229], v1 offset:25600
v_add_f32 v12, v12, v66
v_mfma_f32_16x16x32_bf16 v[92:95], a[236:239], v[124:127], v[92:95]
ds_read_b64_tr_b16 a[230:231], v1 offset:28160
s_waitcnt lgkmcnt(4)
v_mfma_f32_16x16x32_bf16 v[16:19], a[240:243], v[128:131], v[16:19]
ds_read_b64_tr_b16 a[232:233], v1 offset:25608
v_exp_f32 v67, v67
v_mfma_f32_16x16x32_bf16 v[76:79], a[240:243], v[132:135], v[76:79]
ds_read_b64_tr_b16 a[234:235], v1 offset:28168
v_add_f32 v12, v12, v67
v_mfma_f32_16x16x32_bf16 v[20:23], a[244:247], v[128:131], v[20:23]
ds_read_b64_tr_b16 a[236:237], v1 offset:25616
buffer_load_dwordx4 v10, s[28:31], s68 offen offset:3072 lds
v_mfma_f32_16x16x32_bf16 v[80:83], a[244:247], v[132:135], v[80:83]
ds_read_b64_tr_b16 a[238:239], v1 offset:28176
v_exp_f32 v68, v68
v_mfma_f32_16x16x32_bf16 v[24:27], a[248:251], v[128:131], v[24:27]
ds_read_b64_tr_b16 a[240:241], v1 offset:25624
v_add_f32 v12, v12, v68
v_mfma_f32_16x16x32_bf16 v[84:87], a[248:251], v[132:135], v[84:87]
ds_read_b64_tr_b16 a[242:243], v1 offset:28184
v_exp_f32 v69, v69
v_mfma_f32_16x16x32_bf16 v[28:31], a[252:255], v[128:131], v[28:31]
ds_read_b64_tr_b16 a[244:245], v1 offset:25632
v_add_f32 v12, v12, v69
v_mfma_f32_16x16x32_bf16 v[88:91], a[252:255], v[132:135], v[88:91]
ds_read_b64_tr_b16 a[246:247], v1 offset:28192
s_waitcnt lgkmcnt(4)
v_mfma_f32_16x16x32_bf16 v[32:35], a[224:227], v[128:131], v[32:35]
ds_read_b64_tr_b16 a[248:249], v1 offset:30720
s_add_u32 m0, s61, 126976
v_mfma_f32_16x16x32_bf16 v[92:95], a[224:227], v[132:135], v[92:95]
ds_read_b64_tr_b16 a[250:251], v1 offset:33280
buffer_load_dwordx4 v11, s[28:31], s68 offen lds
v_mfma_f32_16x16x32_bf16 v[16:19], a[228:231], v[136:139], v[16:19]
ds_read_b64_tr_b16 a[252:253], v1 offset:30728
v_exp_f32 v70, v70
v_mfma_f32_16x16x32_bf16 v[76:79], a[228:231], v[140:143], v[76:79]
ds_read_b64_tr_b16 a[254:255], v1 offset:33288
v_add_f32 v12, v12, v70
v_mfma_f32_16x16x32_bf16 v[20:23], a[232:235], v[136:139], v[20:23]
ds_read_b64_tr_b16 a[224:225], v1 offset:30736
v_exp_f32 v71, v71
v_mfma_f32_16x16x32_bf16 v[80:83], a[232:235], v[140:143], v[80:83]
ds_read_b64_tr_b16 a[226:227], v1 offset:33296
v_add_f32 v12, v12, v71
v_mfma_f32_16x16x32_bf16 v[24:27], a[236:239], v[136:139], v[24:27]
ds_read_b64_tr_b16 a[228:229], v1 offset:30744
v_accvgpr_read_b32 v7, a188
v_mfma_f32_16x16x32_bf16 v[84:87], a[236:239], v[140:143], v[84:87]
ds_read_b64_tr_b16 a[230:231], v1 offset:33304
s_waitcnt lgkmcnt(4)
v_mfma_f32_16x16x32_bf16 v[28:31], a[240:243], v[136:139], v[28:31]
ds_read_b64_tr_b16 a[232:233], v1 offset:30752
v_add_u32 v8, 2016, v7
v_mfma_f32_16x16x32_bf16 v[88:91], a[240:243], v[140:143], v[88:91]
ds_read_b64_tr_b16 a[234:235], v1 offset:33312
v_add_u32 v9, 4032, v7
v_mfma_f32_16x16x32_bf16 v[32:35], a[244:247], v[136:139], v[32:35]
ds_read_b64_tr_b16 a[236:237], v1 offset:35840
v_add_u32 v10, 6048, v7
v_mfma_f32_16x16x32_bf16 v[92:95], a[244:247], v[140:143], v[92:95]
ds_read_b64_tr_b16 a[238:239], v1 offset:38400
v_exp_f32 v72, v72
v_mfma_f32_16x16x32_bf16 v[16:19], a[248:251], v[144:147], v[16:19]
ds_read_b64_tr_b16 a[240:241], v1 offset:35848
v_add_f32 v12, v12, v72
v_mfma_f32_16x16x32_bf16 v[76:79], a[248:251], v[148:151], v[76:79]
ds_read_b64_tr_b16 a[242:243], v1 offset:38408
v_exp_f32 v73, v73
v_mfma_f32_16x16x32_bf16 v[20:23], a[252:255], v[144:147], v[20:23]
ds_read_b64_tr_b16 a[244:245], v1 offset:35856
v_add_f32 v12, v12, v73
v_mfma_f32_16x16x32_bf16 v[80:83], a[252:255], v[148:151], v[80:83]
ds_read_b64_tr_b16 a[246:247], v1 offset:38416
s_waitcnt lgkmcnt(4)
v_mfma_f32_16x16x32_bf16 v[24:27], a[224:227], v[144:147], v[24:27]
ds_read_b64_tr_b16 a[248:249], v1 offset:35864
v_exp_f32 v74, v74
v_mfma_f32_16x16x32_bf16 v[84:87], a[224:227], v[148:151], v[84:87]
ds_read_b64_tr_b16 a[250:251], v1 offset:38424
s_waitcnt vmcnt(11)
v_mfma_f32_16x16x32_bf16 v[28:31], a[228:231], v[144:147], v[28:31]
ds_read_b64_tr_b16 a[252:253], v1 offset:35872
s_barrier
v_mfma_f32_16x16x32_bf16 v[88:91], a[228:231], v[148:151], v[88:91]
ds_read_b64_tr_b16 a[254:255], v1 offset:38432
s_mul_i32 s68, s73, 98304
v_mfma_f32_16x16x32_bf16 v[32:35], a[232:235], v[144:147], v[32:35]
ds_read_b64_tr_b16 a[224:225], v2 offset:20480
s_add_u32 m0, s61, 0
v_mfma_f32_16x16x32_bf16 v[92:95], a[232:235], v[148:151], v[92:95]
ds_read_b64_tr_b16 a[226:227], v2 offset:23040
buffer_load_dwordx4 v7, s[32:35], s68 offen lds
v_mfma_f32_16x16x32_bf16 v[16:19], a[236:239], v[152:155], v[16:19]
ds_read_b64_tr_b16 a[228:229], v2 offset:20488
buffer_load_dwordx4 v8, s[32:35], s68 offen offset:1056 lds
v_mfma_f32_16x16x32_bf16 v[76:79], a[236:239], v[156:159], v[76:79]
ds_read_b64_tr_b16 a[230:231], v2 offset:23048
s_waitcnt lgkmcnt(4)
v_mfma_f32_16x16x32_bf16 v[20:23], a[240:243], v[152:155], v[20:23]
ds_read_b64_tr_b16 a[232:233], v2 offset:20496
buffer_load_dwordx4 v9, s[32:35], s68 offen offset:2112 lds
v_mfma_f32_16x16x32_bf16 v[80:83], a[240:243], v[156:159], v[80:83]
ds_read_b64_tr_b16 a[234:235], v2 offset:23056
v_add_f32 v12, v12, v74
v_mfma_f32_16x16x32_bf16 v[24:27], a[244:247], v[152:155], v[24:27]
ds_read_b64_tr_b16 a[236:237], v2 offset:20504
v_exp_f32 v75, v75
v_mfma_f32_16x16x32_bf16 v[84:87], a[244:247], v[156:159], v[84:87]
ds_read_b64_tr_b16 a[238:239], v2 offset:23064
buffer_load_dwordx4 v10, s[32:35], s68 offen offset:3168 lds
v_mfma_f32_16x16x32_bf16 v[28:31], a[248:251], v[152:155], v[28:31]
ds_read_b64_tr_b16 a[240:241], v2 offset:20512
v_add_f32 v12, v12, v75
v_mfma_f32_16x16x32_bf16 v[88:91], a[248:251], v[156:159], v[88:91]
ds_read_b64_tr_b16 a[242:243], v2 offset:23072
v_rcp_f32 v12, v12
v_mfma_f32_16x16x32_bf16 v[32:35], a[252:255], v[152:155], v[32:35]
ds_read_b64_tr_b16 a[244:245], v2 offset:25600
v_exp_f32 v36, v36
v_mfma_f32_16x16x32_bf16 v[92:95], a[252:255], v[156:159], v[92:95]
ds_read_b64_tr_b16 a[246:247], v2 offset:28160
s_waitcnt lgkmcnt(4)
v_mfma_f32_16x16x32_bf16 v[16:19], a[224:227], v[160:163], v[16:19]
ds_read_b64_tr_b16 a[248:249], v2 offset:25608
v_exp_f32 v37, v37
v_mfma_f32_16x16x32_bf16 v[76:79], a[224:227], v[164:167], v[76:79]
ds_read_b64_tr_b16 a[250:251], v2 offset:28168
v_add_f32 v13, v36, v37
v_mfma_f32_16x16x32_bf16 v[20:23], a[228:231], v[160:163], v[20:23]
ds_read_b64_tr_b16 a[252:253], v2 offset:25616
v_exp_f32 v38, v38
v_mfma_f32_16x16x32_bf16 v[80:83], a[228:231], v[164:167], v[80:83]
ds_read_b64_tr_b16 a[254:255], v2 offset:28176
v_add_f32 v13, v13, v38
v_mfma_f32_16x16x32_bf16 v[24:27], a[232:235], v[160:163], v[24:27]
ds_read_b64_tr_b16 a[224:225], v2 offset:25624
v_exp_f32 v39, v39
v_mfma_f32_16x16x32_bf16 v[84:87], a[232:235], v[164:167], v[84:87]
ds_read_b64_tr_b16 a[226:227], v2 offset:28184
v_add_f32 v13, v13, v39
v_mfma_f32_16x16x32_bf16 v[28:31], a[236:239], v[160:163], v[28:31]
ds_read_b64_tr_b16 a[228:229], v2 offset:25632
v_exp_f32 v40, v40
v_mfma_f32_16x16x32_bf16 v[88:91], a[236:239], v[164:167], v[88:91]
ds_read_b64_tr_b16 a[230:231], v2 offset:28192
s_waitcnt lgkmcnt(4)
v_mfma_f32_16x16x32_bf16 v[32:35], a[240:243], v[160:163], v[32:35]
ds_read_b64_tr_b16 a[232:233], v2 offset:30720
v_add_f32 v13, v13, v40
v_mfma_f32_16x16x32_bf16 v[92:95], a[240:243], v[164:167], v[92:95]
ds_read_b64_tr_b16 a[234:235], v2 offset:33280
v_exp_f32 v41, v41
v_mfma_f32_16x16x32_bf16 v[16:19], a[244:247], v[168:171], v[16:19]
ds_read_b64_tr_b16 a[236:237], v2 offset:30728
v_add_f32 v13, v13, v41
v_mfma_f32_16x16x32_bf16 v[76:79], a[244:247], v[172:175], v[76:79]
ds_read_b64_tr_b16 a[238:239], v2 offset:33288
v_exp_f32 v42, v42
v_mfma_f32_16x16x32_bf16 v[20:23], a[248:251], v[168:171], v[20:23]
ds_read_b64_tr_b16 a[240:241], v2 offset:30736
v_add_f32 v13, v13, v42
v_mfma_f32_16x16x32_bf16 v[80:83], a[248:251], v[172:175], v[80:83]
ds_read_b64_tr_b16 a[242:243], v2 offset:33296
v_exp_f32 v43, v43
v_mfma_f32_16x16x32_bf16 v[24:27], a[252:255], v[168:171], v[24:27]
ds_read_b64_tr_b16 a[244:245], v2 offset:30744
v_add_f32 v13, v13, v43
v_mfma_f32_16x16x32_bf16 v[84:87], a[252:255], v[172:175], v[84:87]
ds_read_b64_tr_b16 a[246:247], v2 offset:33304
s_waitcnt lgkmcnt(4)
v_mfma_f32_16x16x32_bf16 v[28:31], a[224:227], v[168:171], v[28:31]
ds_read_b64_tr_b16 a[248:249], v2 offset:30752
)ASM"
R"ASM(
v_exp_f32 v44, v44
v_mfma_f32_16x16x32_bf16 v[88:91], a[224:227], v[172:175], v[88:91]
ds_read_b64_tr_b16 a[250:251], v2 offset:33312
v_add_f32 v13, v13, v44
v_mfma_f32_16x16x32_bf16 v[32:35], a[228:231], v[168:171], v[32:35]
ds_read_b64_tr_b16 a[252:253], v2 offset:35840
v_exp_f32 v45, v45
v_mfma_f32_16x16x32_bf16 v[92:95], a[228:231], v[172:175], v[92:95]
ds_read_b64_tr_b16 a[254:255], v2 offset:38400
v_add_f32 v13, v13, v45
v_mfma_f32_16x16x32_bf16 v[16:19], a[232:235], v[176:179], v[16:19]
ds_read_b64_tr_b16 a[224:225], v2 offset:35848
v_exp_f32 v46, v46
v_mfma_f32_16x16x32_bf16 v[76:79], a[232:235], v[180:183], v[76:79]
ds_read_b64_tr_b16 a[226:227], v2 offset:38408
v_add_f32 v13, v13, v46
v_mfma_f32_16x16x32_bf16 v[20:23], a[236:239], v[176:179], v[20:23]
ds_read_b64_tr_b16 a[228:229], v2 offset:35856
v_exp_f32 v47, v47
v_mfma_f32_16x16x32_bf16 v[80:83], a[236:239], v[180:183], v[80:83]
ds_read_b64_tr_b16 a[230:231], v2 offset:38416
s_waitcnt lgkmcnt(4)
v_mfma_f32_16x16x32_bf16 v[24:27], a[240:243], v[176:179], v[24:27]
ds_read_b64_tr_b16 a[232:233], v2 offset:35864
v_add_f32 v13, v13, v47
v_mfma_f32_16x16x32_bf16 v[84:87], a[240:243], v[180:183], v[84:87]
ds_read_b64_tr_b16 a[234:235], v2 offset:38424
s_waitcnt vmcnt(9)
v_mfma_f32_16x16x32_bf16 v[28:31], a[244:247], v[176:179], v[28:31]
ds_read_b64_tr_b16 a[236:237], v2 offset:35872
s_barrier
v_mfma_f32_16x16x32_bf16 v[88:91], a[244:247], v[180:183], v[88:91]
ds_read_b64_tr_b16 a[238:239], v2 offset:38432
v_exp_f32 v48, v48
v_mfma_f32_16x16x32_bf16 v[32:35], a[248:251], v[176:179], v[32:35]
ds_read_b64_tr_b16 a[240:241], v2 offset:40960
v_add_f32 v13, v13, v48
v_mfma_f32_16x16x32_bf16 v[92:95], a[248:251], v[180:183], v[92:95]
ds_read_b64_tr_b16 a[242:243], v2 offset:43520
v_exp_f32 v49, v49
v_mfma_f32_16x16x32_bf16 v[16:19], a[252:255], v[184:187], v[16:19]
ds_read_b64_tr_b16 a[244:245], v2 offset:40968
v_add_f32 v13, v13, v49
v_mfma_f32_16x16x32_bf16 v[76:79], a[252:255], v[188:191], v[76:79]
ds_read_b64_tr_b16 a[246:247], v2 offset:43528
s_waitcnt lgkmcnt(4)
v_mfma_f32_16x16x32_bf16 v[20:23], a[224:227], v[184:187], v[20:23]
ds_read_b64_tr_b16 a[248:249], v2 offset:40976
v_exp_f32 v50, v50
v_mfma_f32_16x16x32_bf16 v[80:83], a[224:227], v[188:191], v[80:83]
ds_read_b64_tr_b16 a[250:251], v2 offset:43536
v_add_f32 v13, v13, v50
v_mfma_f32_16x16x32_bf16 v[24:27], a[228:231], v[184:187], v[24:27]
ds_read_b64_tr_b16 a[252:253], v2 offset:40984
v_exp_f32 v51, v51
v_mfma_f32_16x16x32_bf16 v[84:87], a[228:231], v[188:191], v[84:87]
ds_read_b64_tr_b16 a[254:255], v2 offset:43544
v_add_f32 v13, v13, v51
v_mfma_f32_16x16x32_bf16 v[28:31], a[232:235], v[184:187], v[28:31]
ds_read_b64_tr_b16 a[224:225], v2 offset:40992
v_exp_f32 v52, v52
v_mfma_f32_16x16x32_bf16 v[88:91], a[232:235], v[188:191], v[88:91]
ds_read_b64_tr_b16 a[226:227], v2 offset:43552
v_add_f32 v13, v13, v52
v_mfma_f32_16x16x32_bf16 v[32:35], a[236:239], v[184:187], v[32:35]
ds_read_b64_tr_b16 a[228:229], v2 offset:46080
v_exp_f32 v53, v53
v_mfma_f32_16x16x32_bf16 v[92:95], a[236:239], v[188:191], v[92:95]
ds_read_b64_tr_b16 a[230:231], v2 offset:48640
s_waitcnt lgkmcnt(4)
v_mfma_f32_16x16x32_bf16 v[16:19], a[240:243], v[192:195], v[16:19]
ds_read_b64_tr_b16 a[232:233], v2 offset:46088
v_accvgpr_read_b32 v4, a213
v_mfma_f32_16x16x32_bf16 v[76:79], a[240:243], v[196:199], v[76:79]
ds_read_b64_tr_b16 a[234:235], v2 offset:48648
s_mov_b32 s65, 20709376
v_mfma_f32_16x16x32_bf16 v[20:23], a[244:247], v[192:195], v[20:23]
ds_read_b64_tr_b16 a[236:237], v2 offset:46096
buffer_load_dword v5, v4, s[44:47], s65 offen sc1
v_mfma_f32_16x16x32_bf16 v[80:83], a[244:247], v[196:199], v[80:83]
ds_read_b64_tr_b16 a[238:239], v2 offset:48656
v_add_f32 v13, v13, v53
v_mfma_f32_16x16x32_bf16 v[24:27], a[248:251], v[192:195], v[24:27]
ds_read_b64_tr_b16 a[240:241], v2 offset:46104
v_exp_f32 v54, v54
v_mfma_f32_16x16x32_bf16 v[84:87], a[248:251], v[196:199], v[84:87]
ds_read_b64_tr_b16 a[242:243], v2 offset:48664
v_add_f32 v13, v13, v54
v_mfma_f32_16x16x32_bf16 v[28:31], a[252:255], v[192:195], v[28:31]
ds_read_b64_tr_b16 a[244:245], v2 offset:46112
v_exp_f32 v55, v55
v_mfma_f32_16x16x32_bf16 v[88:91], a[252:255], v[196:199], v[88:91]
ds_read_b64_tr_b16 a[246:247], v2 offset:48672
s_waitcnt lgkmcnt(4)
v_mfma_f32_16x16x32_bf16 v[32:35], a[224:227], v[192:195], v[32:35]
ds_read_b64_tr_b16 a[248:249], v2 offset:51200
v_add_f32 v13, v13, v55
v_mfma_f32_16x16x32_bf16 v[92:95], a[224:227], v[196:199], v[92:95]
ds_read_b64_tr_b16 a[250:251], v2 offset:53760
v_rcp_f32 v13, v13
v_mfma_f32_16x16x32_bf16 v[16:19], a[228:231], v[200:203], v[16:19]
ds_read_b64_tr_b16 a[252:253], v2 offset:51208
v_mul_f32 v56, v12, v56
v_mfma_f32_16x16x32_bf16 v[76:79], a[228:231], v[204:207], v[76:79]
ds_read_b64_tr_b16 a[254:255], v2 offset:53768
v_mul_f32 v57, v12, v57
v_mfma_f32_16x16x32_bf16 v[20:23], a[232:235], v[200:203], v[20:23]
ds_read_b64_tr_b16 a[224:225], v2 offset:51216
v_cvt_pk_bf16_f32 v56, v56, v57
v_mfma_f32_16x16x32_bf16 v[80:83], a[232:235], v[204:207], v[80:83]
ds_read_b64_tr_b16 a[226:227], v2 offset:53776
v_mul_f32 v58, v12, v58
v_mfma_f32_16x16x32_bf16 v[24:27], a[236:239], v[200:203], v[24:27]
ds_read_b64_tr_b16 a[228:229], v2 offset:51224
v_mul_f32 v59, v12, v59
v_mfma_f32_16x16x32_bf16 v[84:87], a[236:239], v[204:207], v[84:87]
ds_read_b64_tr_b16 a[230:231], v2 offset:53784
s_waitcnt lgkmcnt(4)
v_mfma_f32_16x16x32_bf16 v[28:31], a[240:243], v[200:203], v[28:31]
ds_read_b64_tr_b16 a[232:233], v2 offset:51232
v_cvt_pk_bf16_f32 v57, v58, v59
v_mfma_f32_16x16x32_bf16 v[88:91], a[240:243], v[204:207], v[88:91]
ds_read_b64_tr_b16 a[234:235], v2 offset:53792
v_mul_f32 v60, v12, v60
v_mfma_f32_16x16x32_bf16 v[32:35], a[244:247], v[200:203], v[32:35]
ds_read_b64_tr_b16 a[236:237], v2 offset:56320
v_mul_f32 v61, v12, v61
v_mfma_f32_16x16x32_bf16 v[92:95], a[244:247], v[204:207], v[92:95]
ds_read_b64_tr_b16 a[238:239], v2 offset:58880
v_cvt_pk_bf16_f32 v58, v60, v61
v_mfma_f32_16x16x32_bf16 v[16:19], a[248:251], v[208:211], v[16:19]
ds_read_b64_tr_b16 a[240:241], v2 offset:56328
v_mul_f32 v62, v12, v62
v_mfma_f32_16x16x32_bf16 v[76:79], a[248:251], v[212:215], v[76:79]
ds_read_b64_tr_b16 a[242:243], v2 offset:58888
v_mul_f32 v63, v12, v63
v_mfma_f32_16x16x32_bf16 v[20:23], a[252:255], v[208:211], v[20:23]
ds_read_b64_tr_b16 a[244:245], v2 offset:56336
v_cvt_pk_bf16_f32 v59, v62, v63
v_mfma_f32_16x16x32_bf16 v[80:83], a[252:255], v[212:215], v[80:83]
ds_read_b64_tr_b16 a[246:247], v2 offset:58896
s_waitcnt lgkmcnt(4)
v_mfma_f32_16x16x32_bf16 v[24:27], a[224:227], v[208:211], v[24:27]
ds_read_b64_tr_b16 a[248:249], v2 offset:56344
v_mul_f32 v64, v12, v64
v_mfma_f32_16x16x32_bf16 v[84:87], a[224:227], v[212:215], v[84:87]
ds_read_b64_tr_b16 a[250:251], v2 offset:58904
s_waitcnt vmcnt(5)
v_mfma_f32_16x16x32_bf16 v[28:31], a[228:231], v[208:211], v[28:31]
ds_read_b64_tr_b16 a[252:253], v2 offset:56352
s_barrier
v_mfma_f32_16x16x32_bf16 v[88:91], a[228:231], v[212:215], v[88:91]
ds_read_b64_tr_b16 a[254:255], v2 offset:58912
s_mul_i32 s68, s74, 98304
v_mfma_f32_16x16x32_bf16 v[32:35], a[232:235], v[208:211], v[32:35]
ds_read_b64_tr_b16 a[224:225], v3
s_waitcnt vmcnt(0)
v_mfma_f32_16x16x32_bf16 v[92:95], a[232:235], v[212:215], v[92:95]
ds_read_b64_tr_b16 a[226:227], v3 offset:2560
v_cmp_ne_u32 vcc, s62, v5
v_mfma_f32_16x16x32_bf16 v[16:19], a[236:239], v[216:219], v[16:19]
ds_read_b64_tr_b16 a[228:229], v3 offset:8
s_cbranch_vccnz .Lpk_1s
.Lpk_1:
v_mfma_f32_16x16x32_bf16 v[76:79], a[236:239], v[220:223], v[76:79]
ds_read_b64_tr_b16 a[230:231], v3 offset:2568
s_waitcnt lgkmcnt(4)
v_mfma_f32_16x16x32_bf16 v[20:23], a[240:243], v[216:219], v[20:23]
ds_read_b64_tr_b16 a[232:233], v3 offset:16
s_add_u32 m0, s61, 20480
v_mfma_f32_16x16x32_bf16 v[80:83], a[240:243], v[220:223], v[80:83]
ds_read_b64_tr_b16 a[234:235], v3 offset:2576
buffer_load_dwordx4 v7, s[32:35], s68 offen lds
v_mfma_f32_16x16x32_bf16 v[24:27], a[244:247], v[216:219], v[24:27]
ds_read_b64_tr_b16 a[236:237], v3 offset:24
buffer_load_dwordx4 v8, s[32:35], s68 offen offset:1056 lds
v_mfma_f32_16x16x32_bf16 v[84:87], a[244:247], v[220:223], v[84:87]
ds_read_b64_tr_b16 a[238:239], v3 offset:2584
buffer_load_dwordx4 v9, s[32:35], s68 offen offset:2112 lds
v_mfma_f32_16x16x32_bf16 v[28:31], a[248:251], v[216:219], v[28:31]
ds_read_b64_tr_b16 a[240:241], v3 offset:32
v_mul_f32 v65, v12, v65
v_mfma_f32_16x16x32_bf16 v[88:91], a[248:251], v[220:223], v[88:91]
ds_read_b64_tr_b16 a[242:243], v3 offset:2592
v_cvt_pk_bf16_f32 v60, v64, v65
v_mfma_f32_16x16x32_bf16 v[32:35], a[252:255], v[216:219], v[32:35]
ds_read_b64_tr_b16 a[244:245], v3 offset:5120
buffer_load_dwordx4 v10, s[32:35], s68 offen offset:3168 lds
v_mfma_f32_16x16x32_bf16 v[92:95], a[252:255], v[220:223], v[92:95]
ds_read_b64_tr_b16 a[246:247], v3 offset:7680
s_waitcnt lgkmcnt(4)
v_mfma_f32_16x16x32_bf16 v[16:19], a[224:227], v[224:227], v[16:19]
ds_read_b64_tr_b16 a[248:249], v3 offset:5128
v_mul_f32 v66, v12, v66
v_mfma_f32_16x16x32_bf16 v[76:79], a[224:227], v[228:231], v[76:79]
ds_read_b64_tr_b16 a[250:251], v3 offset:7688
v_mul_f32 v67, v12, v67
v_mfma_f32_16x16x32_bf16 v[20:23], a[228:231], v[224:227], v[20:23]
ds_read_b64_tr_b16 a[252:253], v3 offset:5136
v_cvt_pk_bf16_f32 v61, v66, v67
v_mfma_f32_16x16x32_bf16 v[80:83], a[228:231], v[228:231], v[80:83]
ds_read_b64_tr_b16 a[254:255], v3 offset:7696
v_mul_f32 v68, v12, v68
v_mfma_f32_16x16x32_bf16 v[24:27], a[232:235], v[224:227], v[24:27]
ds_read_b64_tr_b16 a[224:225], v3 offset:5144
v_mul_f32 v69, v12, v69
v_mfma_f32_16x16x32_bf16 v[84:87], a[232:235], v[228:231], v[84:87]
ds_read_b64_tr_b16 a[226:227], v3 offset:7704
v_cvt_pk_bf16_f32 v62, v68, v69
v_mfma_f32_16x16x32_bf16 v[28:31], a[236:239], v[224:227], v[28:31]
ds_read_b64_tr_b16 a[228:229], v3 offset:5152
v_mul_f32 v70, v12, v70
v_mfma_f32_16x16x32_bf16 v[88:91], a[236:239], v[228:231], v[88:91]
ds_read_b64_tr_b16 a[230:231], v3 offset:7712
s_waitcnt lgkmcnt(4)
v_mfma_f32_16x16x32_bf16 v[32:35], a[240:243], v[224:227], v[32:35]
ds_read_b64_tr_b16 a[232:233], v3 offset:10240
v_mul_f32 v71, v12, v71
v_mfma_f32_16x16x32_bf16 v[92:95], a[240:243], v[228:231], v[92:95]
ds_read_b64_tr_b16 a[234:235], v3 offset:12800
v_cvt_pk_bf16_f32 v63, v70, v71
v_mfma_f32_16x16x32_bf16 v[16:19], a[244:247], v[232:235], v[16:19]
ds_read_b64_tr_b16 a[236:237], v3 offset:10248
v_mul_f32 v72, v12, v72
v_mfma_f32_16x16x32_bf16 v[76:79], a[244:247], v[236:239], v[76:79]
ds_read_b64_tr_b16 a[238:239], v3 offset:12808
v_mul_f32 v73, v12, v73
v_mfma_f32_16x16x32_bf16 v[20:23], a[248:251], v[232:235], v[20:23]
ds_read_b64_tr_b16 a[240:241], v3 offset:10256
v_cvt_pk_bf16_f32 v64, v72, v73
v_mfma_f32_16x16x32_bf16 v[80:83], a[248:251], v[236:239], v[80:83]
ds_read_b64_tr_b16 a[242:243], v3 offset:12816
v_mul_f32 v74, v12, v74
v_mfma_f32_16x16x32_bf16 v[24:27], a[252:255], v[232:235], v[24:27]
ds_read_b64_tr_b16 a[244:245], v3 offset:10264
v_mul_f32 v75, v12, v75
v_mfma_f32_16x16x32_bf16 v[84:87], a[252:255], v[236:239], v[84:87]
ds_read_b64_tr_b16 a[246:247], v3 offset:12824
s_waitcnt lgkmcnt(4)
v_mfma_f32_16x16x32_bf16 v[28:31], a[224:227], v[232:235], v[28:31]
ds_read_b64_tr_b16 a[248:249], v3 offset:10272
v_cvt_pk_bf16_f32 v65, v74, v75
v_mfma_f32_16x16x32_bf16 v[88:91], a[224:227], v[236:239], v[88:91]
ds_read_b64_tr_b16 a[250:251], v3 offset:12832
v_mul_f32 v36, v13, v36
v_mfma_f32_16x16x32_bf16 v[32:35], a[228:231], v[232:235], v[32:35]
ds_read_b64_tr_b16 a[252:253], v3 offset:15360
v_mul_f32 v37, v13, v37
v_mfma_f32_16x16x32_bf16 v[92:95], a[228:231], v[236:239], v[92:95]
ds_read_b64_tr_b16 a[254:255], v3 offset:17920
v_cvt_pk_bf16_f32 v66, v36, v37
v_mfma_f32_16x16x32_bf16 v[16:19], a[232:235], v[240:243], v[16:19]
ds_read_b64_tr_b16 a[224:225], v3 offset:15368
v_mul_f32 v38, v13, v38
v_mfma_f32_16x16x32_bf16 v[76:79], a[232:235], v[244:247], v[76:79]
ds_read_b64_tr_b16 a[226:227], v3 offset:17928
v_mul_f32 v39, v13, v39
v_mfma_f32_16x16x32_bf16 v[20:23], a[236:239], v[240:243], v[20:23]
ds_read_b64_tr_b16 a[228:229], v3 offset:15376
v_cvt_pk_bf16_f32 v67, v38, v39
v_mfma_f32_16x16x32_bf16 v[80:83], a[236:239], v[244:247], v[80:83]
ds_read_b64_tr_b16 a[230:231], v3 offset:17936
s_waitcnt lgkmcnt(4)
v_mfma_f32_16x16x32_bf16 v[24:27], a[240:243], v[240:243], v[24:27]
ds_read_b64_tr_b16 a[232:233], v3 offset:15384
v_mul_f32 v40, v13, v40
v_mfma_f32_16x16x32_bf16 v[84:87], a[240:243], v[244:247], v[84:87]
ds_read_b64_tr_b16 a[234:235], v3 offset:17944
v_mul_f32 v41, v13, v41
v_mfma_f32_16x16x32_bf16 v[28:31], a[244:247], v[240:243], v[28:31]
ds_read_b64_tr_b16 a[236:237], v3 offset:15392
v_cvt_pk_bf16_f32 v68, v40, v41
v_mfma_f32_16x16x32_bf16 v[88:91], a[244:247], v[244:247], v[88:91]
ds_read_b64_tr_b16 a[238:239], v3 offset:17952
v_mul_f32 v42, v13, v42
v_mfma_f32_16x16x32_bf16 v[32:35], a[248:251], v[240:243], v[32:35]
v_mul_f32 v43, v13, v43
v_cvt_pk_bf16_f32 v69, v42, v43
v_mfma_f32_16x16x32_bf16 v[92:95], a[248:251], v[244:247], v[92:95]
v_mul_f32 v44, v13, v44
v_mul_f32 v45, v13, v45
v_mfma_f32_16x16x32_bf16 v[16:19], a[252:255], v[248:251], v[16:19]
v_cvt_pk_bf16_f32 v70, v44, v45
v_mul_f32 v46, v13, v46
v_mfma_f32_16x16x32_bf16 v[76:79], a[252:255], v[252:255], v[76:79]
s_waitcnt lgkmcnt(0)
v_mul_f32 v47, v13, v47
v_mfma_f32_16x16x32_bf16 v[20:23], a[224:227], v[248:251], v[20:23]
v_cvt_pk_bf16_f32 v71, v46, v47
v_mul_f32 v48, v13, v48
v_mfma_f32_16x16x32_bf16 v[80:83], a[224:227], v[252:255], v[80:83]
v_mul_f32 v49, v13, v49
v_cvt_pk_bf16_f32 v72, v48, v49
v_mfma_f32_16x16x32_bf16 v[24:27], a[228:231], v[248:251], v[24:27]
v_mul_f32 v50, v13, v50
v_mul_f32 v51, v13, v51
v_mfma_f32_16x16x32_bf16 v[84:87], a[228:231], v[252:255], v[84:87]
v_cvt_pk_bf16_f32 v73, v50, v51
v_mul_f32 v52, v13, v52
v_mfma_f32_16x16x32_bf16 v[28:31], a[232:235], v[248:251], v[28:31]
v_mul_f32 v53, v13, v53
v_cvt_pk_bf16_f32 v74, v52, v53
v_mfma_f32_16x16x32_bf16 v[88:91], a[232:235], v[252:255], v[88:91]
v_mul_f32 v54, v13, v54
v_mul_f32 v55, v13, v55
v_mfma_f32_16x16x32_bf16 v[32:35], a[236:239], v[248:251], v[32:35]
v_cvt_pk_bf16_f32 v75, v54, v55
ds_write_b128 v6, v[56:59] offset:1280
v_mfma_f32_16x16x32_bf16 v[92:95], a[236:239], v[252:255], v[92:95]
ds_write_b128 v6, v[60:63] offset:1296
ds_write_b128 v6, v[64:67] offset:1312
ds_write_b128 v6, v[68:71] offset:1328
ds_write_b128 v6, v[72:75] offset:1344
v_accvgpr_read_b32 v13, a195
v_add_u32 v13, 81920, v13
v_pk_mul_f32 v[16:17], v[16:17], v[14:15] op_sel:[0,0] op_sel_hi:[1,0]
v_pk_mul_f32 v[18:19], v[18:19], v[14:15] op_sel:[0,0] op_sel_hi:[1,0]
v_pk_mul_f32 v[20:21], v[20:21], v[14:15] op_sel:[0,0] op_sel_hi:[1,0]
v_pk_mul_f32 v[22:23], v[22:23], v[14:15] op_sel:[0,0] op_sel_hi:[1,0]
v_pk_mul_f32 v[24:25], v[24:25], v[14:15] op_sel:[0,0] op_sel_hi:[1,0]
v_pk_mul_f32 v[26:27], v[26:27], v[14:15] op_sel:[0,0] op_sel_hi:[1,0]
v_pk_mul_f32 v[28:29], v[28:29], v[14:15] op_sel:[0,0] op_sel_hi:[1,0]
v_pk_mul_f32 v[30:31], v[30:31], v[14:15] op_sel:[0,0] op_sel_hi:[1,0]
v_pk_mul_f32 v[32:33], v[32:33], v[14:15] op_sel:[0,0] op_sel_hi:[1,0]
v_pk_mul_f32 v[34:35], v[34:35], v[14:15] op_sel:[0,0] op_sel_hi:[1,0]
v_cvt_pk_bf16_f32 v16, v16, v17
v_cvt_pk_bf16_f32 v17, v18, v19
v_cvt_pk_bf16_f32 v18, v20, v21
v_cvt_pk_bf16_f32 v19, v22, v23
v_cvt_pk_bf16_f32 v20, v24, v25
v_cvt_pk_bf16_f32 v21, v26, v27
v_cvt_pk_bf16_f32 v22, v28, v29
v_cvt_pk_bf16_f32 v23, v30, v31
v_cvt_pk_bf16_f32 v24, v32, v33
v_cvt_pk_bf16_f32 v25, v34, v35
ds_write_b64 v13, v[16:17]
ds_write_b64 v13, v[18:19] offset:8
ds_write_b64 v13, v[20:21] offset:16
ds_write_b64 v13, v[22:23] offset:24
ds_write_b64 v13, v[24:25] offset:32
v_pk_mul_f32 v[76:77], v[76:77], v[14:15] op_sel:[0,1] op_sel_hi:[1,1]
v_pk_mul_f32 v[78:79], v[78:79], v[14:15] op_sel:[0,1] op_sel_hi:[1,1]
v_pk_mul_f32 v[80:81], v[80:81], v[14:15] op_sel:[0,1] op_sel_hi:[1,1]
v_pk_mul_f32 v[82:83], v[82:83], v[14:15] op_sel:[0,1] op_sel_hi:[1,1]
v_pk_mul_f32 v[84:85], v[84:85], v[14:15] op_sel:[0,1] op_sel_hi:[1,1]
v_pk_mul_f32 v[86:87], v[86:87], v[14:15] op_sel:[0,1] op_sel_hi:[1,1]
v_pk_mul_f32 v[88:89], v[88:89], v[14:15] op_sel:[0,1] op_sel_hi:[1,1]
v_pk_mul_f32 v[90:91], v[90:91], v[14:15] op_sel:[0,1] op_sel_hi:[1,1]
v_pk_mul_f32 v[92:93], v[92:93], v[14:15] op_sel:[0,1] op_sel_hi:[1,1]
v_pk_mul_f32 v[94:95], v[94:95], v[14:15] op_sel:[0,1] op_sel_hi:[1,1]
v_cvt_pk_bf16_f32 v76, v76, v77
v_cvt_pk_bf16_f32 v77, v78, v79
v_cvt_pk_bf16_f32 v78, v80, v81
v_cvt_pk_bf16_f32 v79, v82, v83
v_cvt_pk_bf16_f32 v80, v84, v85
v_cvt_pk_bf16_f32 v81, v86, v87
v_cvt_pk_bf16_f32 v82, v88, v89
v_cvt_pk_bf16_f32 v83, v90, v91
v_cvt_pk_bf16_f32 v84, v92, v93
v_cvt_pk_bf16_f32 v85, v94, v95
ds_write_b64 v13, v[76:77] offset:80
ds_write_b64 v13, v[78:79] offset:88
ds_write_b64 v13, v[80:81] offset:96
ds_write_b64 v13, v[82:83] offset:104
ds_write_b64 v13, v[84:85] offset:112
v_accvgpr_read_b32 v4, a196
v_add_u32 v5, 61440, v4
v_add_u32 v4, 40960, v4
v_accvgpr_read_b32 v6, a197
v_add_u32 v6, 81920, v6
v_mov_b32 v12, 1065369472
s_mov_b32 s4, 1048592
s_mov_b32 s5, 1048592
s_waitcnt lgkmcnt(0)
s_barrier
ds_read_b64_tr_b16 a[48:49], v6
ds_read_b64_tr_b16 a[50:51], v6 offset:80
ds_read_b64_tr_b16 a[0:1], v4
ds_read_b64_tr_b16 a[2:3], v4 offset:1280
ds_read_b64_tr_b16 a[4:5], v4 offset:32
ds_read_b64_tr_b16 a[6:7], v4 offset:1312
ds_read_b64_tr_b16 a[8:9], v4 offset:64
ds_read_b64_tr_b16 a[10:11], v4 offset:1344
ds_read_b64_tr_b16 a[52:53], v6 offset:1280
ds_read_b64_tr_b16 a[54:55], v6 offset:1360
ds_read_b64_tr_b16 a[12:13], v4 offset:2560
ds_read_b64_tr_b16 a[14:15], v4 offset:3840
ds_read_b64_tr_b16 a[16:17], v4 offset:2592
ds_read_b64_tr_b16 a[18:19], v4 offset:3872
s_waitcnt lgkmcnt(10)
v_mfma_f32_16x16x32_bf16 v[16:19], a[0:3], a[48:51], 0
ds_read_b64_tr_b16 a[20:21], v4 offset:2624
ds_read_b64_tr_b16 a[22:23], v4 offset:3904
ds_read_b64_tr_b16 a[56:57], v6 offset:2560
s_waitcnt lgkmcnt(11)
v_mfma_f32_16x16x32_bf16 v[20:23], a[4:7], a[48:51], 0
ds_read_b64_tr_b16 a[58:59], v6 offset:2640
ds_read_b64_tr_b16 a[24:25], v5
ds_read_b64_tr_b16 a[26:27], v5 offset:1280
s_waitcnt lgkmcnt(12)
)ASM"
R"ASM(
v_mfma_f32_16x16x32_bf16 v[24:27], a[8:11], a[48:51], 0
ds_read_b64_tr_b16 a[28:29], v5 offset:32
ds_read_b64_tr_b16 a[30:31], v5 offset:1312
ds_read_b64_tr_b16 a[32:33], v5 offset:64
s_waitcnt lgkmcnt(11)
v_mfma_f32_16x16x32_bf16 v[16:19], a[12:15], a[52:55], v[16:19]
ds_read_b64_tr_b16 a[34:35], v5 offset:1344
ds_read_b64_tr_b16 a[60:61], v6 offset:3840
ds_read_b64_tr_b16 a[62:63], v6 offset:3920
s_waitcnt lgkmcnt(12)
v_mfma_f32_16x16x32_bf16 v[20:23], a[16:19], a[52:55], v[20:23]
ds_read_b64_tr_b16 a[36:37], v5 offset:2560
ds_read_b64_tr_b16 a[38:39], v5 offset:3840
ds_read_b64_tr_b16 a[40:41], v5 offset:2592
s_waitcnt lgkmcnt(13)
v_mfma_f32_16x16x32_bf16 v[24:27], a[20:23], a[52:55], v[24:27]
ds_read_b64_tr_b16 a[42:43], v5 offset:3872
ds_read_b64_tr_b16 a[44:45], v5 offset:2624
s_waitcnt lgkmcnt(14)
ds_read_b64_tr_b16 a[46:47], v5 offset:3904
s_waitcnt lgkmcnt(12)
v_mfma_f32_16x16x32_bf16 v[16:19], a[24:27], a[56:59], v[16:19]
ds_read_b64_tr_b16 v[40:41], v6 offset:32
ds_read_b64_tr_b16 v[42:43], v6 offset:112
ds_read_b64_tr_b16 v[44:45], v6 offset:1312
s_waitcnt lgkmcnt(13)
v_mfma_f32_16x16x32_bf16 v[20:23], a[28:31], a[56:59], v[20:23]
ds_read_b64_tr_b16 v[46:47], v6 offset:1392
ds_read_b64_tr_b16 v[48:49], v6 offset:2592
s_waitcnt lgkmcnt(14)
ds_read_b64_tr_b16 v[50:51], v6 offset:2672
s_waitcnt lgkmcnt(14)
v_mfma_f32_16x16x32_bf16 v[24:27], a[32:35], a[56:59], v[24:27]
ds_read_b64_tr_b16 v[52:53], v6 offset:3872
s_waitcnt lgkmcnt(14)
ds_read_b64_tr_b16 v[54:55], v6 offset:3952
s_waitcnt lgkmcnt(12)
v_mfma_f32_16x16x32_bf16 v[16:19], a[36:39], a[60:63], v[16:19]
s_waitcnt lgkmcnt(10)
v_mfma_f32_16x16x32_bf16 v[20:23], a[40:43], a[60:63], v[20:23]
s_waitcnt lgkmcnt(8)
v_mfma_f32_16x16x32_bf16 v[24:27], a[44:47], a[60:63], v[24:27]
s_waitcnt lgkmcnt(6)
v_cndmask_b32 v40, v40, v12, s[4:5]
v_cndmask_b32 v41, v41, v12, s[4:5]
v_cndmask_b32 v42, v42, v12, s[4:5]
v_cndmask_b32 v43, v43, v12, s[4:5]
s_nop 1
v_mfma_f32_16x16x32_bf16 v[28:31], a[0:3], v[40:43], 0
v_mfma_f32_16x16x32_bf16 v[32:35], a[4:7], v[40:43], 0
s_waitcnt lgkmcnt(4)
v_cndmask_b32 v44, v44, v12, s[4:5]
v_cndmask_b32 v45, v45, v12, s[4:5]
v_cndmask_b32 v46, v46, v12, s[4:5]
v_cndmask_b32 v47, v47, v12, s[4:5]
v_mfma_f32_16x16x32_bf16 v[36:39], a[8:11], v[40:43], 0
v_accvgpr_read_b32 v68, a198
v_accvgpr_read_b32 v69, a199
v_mfma_f32_16x16x32_bf16 v[28:31], a[12:15], v[44:47], v[28:31]
v_accvgpr_read_b32 v70, a200
v_cvt_pk_bf16_f32 v56, v16, v17
v_mfma_f32_16x16x32_bf16 v[32:35], a[16:19], v[44:47], v[32:35]
s_waitcnt lgkmcnt(2)
v_cndmask_b32 v48, v48, v12, s[4:5]
v_cndmask_b32 v49, v49, v12, s[4:5]
v_cndmask_b32 v50, v50, v12, s[4:5]
v_cndmask_b32 v51, v51, v12, s[4:5]
v_mfma_f32_16x16x32_bf16 v[36:39], a[20:23], v[44:47], v[36:39]
v_cvt_pk_bf16_f32 v57, v18, v19
v_cvt_pk_bf16_f32 v58, v20, v21
v_mfma_f32_16x16x32_bf16 v[28:31], a[24:27], v[48:51], v[28:31]
v_cvt_pk_bf16_f32 v59, v22, v23
v_cvt_pk_bf16_f32 v60, v24, v25
v_mfma_f32_16x16x32_bf16 v[32:35], a[28:31], v[48:51], v[32:35]
s_waitcnt lgkmcnt(0)
v_cndmask_b32 v52, v52, v12, s[4:5]
v_cndmask_b32 v53, v53, v12, s[4:5]
v_cndmask_b32 v54, v54, v12, s[4:5]
v_cndmask_b32 v55, v55, v12, s[4:5]
v_mfma_f32_16x16x32_bf16 v[36:39], a[32:35], v[48:51], v[36:39]
v_cvt_pk_bf16_f32 v61, v26, v27
buffer_store_dwordx4 v[56:59], v0, s[44:47], s16 offen sc1
v_mfma_f32_16x16x32_bf16 v[28:31], a[36:39], v[52:55], v[28:31]
ds_write_b64 v69, v[60:61] offset:320
v_accvgpr_read_b32 v69, a205
v_mfma_f32_16x16x32_bf16 v[32:35], a[40:43], v[52:55], v[32:35]
v_accvgpr_read_b32 v71, a201
v_mfma_f32_16x16x32_bf16 v[36:39], a[44:47], v[52:55], v[36:39]
v_accvgpr_read_b32 v4, a189
v_add_u32 v5, 61440, v4
v_add_u32 v6, 122880, v4
v_cvt_pk_bf16_f32 v62, v28, v29
v_cvt_pk_bf16_f32 v63, v30, v31
s_nop 0
v_cvt_pk_bf16_f32 v64, v32, v33
v_cvt_pk_bf16_f32 v65, v34, v35
v_cvt_pk_bf16_f32 v66, v36, v37
v_cvt_pk_bf16_f32 v67, v38, v39
ds_write_b128 v68, v[62:65]
ds_write_b64 v70, v[66:67] offset:576
ds_read_b128 v[40:43], v69
v_mov_b32 v72, s62
v_lshrrev_b32 v73, 2, v0
s_waitcnt lgkmcnt(0)
buffer_store_dwordx4 v[40:43], v71, s[44:47], s16 offen sc1
s_barrier
ds_read_b64_tr_b16 a[224:225], v4
ds_read_b64_tr_b16 a[226:227], v4 offset:512
ds_read_b64_tr_b16 a[228:229], v4 offset:32
ds_read_b64_tr_b16 a[230:231], v4 offset:544
ds_read_b64_tr_b16 a[232:233], v4 offset:64
ds_read_b64_tr_b16 a[234:235], v4 offset:576
ds_read_b64_tr_b16 a[236:237], v4 offset:96
ds_read_b64_tr_b16 a[238:239], v4 offset:608
ds_read_b64_tr_b16 a[240:241], v4 offset:5120
ds_read_b64_tr_b16 a[242:243], v4 offset:5632
ds_read_b64_tr_b16 a[244:245], v4 offset:5152
ds_read_b64_tr_b16 a[246:247], v4 offset:5664
s_waitcnt lgkmcnt(8)
v_mfma_f32_16x16x32_bf16 v[16:19], a[224:227], v[96:99], 0
ds_read_b64_tr_b16 a[248:249], v4 offset:5184
s_mul_i32 s68, s75, 98304
v_mfma_f32_16x16x32_bf16 v[76:79], a[224:227], v[100:103], 0
ds_read_b64_tr_b16 a[250:251], v4 offset:5696
s_add_u32 m0, s61, 102400
v_mfma_f32_16x16x32_bf16 v[20:23], a[228:231], v[96:99], 0
ds_read_b64_tr_b16 a[252:253], v4 offset:5216
buffer_load_dwordx4 v7, s[32:35], s68 offen lds
v_mfma_f32_16x16x32_bf16 v[80:83], a[228:231], v[100:103], 0
ds_read_b64_tr_b16 a[254:255], v4 offset:5728
s_waitcnt lgkmcnt(8)
v_mfma_f32_16x16x32_bf16 v[24:27], a[232:235], v[96:99], 0
ds_read_b64_tr_b16 a[224:225], v4 offset:10240
buffer_load_dwordx4 v8, s[32:35], s68 offen offset:1056 lds
v_mfma_f32_16x16x32_bf16 v[84:87], a[232:235], v[100:103], 0
ds_read_b64_tr_b16 a[226:227], v4 offset:10752
buffer_load_dwordx4 v9, s[32:35], s68 offen offset:2112 lds
v_mfma_f32_16x16x32_bf16 v[28:31], a[236:239], v[96:99], 0
ds_read_b64_tr_b16 a[228:229], v4 offset:10272
v_mfma_f32_16x16x32_bf16 v[88:91], a[236:239], v[100:103], 0
ds_read_b64_tr_b16 a[230:231], v4 offset:10784
s_waitcnt lgkmcnt(4)
v_mfma_f32_16x16x32_bf16 v[16:19], a[240:243], v[104:107], v[16:19]
ds_read_b64_tr_b16 a[232:233], v4 offset:10304
buffer_load_dwordx4 v10, s[32:35], s68 offen offset:3168 lds
v_mfma_f32_16x16x32_bf16 v[76:79], a[240:243], v[108:111], v[76:79]
ds_read_b64_tr_b16 a[234:235], v4 offset:10816
v_mfma_f32_16x16x32_bf16 v[20:23], a[244:247], v[104:107], v[20:23]
ds_read_b64_tr_b16 a[236:237], v4 offset:10336
v_mfma_f32_16x16x32_bf16 v[80:83], a[244:247], v[108:111], v[80:83]
ds_read_b64_tr_b16 a[238:239], v4 offset:10848
v_mfma_f32_16x16x32_bf16 v[24:27], a[248:251], v[104:107], v[24:27]
ds_read_b64_tr_b16 a[240:241], v4 offset:15360
s_mul_i32 s68, s76, 98304
v_mfma_f32_16x16x32_bf16 v[84:87], a[248:251], v[108:111], v[84:87]
ds_read_b64_tr_b16 a[242:243], v4 offset:15872
s_add_u32 m0, s61, 122880
v_mfma_f32_16x16x32_bf16 v[28:31], a[252:255], v[104:107], v[28:31]
ds_read_b64_tr_b16 a[244:245], v4 offset:15392
buffer_load_dwordx4 v7, s[32:35], s68 offen lds
v_mfma_f32_16x16x32_bf16 v[88:91], a[252:255], v[108:111], v[88:91]
ds_read_b64_tr_b16 a[246:247], v4 offset:15904
s_waitcnt lgkmcnt(4)
v_mfma_f32_16x16x32_bf16 v[16:19], a[224:227], v[112:115], v[16:19]
ds_read_b64_tr_b16 a[248:249], v4 offset:15424
buffer_load_dwordx4 v8, s[32:35], s68 offen offset:1056 lds
v_mfma_f32_16x16x32_bf16 v[76:79], a[224:227], v[116:119], v[76:79]
ds_read_b64_tr_b16 a[250:251], v4 offset:15936
s_waitcnt vmcnt(8)
v_mfma_f32_16x16x32_bf16 v[20:23], a[228:231], v[112:115], v[20:23]
ds_read_b64_tr_b16 a[252:253], v4 offset:15456
s_barrier
v_mfma_f32_16x16x32_bf16 v[80:83], a[228:231], v[116:119], v[80:83]
ds_read_b64_tr_b16 a[254:255], v4 offset:15968
v_mfma_f32_16x16x32_bf16 v[24:27], a[232:235], v[112:115], v[24:27]
ds_read_b64_tr_b16 a[224:225], v4 offset:20480
buffer_load_dwordx4 v9, s[32:35], s68 offen offset:2112 lds
v_mfma_f32_16x16x32_bf16 v[84:87], a[232:235], v[116:119], v[84:87]
ds_read_b64_tr_b16 a[226:227], v4 offset:20992
v_mfma_f32_16x16x32_bf16 v[28:31], a[236:239], v[112:115], v[28:31]
ds_read_b64_tr_b16 a[228:229], v4 offset:20512
v_mfma_f32_16x16x32_bf16 v[88:91], a[236:239], v[116:119], v[88:91]
ds_read_b64_tr_b16 a[230:231], v4 offset:21024
s_waitcnt lgkmcnt(4)
v_mfma_f32_16x16x32_bf16 v[16:19], a[240:243], v[120:123], v[16:19]
ds_read_b64_tr_b16 a[232:233], v4 offset:20544
buffer_load_dwordx4 v10, s[32:35], s68 offen offset:3168 lds
v_mfma_f32_16x16x32_bf16 v[76:79], a[240:243], v[124:127], v[76:79]
ds_read_b64_tr_b16 a[234:235], v4 offset:21056
v_mfma_f32_16x16x32_bf16 v[20:23], a[244:247], v[120:123], v[20:23]
ds_read_b64_tr_b16 a[236:237], v4 offset:20576
v_mfma_f32_16x16x32_bf16 v[80:83], a[244:247], v[124:127], v[80:83]
ds_read_b64_tr_b16 a[238:239], v4 offset:21088
v_mfma_f32_16x16x32_bf16 v[24:27], a[248:251], v[120:123], v[24:27]
ds_read_b64_tr_b16 a[240:241], v4 offset:25600
s_add_u32 s65, s64, 0
v_mfma_f32_16x16x32_bf16 v[84:87], a[248:251], v[124:127], v[84:87]
ds_read_b64_tr_b16 a[242:243], v4 offset:26112
s_waitcnt vmcnt(8)
v_mfma_f32_16x16x32_bf16 v[28:31], a[252:255], v[120:123], v[28:31]
ds_read_b64_tr_b16 a[244:245], v4 offset:25632
buffer_store_dword v72, v73, s[44:47], s65 offen sc1
v_mfma_f32_16x16x32_bf16 v[88:91], a[252:255], v[124:127], v[88:91]
ds_read_b64_tr_b16 a[246:247], v4 offset:26144
s_waitcnt lgkmcnt(4)
v_mfma_f32_16x16x32_bf16 v[16:19], a[224:227], v[128:131], v[16:19]
ds_read_b64_tr_b16 a[248:249], v4 offset:25664
s_mul_i32 s68, s77, 98304
v_mfma_f32_16x16x32_bf16 v[76:79], a[224:227], v[132:135], v[76:79]
ds_read_b64_tr_b16 a[250:251], v4 offset:26176
s_add_u32 m0, s61, 40960
v_mfma_f32_16x16x32_bf16 v[20:23], a[228:231], v[128:131], v[20:23]
ds_read_b64_tr_b16 a[252:253], v4 offset:25696
buffer_load_dwordx4 v7, s[32:35], s68 offen lds
v_mfma_f32_16x16x32_bf16 v[80:83], a[228:231], v[132:135], v[80:83]
ds_read_b64_tr_b16 a[254:255], v4 offset:26208
buffer_load_dwordx4 v8, s[32:35], s68 offen offset:1056 lds
v_mfma_f32_16x16x32_bf16 v[24:27], a[232:235], v[128:131], v[24:27]
ds_read_b64_tr_b16 a[224:225], v4 offset:30720
buffer_load_dwordx4 v9, s[32:35], s68 offen offset:2112 lds
v_mfma_f32_16x16x32_bf16 v[84:87], a[232:235], v[132:135], v[84:87]
ds_read_b64_tr_b16 a[226:227], v4 offset:31232
v_mfma_f32_16x16x32_bf16 v[28:31], a[236:239], v[128:131], v[28:31]
ds_read_b64_tr_b16 a[228:229], v4 offset:30752
v_mfma_f32_16x16x32_bf16 v[88:91], a[236:239], v[132:135], v[88:91]
ds_read_b64_tr_b16 a[230:231], v4 offset:31264
s_waitcnt lgkmcnt(4)
v_mfma_f32_16x16x32_bf16 v[16:19], a[240:243], v[136:139], v[16:19]
ds_read_b64_tr_b16 a[232:233], v4 offset:30784
buffer_load_dwordx4 v10, s[32:35], s68 offen offset:3168 lds
v_mfma_f32_16x16x32_bf16 v[76:79], a[240:243], v[140:143], v[76:79]
ds_read_b64_tr_b16 a[234:235], v4 offset:31296
v_mfma_f32_16x16x32_bf16 v[20:23], a[244:247], v[136:139], v[20:23]
ds_read_b64_tr_b16 a[236:237], v4 offset:30816
v_mfma_f32_16x16x32_bf16 v[80:83], a[244:247], v[140:143], v[80:83]
ds_read_b64_tr_b16 a[238:239], v4 offset:31328
v_mfma_f32_16x16x32_bf16 v[24:27], a[248:251], v[136:139], v[24:27]
ds_read_b64_tr_b16 a[240:241], v4 offset:35840
s_add_u32 s68, s23, 21506048
v_mfma_f32_16x16x32_bf16 v[84:87], a[248:251], v[140:143], v[84:87]
ds_read_b64_tr_b16 a[242:243], v4 offset:36352
s_add_u32 m0, s61, 61440
v_mfma_f32_16x16x32_bf16 v[28:31], a[252:255], v[136:139], v[28:31]
ds_read_b64_tr_b16 a[244:245], v4 offset:35872
buffer_load_dwordx4 v0, s[44:47], s68 offen sc1 lds
v_mfma_f32_16x16x32_bf16 v[88:91], a[252:255], v[140:143], v[88:91]
ds_read_b64_tr_b16 a[246:247], v4 offset:36384
s_waitcnt lgkmcnt(4)
v_mfma_f32_16x16x32_bf16 v[16:19], a[224:227], v[144:147], v[16:19]
ds_read_b64_tr_b16 a[248:249], v4 offset:35904
buffer_load_dwordx4 v0, s[44:47], s68 offen offset:1024 sc1 lds
v_mfma_f32_16x16x32_bf16 v[76:79], a[224:227], v[148:151], v[76:79]
ds_read_b64_tr_b16 a[250:251], v4 offset:36416
s_waitcnt vmcnt(11)
v_mfma_f32_16x16x32_bf16 v[20:23], a[228:231], v[144:147], v[20:23]
ds_read_b64_tr_b16 a[252:253], v4 offset:35936
s_barrier
v_mfma_f32_16x16x32_bf16 v[80:83], a[228:231], v[148:151], v[80:83]
ds_read_b64_tr_b16 a[254:255], v4 offset:36448
v_mfma_f32_16x16x32_bf16 v[24:27], a[232:235], v[144:147], v[24:27]
ds_read_b64_tr_b16 a[224:225], v5 offset:40960
buffer_load_dwordx4 v0, s[44:47], s68 offen offset:2048 sc1 lds
v_mfma_f32_16x16x32_bf16 v[84:87], a[232:235], v[148:151], v[84:87]
ds_read_b64_tr_b16 a[226:227], v5 offset:41472
v_mfma_f32_16x16x32_bf16 v[28:31], a[236:239], v[144:147], v[28:31]
ds_read_b64_tr_b16 a[228:229], v5 offset:40992
v_mfma_f32_16x16x32_bf16 v[88:91], a[236:239], v[148:151], v[88:91]
ds_read_b64_tr_b16 a[230:231], v5 offset:41504
s_waitcnt lgkmcnt(4)
v_mfma_f32_16x16x32_bf16 v[16:19], a[240:243], v[152:155], v[16:19]
ds_read_b64_tr_b16 a[232:233], v5 offset:41024
buffer_load_dwordx4 v0, s[44:47], s68 offen offset:3072 sc1 lds
v_mfma_f32_16x16x32_bf16 v[76:79], a[240:243], v[156:159], v[76:79]
ds_read_b64_tr_b16 a[234:235], v5 offset:41536
v_mfma_f32_16x16x32_bf16 v[20:23], a[244:247], v[152:155], v[20:23]
ds_read_b64_tr_b16 a[236:237], v5 offset:41056
v_mfma_f32_16x16x32_bf16 v[80:83], a[244:247], v[156:159], v[80:83]
ds_read_b64_tr_b16 a[238:239], v5 offset:41568
s_add_u32 s68, s23, 21510144
v_mfma_f32_16x16x32_bf16 v[24:27], a[248:251], v[152:155], v[24:27]
ds_read_b64_tr_b16 a[240:241], v5 offset:46080
s_add_u32 m0, s61, 65536
v_mfma_f32_16x16x32_bf16 v[84:87], a[248:251], v[156:159], v[84:87]
ds_read_b64_tr_b16 a[242:243], v5 offset:46592
buffer_load_dwordx4 v0, s[44:47], s68 offen sc1 lds
v_mfma_f32_16x16x32_bf16 v[28:31], a[252:255], v[152:155], v[28:31]
ds_read_b64_tr_b16 a[244:245], v5 offset:46112
v_mfma_f32_16x16x32_bf16 v[88:91], a[252:255], v[156:159], v[88:91]
ds_read_b64_tr_b16 a[246:247], v5 offset:46624
s_waitcnt lgkmcnt(4)
v_mfma_f32_16x16x32_bf16 v[16:19], a[224:227], v[160:163], v[16:19]
ds_read_b64_tr_b16 a[248:249], v5 offset:46144
s_add_u32 s68, s22, 21506048
v_mfma_f32_16x16x32_bf16 v[76:79], a[224:227], v[164:167], v[76:79]
ds_read_b64_tr_b16 a[250:251], v5 offset:46656
s_add_u32 m0, s61, 81920
v_mfma_f32_16x16x32_bf16 v[20:23], a[228:231], v[160:163], v[20:23]
ds_read_b64_tr_b16 a[252:253], v5 offset:46176
buffer_load_dwordx4 v0, s[44:47], s68 offen sc1 lds
v_mfma_f32_16x16x32_bf16 v[80:83], a[228:231], v[164:167], v[80:83]
ds_read_b64_tr_b16 a[254:255], v5 offset:46688
buffer_load_dwordx4 v0, s[44:47], s68 offen offset:1024 sc1 lds
v_mfma_f32_16x16x32_bf16 v[24:27], a[232:235], v[160:163], v[24:27]
ds_read_b64_tr_b16 a[224:225], v5 offset:51200
v_mfma_f32_16x16x32_bf16 v[84:87], a[232:235], v[164:167], v[84:87]
ds_read_b64_tr_b16 a[226:227], v5 offset:51712
v_mfma_f32_16x16x32_bf16 v[28:31], a[236:239], v[160:163], v[28:31]
ds_read_b64_tr_b16 a[228:229], v5 offset:51232
v_mfma_f32_16x16x32_bf16 v[88:91], a[236:239], v[164:167], v[88:91]
ds_read_b64_tr_b16 a[230:231], v5 offset:51744
s_waitcnt lgkmcnt(4)
v_mfma_f32_16x16x32_bf16 v[16:19], a[240:243], v[168:171], v[16:19]
ds_read_b64_tr_b16 a[232:233], v5 offset:51264
buffer_load_dwordx4 v0, s[44:47], s68 offen offset:2048 sc1 lds
v_mfma_f32_16x16x32_bf16 v[76:79], a[240:243], v[172:175], v[76:79]
ds_read_b64_tr_b16 a[234:235], v5 offset:51776
v_mfma_f32_16x16x32_bf16 v[20:23], a[244:247], v[168:171], v[20:23]
ds_read_b64_tr_b16 a[236:237], v5 offset:51296
v_mfma_f32_16x16x32_bf16 v[80:83], a[244:247], v[172:175], v[80:83]
ds_read_b64_tr_b16 a[238:239], v5 offset:51808
buffer_load_dwordx4 v0, s[44:47], s68 offen offset:3072 sc1 lds
v_mfma_f32_16x16x32_bf16 v[24:27], a[248:251], v[168:171], v[24:27]
ds_read_b64_tr_b16 a[240:241], v5 offset:56320
v_mfma_f32_16x16x32_bf16 v[84:87], a[248:251], v[172:175], v[84:87]
ds_read_b64_tr_b16 a[242:243], v5 offset:56832
v_mfma_f32_16x16x32_bf16 v[28:31], a[252:255], v[168:171], v[28:31]
ds_read_b64_tr_b16 a[244:245], v5 offset:56352
v_mfma_f32_16x16x32_bf16 v[88:91], a[252:255], v[172:175], v[88:91]
ds_read_b64_tr_b16 a[246:247], v5 offset:56864
s_waitcnt lgkmcnt(4)
v_mfma_f32_16x16x32_bf16 v[16:19], a[224:227], v[176:179], v[16:19]
ds_read_b64_tr_b16 a[248:249], v5 offset:56384
s_add_u32 s68, s22, 21510144
v_mfma_f32_16x16x32_bf16 v[76:79], a[224:227], v[180:183], v[76:79]
ds_read_b64_tr_b16 a[250:251], v5 offset:56896
s_waitcnt vmcnt(14)
v_mfma_f32_16x16x32_bf16 v[20:23], a[228:231], v[176:179], v[20:23]
ds_read_b64_tr_b16 a[252:253], v5 offset:56416
s_barrier
v_mfma_f32_16x16x32_bf16 v[80:83], a[228:231], v[180:183], v[80:83]
ds_read_b64_tr_b16 a[254:255], v5 offset:56928
s_add_u32 m0, s61, 86016
v_mfma_f32_16x16x32_bf16 v[24:27], a[232:235], v[176:179], v[24:27]
ds_read_b64_tr_b16 a[224:225], v6
buffer_load_dwordx4 v0, s[44:47], s68 offen sc1 lds
v_mfma_f32_16x16x32_bf16 v[84:87], a[232:235], v[180:183], v[84:87]
ds_read_b64_tr_b16 a[226:227], v6 offset:512
s_add_u32 s68, s23, 21516288
v_mfma_f32_16x16x32_bf16 v[28:31], a[236:239], v[176:179], v[28:31]
ds_read_b64_tr_b16 a[228:229], v6 offset:32
s_add_u32 m0, s61, 0
v_mfma_f32_16x16x32_bf16 v[88:91], a[236:239], v[180:183], v[88:91]
ds_read_b64_tr_b16 a[230:231], v6 offset:544
s_waitcnt lgkmcnt(4)
v_mfma_f32_16x16x32_bf16 v[16:19], a[240:243], v[184:187], v[16:19]
ds_read_b64_tr_b16 a[232:233], v6 offset:64
v_accvgpr_read_b32 v74, a209
v_mfma_f32_16x16x32_bf16 v[76:79], a[240:243], v[188:191], v[76:79]
ds_read_b64_tr_b16 a[234:235], v6 offset:576
s_mov_b32 s65, 20447232
v_mfma_f32_16x16x32_bf16 v[20:23], a[244:247], v[184:187], v[20:23]
ds_read_b64_tr_b16 a[236:237], v6 offset:96
buffer_load_dword v75, v74, s[44:47], s65 offen sc1
v_mfma_f32_16x16x32_bf16 v[80:83], a[244:247], v[188:191], v[80:83]
ds_read_b64_tr_b16 a[238:239], v6 offset:608
buffer_load_dwordx4 v0, s[44:47], s68 offen sc1 lds
v_mfma_f32_16x16x32_bf16 v[24:27], a[248:251], v[184:187], v[24:27]
ds_read_b64_tr_b16 a[240:241], v6 offset:5120
buffer_load_dwordx4 v0, s[44:47], s68 offen offset:1024 sc1 lds
v_mfma_f32_16x16x32_bf16 v[84:87], a[248:251], v[188:191], v[84:87]
ds_read_b64_tr_b16 a[242:243], v6 offset:5632
buffer_load_dwordx4 v0, s[44:47], s68 offen offset:2048 sc1 lds
v_mfma_f32_16x16x32_bf16 v[28:31], a[252:255], v[184:187], v[28:31]
ds_read_b64_tr_b16 a[244:245], v6 offset:5152
v_mfma_f32_16x16x32_bf16 v[88:91], a[252:255], v[188:191], v[88:91]
ds_read_b64_tr_b16 a[246:247], v6 offset:5664
s_waitcnt lgkmcnt(4)
v_mfma_f32_16x16x32_bf16 v[16:19], a[224:227], v[192:195], v[16:19]
ds_read_b64_tr_b16 a[248:249], v6 offset:5184
buffer_load_dwordx4 v0, s[44:47], s68 offen offset:3072 sc1 lds
v_mfma_f32_16x16x32_bf16 v[76:79], a[224:227], v[196:199], v[76:79]
ds_read_b64_tr_b16 a[250:251], v6 offset:5696
v_mfma_f32_16x16x32_bf16 v[20:23], a[228:231], v[192:195], v[20:23]
ds_read_b64_tr_b16 a[252:253], v6 offset:5216
v_mfma_f32_16x16x32_bf16 v[80:83], a[228:231], v[196:199], v[80:83]
ds_read_b64_tr_b16 a[254:255], v6 offset:5728
v_mfma_f32_16x16x32_bf16 v[24:27], a[232:235], v[192:195], v[24:27]
ds_read_b64_tr_b16 a[224:225], v6 offset:10240
s_add_u32 s68, s23, 21520384
v_mfma_f32_16x16x32_bf16 v[84:87], a[232:235], v[196:199], v[84:87]
ds_read_b64_tr_b16 a[226:227], v6 offset:10752
s_add_u32 m0, s61, 4096
v_mfma_f32_16x16x32_bf16 v[28:31], a[236:239], v[192:195], v[28:31]
ds_read_b64_tr_b16 a[228:229], v6 offset:10272
buffer_load_dwordx4 v0, s[44:47], s68 offen sc1 lds
v_mfma_f32_16x16x32_bf16 v[88:91], a[236:239], v[196:199], v[88:91]
ds_read_b64_tr_b16 a[230:231], v6 offset:10784
s_waitcnt lgkmcnt(4)
v_mfma_f32_16x16x32_bf16 v[16:19], a[240:243], v[200:203], v[16:19]
ds_read_b64_tr_b16 a[232:233], v6 offset:10304
)ASM"
R"ASM(
s_add_u32 s68, s22, 21516288
v_mfma_f32_16x16x32_bf16 v[76:79], a[240:243], v[204:207], v[76:79]
ds_read_b64_tr_b16 a[234:235], v6 offset:10816
s_add_u32 m0, s61, 20480
v_mfma_f32_16x16x32_bf16 v[20:23], a[244:247], v[200:203], v[20:23]
ds_read_b64_tr_b16 a[236:237], v6 offset:10336
buffer_load_dwordx4 v0, s[44:47], s68 offen sc1 lds
v_mfma_f32_16x16x32_bf16 v[80:83], a[244:247], v[204:207], v[80:83]
ds_read_b64_tr_b16 a[238:239], v6 offset:10848
v_mfma_f32_16x16x32_bf16 v[24:27], a[248:251], v[200:203], v[24:27]
ds_read_b64_tr_b16 a[240:241], v6 offset:15360
buffer_load_dwordx4 v0, s[44:47], s68 offen offset:1024 sc1 lds
v_mfma_f32_16x16x32_bf16 v[84:87], a[248:251], v[204:207], v[84:87]
ds_read_b64_tr_b16 a[242:243], v6 offset:15872
v_mfma_f32_16x16x32_bf16 v[28:31], a[252:255], v[200:203], v[28:31]
ds_read_b64_tr_b16 a[244:245], v6 offset:15392
v_mfma_f32_16x16x32_bf16 v[88:91], a[252:255], v[204:207], v[88:91]
ds_read_b64_tr_b16 a[246:247], v6 offset:15904
s_waitcnt lgkmcnt(4)
v_mfma_f32_16x16x32_bf16 v[16:19], a[224:227], v[208:211], v[16:19]
ds_read_b64_tr_b16 a[248:249], v6 offset:15424
buffer_load_dwordx4 v0, s[44:47], s68 offen offset:2048 sc1 lds
v_mfma_f32_16x16x32_bf16 v[76:79], a[224:227], v[212:215], v[76:79]
ds_read_b64_tr_b16 a[250:251], v6 offset:15936
s_waitcnt vmcnt(19)
v_mfma_f32_16x16x32_bf16 v[20:23], a[228:231], v[208:211], v[20:23]
ds_read_b64_tr_b16 a[252:253], v6 offset:15456
s_barrier
v_mfma_f32_16x16x32_bf16 v[80:83], a[228:231], v[212:215], v[80:83]
ds_read_b64_tr_b16 a[254:255], v6 offset:15968
v_mfma_f32_16x16x32_bf16 v[24:27], a[232:235], v[208:211], v[24:27]
ds_read_b64_tr_b16 a[224:225], v4 offset:40960
s_waitcnt vmcnt(8)
v_mfma_f32_16x16x32_bf16 v[84:87], a[232:235], v[212:215], v[84:87]
ds_read_b64_tr_b16 a[226:227], v4 offset:41472
v_cmp_ne_u32 vcc, s62, v75
v_mfma_f32_16x16x32_bf16 v[28:31], a[236:239], v[208:211], v[28:31]
ds_read_b64_tr_b16 a[228:229], v4 offset:40992
s_cbranch_vccnz .Lpk_2s
.Lpk_2:
v_mfma_f32_16x16x32_bf16 v[88:91], a[236:239], v[212:215], v[88:91]
ds_read_b64_tr_b16 a[230:231], v4 offset:41504
s_waitcnt lgkmcnt(4)
v_mfma_f32_16x16x32_bf16 v[16:19], a[240:243], v[216:219], v[16:19]
ds_read_b64_tr_b16 a[232:233], v4 offset:41024
s_add_u32 s67, s14, 4096
v_mfma_f32_16x16x32_bf16 v[76:79], a[240:243], v[220:223], v[76:79]
ds_read_b64_tr_b16 a[234:235], v4 offset:41536
buffer_load_dwordx4 a[32:35], v0, s[44:47], s67 offen sc1
v_mfma_f32_16x16x32_bf16 v[20:23], a[244:247], v[216:219], v[20:23]
ds_read_b64_tr_b16 a[236:237], v4 offset:41056
buffer_load_dwordx4 a[36:39], v0, s[44:47], s67 offen offset:1024 sc1
v_mfma_f32_16x16x32_bf16 v[80:83], a[244:247], v[220:223], v[80:83]
ds_read_b64_tr_b16 a[238:239], v4 offset:41568
v_mfma_f32_16x16x32_bf16 v[24:27], a[248:251], v[216:219], v[24:27]
ds_read_b64_tr_b16 a[240:241], v4 offset:46080
buffer_load_dwordx4 a[40:43], v0, s[44:47], s67 offen offset:2048 sc1
v_mfma_f32_16x16x32_bf16 v[84:87], a[248:251], v[220:223], v[84:87]
ds_read_b64_tr_b16 a[242:243], v4 offset:46592
v_mfma_f32_16x16x32_bf16 v[28:31], a[252:255], v[216:219], v[28:31]
ds_read_b64_tr_b16 a[244:245], v4 offset:46112
v_mfma_f32_16x16x32_bf16 v[88:91], a[252:255], v[220:223], v[88:91]
ds_read_b64_tr_b16 a[246:247], v4 offset:46624
s_waitcnt lgkmcnt(4)
v_mfma_f32_16x16x32_bf16 v[16:19], a[224:227], v[224:227], v[16:19]
ds_read_b64_tr_b16 a[248:249], v4 offset:46144
buffer_load_dwordx4 a[44:47], v0, s[44:47], s67 offen offset:3072 sc1
v_mfma_f32_16x16x32_bf16 v[76:79], a[224:227], v[228:231], v[76:79]
ds_read_b64_tr_b16 a[250:251], v4 offset:46656
v_mfma_f32_16x16x32_bf16 v[20:23], a[228:231], v[224:227], v[20:23]
ds_read_b64_tr_b16 a[252:253], v4 offset:46176
v_mfma_f32_16x16x32_bf16 v[80:83], a[228:231], v[228:231], v[80:83]
ds_read_b64_tr_b16 a[254:255], v4 offset:46688
v_mfma_f32_16x16x32_bf16 v[24:27], a[232:235], v[224:227], v[24:27]
ds_read_b64_tr_b16 a[224:225], v4 offset:51200
s_add_u32 s67, s15, 4096
v_mfma_f32_16x16x32_bf16 v[84:87], a[232:235], v[228:231], v[84:87]
ds_read_b64_tr_b16 a[226:227], v4 offset:51712
buffer_load_dwordx4 a[48:51], v0, s[44:47], s67 offen sc1
v_mfma_f32_16x16x32_bf16 v[28:31], a[236:239], v[224:227], v[28:31]
ds_read_b64_tr_b16 a[228:229], v4 offset:51232
v_mfma_f32_16x16x32_bf16 v[88:91], a[236:239], v[228:231], v[88:91]
ds_read_b64_tr_b16 a[230:231], v4 offset:51744
s_waitcnt lgkmcnt(4)
v_mfma_f32_16x16x32_bf16 v[16:19], a[240:243], v[232:235], v[16:19]
ds_read_b64_tr_b16 a[232:233], v4 offset:51264
buffer_load_dwordx4 a[52:55], v0, s[44:47], s67 offen offset:1024 sc1
v_mfma_f32_16x16x32_bf16 v[76:79], a[240:243], v[236:239], v[76:79]
ds_read_b64_tr_b16 a[234:235], v4 offset:51776
v_mfma_f32_16x16x32_bf16 v[20:23], a[244:247], v[232:235], v[20:23]
ds_read_b64_tr_b16 a[236:237], v4 offset:51296
v_mfma_f32_16x16x32_bf16 v[80:83], a[244:247], v[236:239], v[80:83]
ds_read_b64_tr_b16 a[238:239], v4 offset:51808
v_mfma_f32_16x16x32_bf16 v[24:27], a[248:251], v[232:235], v[24:27]
ds_read_b64_tr_b16 a[240:241], v4 offset:56320
buffer_load_dwordx4 a[56:59], v0, s[44:47], s67 offen offset:2048 sc1
v_mfma_f32_16x16x32_bf16 v[84:87], a[248:251], v[236:239], v[84:87]
ds_read_b64_tr_b16 a[242:243], v4 offset:56832
v_accvgpr_read_b32 v13, a193
v_mfma_f32_16x16x32_bf16 v[28:31], a[252:255], v[232:235], v[28:31]
ds_read_b64_tr_b16 a[244:245], v4 offset:56352
ds_read_b128 v[32:35], v13
v_mfma_f32_16x16x32_bf16 v[88:91], a[252:255], v[236:239], v[88:91]
ds_read_b64_tr_b16 a[246:247], v4 offset:56864
s_waitcnt lgkmcnt(5)
v_mfma_f32_16x16x32_bf16 v[16:19], a[224:227], v[240:243], v[16:19]
ds_read_b64_tr_b16 a[248:249], v4 offset:56384
buffer_load_dwordx4 a[60:63], v0, s[44:47], s67 offen offset:3072 sc1
v_mfma_f32_16x16x32_bf16 v[76:79], a[224:227], v[244:247], v[76:79]
ds_read_b64_tr_b16 a[250:251], v4 offset:56896
ds_read_b128 v[92:95], v13 offset:64
v_mfma_f32_16x16x32_bf16 v[20:23], a[228:231], v[240:243], v[20:23]
ds_read_b64_tr_b16 a[252:253], v4 offset:56416
v_mfma_f32_16x16x32_bf16 v[80:83], a[228:231], v[244:247], v[80:83]
ds_read_b64_tr_b16 a[254:255], v4 offset:56928
v_mfma_f32_16x16x32_bf16 v[24:27], a[232:235], v[240:243], v[24:27]
buffer_load_dwordx4 v0, s[44:47], s68 offen offset:3072 sc1 lds
v_mfma_f32_16x16x32_bf16 v[84:87], a[232:235], v[244:247], v[84:87]
v_mfma_f32_16x16x32_bf16 v[28:31], a[236:239], v[240:243], v[28:31]
v_mfma_f32_16x16x32_bf16 v[88:91], a[236:239], v[244:247], v[88:91]
s_waitcnt lgkmcnt(0)
v_mfma_f32_16x16x32_bf16 v[16:19], a[240:243], v[248:251], v[16:19]
s_add_u32 s68, s22, 21520384
s_add_u32 m0, s61, 24576
v_mfma_f32_16x16x32_bf16 v[76:79], a[240:243], v[252:255], v[76:79]
buffer_load_dwordx4 v0, s[44:47], s68 offen sc1 lds
v_mfma_f32_16x16x32_bf16 v[20:23], a[244:247], v[248:251], v[20:23]
v_mfma_f32_16x16x32_bf16 v[80:83], a[244:247], v[252:255], v[80:83]
v_mfma_f32_16x16x32_bf16 v[24:27], a[248:251], v[248:251], v[24:27]
s_add_u32 s68, s23, 21495808
s_add_u32 m0, s61, 102400
v_mfma_f32_16x16x32_bf16 v[84:87], a[248:251], v[252:255], v[84:87]
buffer_load_dwordx4 v0, s[44:47], s68 offen sc1 lds
v_mfma_f32_16x16x32_bf16 v[28:31], a[252:255], v[248:251], v[28:31]
v_mfma_f32_16x16x32_bf16 v[88:91], a[252:255], v[252:255], v[88:91]
v_pk_fma_f32 v[16:17], v[16:17], v[14:15], v[32:33] op_sel:[0,0,0] op_sel_hi:[1,0,1]
v_pk_fma_f32 v[18:19], v[18:19], v[14:15], v[34:35] op_sel:[0,0,0] op_sel_hi:[1,0,1]
v_pk_fma_f32 v[76:77], v[76:77], v[14:15], v[32:33] op_sel:[0,1,0] op_sel_hi:[1,1,1]
v_pk_fma_f32 v[78:79], v[78:79], v[14:15], v[34:35] op_sel:[0,1,0] op_sel_hi:[1,1,1]
ds_read_b128 v[32:35], v13 offset:128
v_max_f32 v16, 0, v16
v_max_f32 v17, 0, v17
v_max_f32 v18, 0, v18
v_max_f32 v19, 0, v19
v_pk_mul_f32 v[16:17], v[16:17], v[16:17]
v_pk_mul_f32 v[18:19], v[18:19], v[18:19]
v_cvt_pk_bf16_f32 v176, v16, v17
v_cvt_pk_bf16_f32 v177, v18, v19
v_max_f32 v76, 0, v76
v_max_f32 v77, 0, v77
v_max_f32 v78, 0, v78
v_max_f32 v79, 0, v79
v_pk_mul_f32 v[76:77], v[76:77], v[76:77]
v_pk_mul_f32 v[78:79], v[78:79], v[78:79]
v_cvt_pk_bf16_f32 v180, v76, v77
v_cvt_pk_bf16_f32 v181, v78, v79
v_pk_fma_f32 v[20:21], v[20:21], v[14:15], v[92:93] op_sel:[0,0,0] op_sel_hi:[1,0,1]
v_pk_fma_f32 v[22:23], v[22:23], v[14:15], v[94:95] op_sel:[0,0,0] op_sel_hi:[1,0,1]
v_pk_fma_f32 v[80:81], v[80:81], v[14:15], v[92:93] op_sel:[0,1,0] op_sel_hi:[1,1,1]
v_pk_fma_f32 v[82:83], v[82:83], v[14:15], v[94:95] op_sel:[0,1,0] op_sel_hi:[1,1,1]
ds_read_b128 v[92:95], v13 offset:192
v_max_f32 v20, 0, v20
v_max_f32 v21, 0, v21
v_max_f32 v22, 0, v22
v_max_f32 v23, 0, v23
v_pk_mul_f32 v[20:21], v[20:21], v[20:21]
v_pk_mul_f32 v[22:23], v[22:23], v[22:23]
v_cvt_pk_bf16_f32 v178, v20, v21
v_cvt_pk_bf16_f32 v179, v22, v23
v_max_f32 v80, 0, v80
v_max_f32 v81, 0, v81
v_max_f32 v82, 0, v82
v_max_f32 v83, 0, v83
v_pk_mul_f32 v[80:81], v[80:81], v[80:81]
v_pk_mul_f32 v[82:83], v[82:83], v[82:83]
v_cvt_pk_bf16_f32 v182, v80, v81
v_cvt_pk_bf16_f32 v183, v82, v83
s_add_u32 s66, s14, 0
buffer_store_dwordx4 v[176:179], v0, s[44:47], s66 offen sc1
buffer_store_dwordx4 v[180:183], v0, s[44:47], s66 offen offset:1024 sc1
s_barrier
ds_read_b64_tr_b16 a[224:225], v2
ds_read_b64_tr_b16 a[226:227], v2 offset:2560
ds_read_b64_tr_b16 a[228:229], v2 offset:8
ds_read_b64_tr_b16 a[230:231], v2 offset:2568
ds_read_b64_tr_b16 a[232:233], v2 offset:16
ds_read_b64_tr_b16 a[234:235], v2 offset:2576
ds_read_b64_tr_b16 a[236:237], v2 offset:24
ds_read_b64_tr_b16 a[238:239], v2 offset:2584
ds_read_b64_tr_b16 a[240:241], v2 offset:32
ds_read_b64_tr_b16 a[242:243], v2 offset:2592
ds_read_b64_tr_b16 a[244:245], v2 offset:5120
ds_read_b64_tr_b16 a[246:247], v2 offset:7680
s_waitcnt lgkmcnt(13)
v_pk_fma_f32 v[24:25], v[24:25], v[14:15], v[32:33] op_sel:[0,0,0] op_sel_hi:[1,0,1]
v_pk_fma_f32 v[26:27], v[26:27], v[14:15], v[34:35] op_sel:[0,0,0] op_sel_hi:[1,0,1]
v_pk_fma_f32 v[84:85], v[84:85], v[14:15], v[32:33] op_sel:[0,1,0] op_sel_hi:[1,1,1]
v_pk_fma_f32 v[86:87], v[86:87], v[14:15], v[34:35] op_sel:[0,1,0] op_sel_hi:[1,1,1]
v_max_f32 v24, 0, v24
v_max_f32 v25, 0, v25
v_max_f32 v26, 0, v26
v_max_f32 v27, 0, v27
v_pk_mul_f32 v[24:25], v[24:25], v[24:25]
v_pk_mul_f32 v[26:27], v[26:27], v[26:27]
v_cvt_pk_bf16_f32 v184, v24, v25
v_cvt_pk_bf16_f32 v185, v26, v27
v_max_f32 v84, 0, v84
v_max_f32 v85, 0, v85
v_max_f32 v86, 0, v86
v_max_f32 v87, 0, v87
v_pk_mul_f32 v[84:85], v[84:85], v[84:85]
v_pk_mul_f32 v[86:87], v[86:87], v[86:87]
v_cvt_pk_bf16_f32 v188, v84, v85
v_cvt_pk_bf16_f32 v189, v86, v87
s_waitcnt lgkmcnt(12)
v_pk_fma_f32 v[28:29], v[28:29], v[14:15], v[92:93] op_sel:[0,0,0] op_sel_hi:[1,0,1]
v_pk_fma_f32 v[30:31], v[30:31], v[14:15], v[94:95] op_sel:[0,0,0] op_sel_hi:[1,0,1]
v_pk_fma_f32 v[88:89], v[88:89], v[14:15], v[92:93] op_sel:[0,1,0] op_sel_hi:[1,1,1]
v_pk_fma_f32 v[90:91], v[90:91], v[14:15], v[94:95] op_sel:[0,1,0] op_sel_hi:[1,1,1]
v_max_f32 v28, 0, v28
v_max_f32 v29, 0, v29
v_max_f32 v30, 0, v30
v_max_f32 v31, 0, v31
v_pk_mul_f32 v[28:29], v[28:29], v[28:29]
v_pk_mul_f32 v[30:31], v[30:31], v[30:31]
v_cvt_pk_bf16_f32 v186, v28, v29
v_cvt_pk_bf16_f32 v187, v30, v31
v_max_f32 v88, 0, v88
v_max_f32 v89, 0, v89
v_max_f32 v90, 0, v90
v_max_f32 v91, 0, v91
v_pk_mul_f32 v[88:89], v[88:89], v[88:89]
v_pk_mul_f32 v[90:91], v[90:91], v[90:91]
v_cvt_pk_bf16_f32 v190, v88, v89
v_cvt_pk_bf16_f32 v191, v90, v91
buffer_store_dwordx4 v[184:187], v0, s[44:47], s66 offen offset:2048 sc1
buffer_store_dwordx4 v[188:191], v0, s[44:47], s66 offen offset:3072 sc1
v_mov_b32 v224, s62
v_lshrrev_b32 v225, 2, v0
v_lshrrev_b32 v12, 1, v0
s_waitcnt vmcnt(13) lgkmcnt(8)
v_mfma_f32_16x16x32_bf16 v[136:139], a[224:227], a[32:35], 0
ds_read_b64_tr_b16 a[248:249], v2 offset:5128
buffer_load_dwordx4 v0, s[44:47], s68 offen offset:1024 sc1 lds
v_mfma_f32_16x16x32_bf16 v[156:159], a[224:227], a[36:39], 0
ds_read_b64_tr_b16 a[250:251], v2 offset:7688
buffer_load_dwordx4 v0, s[44:47], s68 offen offset:2048 sc1 lds
v_mfma_f32_16x16x32_bf16 v[140:143], a[228:231], a[32:35], 0
ds_read_b64_tr_b16 a[252:253], v2 offset:5136
v_mfma_f32_16x16x32_bf16 v[160:163], a[228:231], a[36:39], 0
ds_read_b64_tr_b16 a[254:255], v2 offset:7696
s_waitcnt lgkmcnt(8)
v_mfma_f32_16x16x32_bf16 v[144:147], a[232:235], a[32:35], 0
ds_read_b64_tr_b16 a[224:225], v2 offset:5144
buffer_load_dwordx4 v0, s[44:47], s68 offen offset:3072 sc1 lds
v_mfma_f32_16x16x32_bf16 v[164:167], a[232:235], a[36:39], 0
ds_read_b64_tr_b16 a[226:227], v2 offset:7704
v_mfma_f32_16x16x32_bf16 v[148:151], a[236:239], a[32:35], 0
ds_read_b64_tr_b16 a[228:229], v2 offset:5152
v_mfma_f32_16x16x32_bf16 v[168:171], a[236:239], a[36:39], 0
ds_read_b64_tr_b16 a[230:231], v2 offset:7712
s_waitcnt lgkmcnt(4)
v_mfma_f32_16x16x32_bf16 v[152:155], a[240:243], a[32:35], 0
ds_read_b64_tr_b16 a[232:233], v2 offset:10240
s_add_u32 s68, s23, 21499904
v_mfma_f32_16x16x32_bf16 v[172:175], a[240:243], a[36:39], 0
ds_read_b64_tr_b16 a[234:235], v2 offset:12800
s_waitcnt vmcnt(14)
v_mfma_f32_16x16x32_bf16 v[136:139], a[244:247], a[40:43], v[136:139]
ds_read_b64_tr_b16 a[236:237], v2 offset:10248
s_add_u32 m0, s61, 106496
v_mfma_f32_16x16x32_bf16 v[156:159], a[244:247], a[44:47], v[156:159]
ds_read_b64_tr_b16 a[238:239], v2 offset:12808
buffer_load_dwordx4 v0, s[44:47], s68 offen sc1 lds
v_mfma_f32_16x16x32_bf16 v[140:143], a[248:251], a[40:43], v[140:143]
ds_read_b64_tr_b16 a[240:241], v2 offset:10256
s_add_u32 s68, s22, 21495808
v_mfma_f32_16x16x32_bf16 v[160:163], a[248:251], a[44:47], v[160:163]
ds_read_b64_tr_b16 a[242:243], v2 offset:12816
s_add_u32 m0, s61, 122880
v_mfma_f32_16x16x32_bf16 v[144:147], a[252:255], a[40:43], v[144:147]
ds_read_b64_tr_b16 a[244:245], v2 offset:10264
buffer_load_dwordx4 v0, s[44:47], s68 offen sc1 lds
v_mfma_f32_16x16x32_bf16 v[164:167], a[252:255], a[44:47], v[164:167]
ds_read_b64_tr_b16 a[246:247], v2 offset:12824
s_waitcnt lgkmcnt(4)
v_mfma_f32_16x16x32_bf16 v[148:151], a[224:227], a[40:43], v[148:151]
ds_read_b64_tr_b16 a[248:249], v2 offset:10272
buffer_load_dwordx4 v0, s[44:47], s68 offen offset:1024 sc1 lds
v_mfma_f32_16x16x32_bf16 v[168:171], a[224:227], a[44:47], v[168:171]
ds_read_b64_tr_b16 a[250:251], v2 offset:12832
v_mfma_f32_16x16x32_bf16 v[152:155], a[228:231], a[40:43], v[152:155]
ds_read_b64_tr_b16 a[252:253], v2 offset:15360
v_mfma_f32_16x16x32_bf16 v[172:175], a[228:231], a[44:47], v[172:175]
ds_read_b64_tr_b16 a[254:255], v2 offset:17920
s_waitcnt vmcnt(15)
v_mfma_f32_16x16x32_bf16 v[136:139], a[232:235], a[48:51], v[136:139]
ds_read_b64_tr_b16 a[224:225], v2 offset:15368
buffer_load_dwordx4 v0, s[44:47], s68 offen offset:2048 sc1 lds
v_mfma_f32_16x16x32_bf16 v[156:159], a[232:235], a[52:55], v[156:159]
ds_read_b64_tr_b16 a[226:227], v2 offset:17928
v_mfma_f32_16x16x32_bf16 v[140:143], a[236:239], a[48:51], v[140:143]
ds_read_b64_tr_b16 a[228:229], v2 offset:15376
v_mfma_f32_16x16x32_bf16 v[160:163], a[236:239], a[52:55], v[160:163]
ds_read_b64_tr_b16 a[230:231], v2 offset:17936
s_waitcnt lgkmcnt(4)
v_mfma_f32_16x16x32_bf16 v[144:147], a[240:243], a[48:51], v[144:147]
ds_read_b64_tr_b16 a[232:233], v2 offset:15384
buffer_load_dwordx4 v0, s[44:47], s68 offen offset:3072 sc1 lds
v_mfma_f32_16x16x32_bf16 v[164:167], a[240:243], a[52:55], v[164:167]
ds_read_b64_tr_b16 a[234:235], v2 offset:17944
v_mfma_f32_16x16x32_bf16 v[148:151], a[244:247], a[48:51], v[148:151]
ds_read_b64_tr_b16 a[236:237], v2 offset:15392
s_barrier
v_mfma_f32_16x16x32_bf16 v[168:171], a[244:247], a[52:55], v[168:171]
ds_read_b64_tr_b16 a[238:239], v2 offset:17952
s_add_u32 s68, s22, 21499904
v_mfma_f32_16x16x32_bf16 v[152:155], a[248:251], a[48:51], v[152:155]
ds_read_b64_tr_b16 a[240:241], v2 offset:20480
v_accvgpr_read_b32 v226, a209
v_mfma_f32_16x16x32_bf16 v[172:175], a[248:251], a[52:55], v[172:175]
ds_read_b64_tr_b16 a[242:243], v2 offset:23040
s_waitcnt vmcnt(15)
v_mfma_f32_16x16x32_bf16 v[136:139], a[252:255], a[56:59], v[136:139]
ds_read_b64_tr_b16 a[244:245], v2 offset:20488
s_mov_b32 s65, 19660800
v_mfma_f32_16x16x32_bf16 v[156:159], a[252:255], a[60:63], v[156:159]
ds_read_b64_tr_b16 a[246:247], v2 offset:23048
s_waitcnt lgkmcnt(4)
v_mfma_f32_16x16x32_bf16 v[140:143], a[224:227], a[56:59], v[140:143]
ds_read_b64_tr_b16 a[248:249], v2 offset:20496
buffer_load_dword v227, v226, s[44:47], s65 offen sc1
v_mfma_f32_16x16x32_bf16 v[160:163], a[224:227], a[60:63], v[160:163]
ds_read_b64_tr_b16 a[250:251], v2 offset:23056
s_add_u32 m0, s61, 126976
v_mfma_f32_16x16x32_bf16 v[144:147], a[228:231], a[56:59], v[144:147]
ds_read_b64_tr_b16 a[252:253], v2 offset:20504
buffer_load_dwordx4 v0, s[44:47], s68 offen sc1 lds
v_mfma_f32_16x16x32_bf16 v[164:167], a[228:231], a[60:63], v[164:167]
ds_read_b64_tr_b16 a[254:255], v2 offset:23064
s_add_u32 s68, s40, 23461888
v_mfma_f32_16x16x32_bf16 v[148:151], a[232:235], a[56:59], v[148:151]
ds_read_b64_tr_b16 a[224:225], v2 offset:20512
s_add_u32 m0, s61, 40960
v_mfma_f32_16x16x32_bf16 v[168:171], a[232:235], a[60:63], v[168:171]
ds_read_b64_tr_b16 a[226:227], v2 offset:23072
buffer_load_dwordx4 v0, s[44:47], s68 offen sc1 lds
v_mfma_f32_16x16x32_bf16 v[152:155], a[236:239], a[56:59], v[152:155]
ds_read_b64_tr_b16 a[228:229], v2 offset:25600
buffer_load_dwordx4 v0, s[44:47], s68 offen offset:1024 sc1 lds
v_mfma_f32_16x16x32_bf16 v[172:175], a[236:239], a[60:63], v[172:175]
ds_read_b64_tr_b16 a[230:231], v2 offset:28160
s_waitcnt lgkmcnt(4)
v_mfma_f32_16x16x32_bf16 v[96:99], a[240:243], a[32:35], 0
ds_read_b64_tr_b16 a[232:233], v2 offset:25608
s_add_u32 s65, s64, 262144
v_mfma_f32_16x16x32_bf16 v[116:119], a[240:243], a[36:39], 0
ds_read_b64_tr_b16 a[234:235], v2 offset:28168
s_waitcnt vmcnt(12)
v_mfma_f32_16x16x32_bf16 v[100:103], a[244:247], a[32:35], 0
ds_read_b64_tr_b16 a[236:237], v2 offset:25616
buffer_store_dword v224, v225, s[44:47], s65 offen sc1
v_mfma_f32_16x16x32_bf16 v[120:123], a[244:247], a[36:39], 0
ds_read_b64_tr_b16 a[238:239], v2 offset:28176
buffer_load_dwordx4 v0, s[44:47], s68 offen offset:2048 sc1 lds
v_mfma_f32_16x16x32_bf16 v[104:107], a[248:251], a[32:35], 0
ds_read_b64_tr_b16 a[240:241], v2 offset:25624
buffer_load_dwordx4 v0, s[44:47], s68 offen offset:3072 sc1 lds
v_mfma_f32_16x16x32_bf16 v[124:127], a[248:251], a[36:39], 0
ds_read_b64_tr_b16 a[242:243], v2 offset:28184
v_mfma_f32_16x16x32_bf16 v[108:111], a[252:255], a[32:35], 0
ds_read_b64_tr_b16 a[244:245], v2 offset:25632
v_mfma_f32_16x16x32_bf16 v[128:131], a[252:255], a[36:39], 0
ds_read_b64_tr_b16 a[246:247], v2 offset:28192
s_waitcnt lgkmcnt(4)
v_mfma_f32_16x16x32_bf16 v[112:115], a[224:227], a[32:35], 0
ds_read_b64_tr_b16 a[248:249], v2 offset:30720
s_waitcnt vmcnt(6)
v_mfma_f32_16x16x32_bf16 v[132:135], a[224:227], a[36:39], 0
ds_read_b64_tr_b16 a[250:251], v2 offset:33280
v_cmp_ne_u32 vcc, s62, v227
v_mfma_f32_16x16x32_bf16 v[96:99], a[228:231], a[40:43], v[96:99]
ds_read_b64_tr_b16 a[252:253], v2 offset:30728
s_cbranch_vccnz .Lpk_3s
.Lpk_3:
v_mfma_f32_16x16x32_bf16 v[116:119], a[228:231], a[44:47], v[116:119]
ds_read_b64_tr_b16 a[254:255], v2 offset:33288
s_add_u32 s67, s14, 8192
v_mfma_f32_16x16x32_bf16 v[100:103], a[232:235], a[40:43], v[100:103]
ds_read_b64_tr_b16 a[224:225], v2 offset:30736
buffer_load_dwordx4 a[64:67], v0, s[44:47], s67 offen sc1
v_mfma_f32_16x16x32_bf16 v[120:123], a[232:235], a[44:47], v[120:123]
ds_read_b64_tr_b16 a[226:227], v2 offset:33296
buffer_load_dwordx4 a[68:71], v0, s[44:47], s67 offen offset:1024 sc1
v_mfma_f32_16x16x32_bf16 v[104:107], a[236:239], a[40:43], v[104:107]
ds_read_b64_tr_b16 a[228:229], v2 offset:30744
v_mfma_f32_16x16x32_bf16 v[124:127], a[236:239], a[44:47], v[124:127]
ds_read_b64_tr_b16 a[230:231], v2 offset:33304
s_waitcnt lgkmcnt(4)
v_mfma_f32_16x16x32_bf16 v[108:111], a[240:243], a[40:43], v[108:111]
)ASM"
R"ASM(
ds_read_b64_tr_b16 a[232:233], v2 offset:30752
buffer_load_dwordx4 a[72:75], v0, s[44:47], s67 offen offset:2048 sc1
v_mfma_f32_16x16x32_bf16 v[128:131], a[240:243], a[44:47], v[128:131]
ds_read_b64_tr_b16 a[234:235], v2 offset:33312
v_mfma_f32_16x16x32_bf16 v[112:115], a[244:247], a[40:43], v[112:115]
ds_read_b64_tr_b16 a[236:237], v2 offset:35840
v_mfma_f32_16x16x32_bf16 v[132:135], a[244:247], a[44:47], v[132:135]
ds_read_b64_tr_b16 a[238:239], v2 offset:38400
buffer_load_dwordx4 a[76:79], v0, s[44:47], s67 offen offset:3072 sc1
v_mfma_f32_16x16x32_bf16 v[96:99], a[248:251], a[48:51], v[96:99]
ds_read_b64_tr_b16 a[240:241], v2 offset:35848
v_mfma_f32_16x16x32_bf16 v[116:119], a[248:251], a[52:55], v[116:119]
ds_read_b64_tr_b16 a[242:243], v2 offset:38408
v_mfma_f32_16x16x32_bf16 v[100:103], a[252:255], a[48:51], v[100:103]
ds_read_b64_tr_b16 a[244:245], v2 offset:35856
v_mfma_f32_16x16x32_bf16 v[120:123], a[252:255], a[52:55], v[120:123]
ds_read_b64_tr_b16 a[246:247], v2 offset:38416
s_waitcnt lgkmcnt(4)
v_mfma_f32_16x16x32_bf16 v[104:107], a[224:227], a[48:51], v[104:107]
ds_read_b64_tr_b16 a[248:249], v2 offset:35864
s_add_u32 s67, s15, 8192
v_mfma_f32_16x16x32_bf16 v[124:127], a[224:227], a[52:55], v[124:127]
ds_read_b64_tr_b16 a[250:251], v2 offset:38424
buffer_load_dwordx4 a[80:83], v0, s[44:47], s67 offen sc1
v_mfma_f32_16x16x32_bf16 v[108:111], a[228:231], a[48:51], v[108:111]
ds_read_b64_tr_b16 a[252:253], v2 offset:35872
s_barrier
v_mfma_f32_16x16x32_bf16 v[128:131], a[228:231], a[52:55], v[128:131]
ds_read_b64_tr_b16 a[254:255], v2 offset:38432
buffer_load_dwordx4 a[84:87], v0, s[44:47], s67 offen offset:1024 sc1
v_mfma_f32_16x16x32_bf16 v[112:115], a[232:235], a[48:51], v[112:115]
ds_read_b64_tr_b16 a[224:225], v1
v_mfma_f32_16x16x32_bf16 v[132:135], a[232:235], a[52:55], v[132:135]
ds_read_b64_tr_b16 a[226:227], v1 offset:2560
v_mfma_f32_16x16x32_bf16 v[96:99], a[236:239], a[56:59], v[96:99]
ds_read_b64_tr_b16 a[228:229], v1 offset:8
v_mfma_f32_16x16x32_bf16 v[116:119], a[236:239], a[60:63], v[116:119]
ds_read_b64_tr_b16 a[230:231], v1 offset:2568
s_waitcnt lgkmcnt(4)
v_mfma_f32_16x16x32_bf16 v[100:103], a[240:243], a[56:59], v[100:103]
ds_read_b64_tr_b16 a[232:233], v1 offset:16
buffer_load_dwordx4 a[88:91], v0, s[44:47], s67 offen offset:2048 sc1
v_mfma_f32_16x16x32_bf16 v[120:123], a[240:243], a[60:63], v[120:123]
ds_read_b64_tr_b16 a[234:235], v1 offset:2576
v_mfma_f32_16x16x32_bf16 v[104:107], a[244:247], a[56:59], v[104:107]
ds_read_b64_tr_b16 a[236:237], v1 offset:24
v_mfma_f32_16x16x32_bf16 v[124:127], a[244:247], a[60:63], v[124:127]
ds_read_b64_tr_b16 a[238:239], v1 offset:2584
buffer_load_dwordx4 a[92:95], v0, s[44:47], s67 offen offset:3072 sc1
v_mfma_f32_16x16x32_bf16 v[108:111], a[248:251], a[56:59], v[108:111]
ds_read_b64_tr_b16 a[240:241], v1 offset:32
v_mfma_f32_16x16x32_bf16 v[128:131], a[248:251], a[60:63], v[128:131]
ds_read_b64_tr_b16 a[242:243], v1 offset:2592
v_mfma_f32_16x16x32_bf16 v[112:115], a[252:255], a[56:59], v[112:115]
ds_read_b64_tr_b16 a[244:245], v1 offset:5120
s_add_u32 s68, s40, 23465984
v_mfma_f32_16x16x32_bf16 v[132:135], a[252:255], a[60:63], v[132:135]
ds_read_b64_tr_b16 a[246:247], v1 offset:7680
s_waitcnt vmcnt(6) lgkmcnt(4)
v_mfma_f32_16x16x32_bf16 v[136:139], a[224:227], a[64:67], v[136:139]
ds_read_b64_tr_b16 a[248:249], v1 offset:5128
s_add_u32 m0, s61, 45056
v_mfma_f32_16x16x32_bf16 v[156:159], a[224:227], a[68:71], v[156:159]
ds_read_b64_tr_b16 a[250:251], v1 offset:7688
buffer_load_dwordx4 v0, s[44:47], s68 offen sc1 lds
v_mfma_f32_16x16x32_bf16 v[140:143], a[228:231], a[64:67], v[140:143]
ds_read_b64_tr_b16 a[252:253], v1 offset:5136
s_add_u32 s68, s40, 23482368
v_mfma_f32_16x16x32_bf16 v[160:163], a[228:231], a[68:71], v[160:163]
ds_read_b64_tr_b16 a[254:255], v1 offset:7696
s_add_u32 m0, s61, 61440
v_mfma_f32_16x16x32_bf16 v[144:147], a[232:235], a[64:67], v[144:147]
ds_read_b64_tr_b16 a[224:225], v1 offset:5144
buffer_load_dwordx4 v0, s[44:47], s68 offen sc1 lds
v_mfma_f32_16x16x32_bf16 v[164:167], a[232:235], a[68:71], v[164:167]
ds_read_b64_tr_b16 a[226:227], v1 offset:7704
v_mfma_f32_16x16x32_bf16 v[148:151], a[236:239], a[64:67], v[148:151]
ds_read_b64_tr_b16 a[228:229], v1 offset:5152
buffer_load_dwordx4 v0, s[44:47], s68 offen offset:1024 sc1 lds
v_mfma_f32_16x16x32_bf16 v[168:171], a[236:239], a[68:71], v[168:171]
ds_read_b64_tr_b16 a[230:231], v1 offset:7712
s_waitcnt lgkmcnt(4)
v_mfma_f32_16x16x32_bf16 v[152:155], a[240:243], a[64:67], v[152:155]
ds_read_b64_tr_b16 a[232:233], v1 offset:10240
v_mfma_f32_16x16x32_bf16 v[172:175], a[240:243], a[68:71], v[172:175]
ds_read_b64_tr_b16 a[234:235], v1 offset:12800
s_waitcnt vmcnt(7)
v_mfma_f32_16x16x32_bf16 v[136:139], a[244:247], a[72:75], v[136:139]
ds_read_b64_tr_b16 a[236:237], v1 offset:10248
buffer_load_dwordx4 v0, s[44:47], s68 offen offset:2048 sc1 lds
v_mfma_f32_16x16x32_bf16 v[156:159], a[244:247], a[76:79], v[156:159]
ds_read_b64_tr_b16 a[238:239], v1 offset:12808
v_mfma_f32_16x16x32_bf16 v[140:143], a[248:251], a[72:75], v[140:143]
ds_read_b64_tr_b16 a[240:241], v1 offset:10256
v_mfma_f32_16x16x32_bf16 v[160:163], a[248:251], a[76:79], v[160:163]
ds_read_b64_tr_b16 a[242:243], v1 offset:12816
v_mfma_f32_16x16x32_bf16 v[144:147], a[252:255], a[72:75], v[144:147]
ds_read_b64_tr_b16 a[244:245], v1 offset:10264
buffer_load_dwordx4 v0, s[44:47], s68 offen offset:3072 sc1 lds
v_mfma_f32_16x16x32_bf16 v[164:167], a[252:255], a[76:79], v[164:167]
ds_read_b64_tr_b16 a[246:247], v1 offset:12824
s_waitcnt lgkmcnt(4)
v_mfma_f32_16x16x32_bf16 v[148:151], a[224:227], a[72:75], v[148:151]
ds_read_b64_tr_b16 a[248:249], v1 offset:10272
v_mfma_f32_16x16x32_bf16 v[168:171], a[224:227], a[76:79], v[168:171]
ds_read_b64_tr_b16 a[250:251], v1 offset:12832
v_mfma_f32_16x16x32_bf16 v[152:155], a[228:231], a[72:75], v[152:155]
ds_read_b64_tr_b16 a[252:253], v1 offset:15360
s_add_u32 s68, s40, 23486464
v_mfma_f32_16x16x32_bf16 v[172:175], a[228:231], a[76:79], v[172:175]
ds_read_b64_tr_b16 a[254:255], v1 offset:17920
s_waitcnt vmcnt(7)
v_mfma_f32_16x16x32_bf16 v[136:139], a[232:235], a[80:83], v[136:139]
ds_read_b64_tr_b16 a[224:225], v1 offset:15368
s_add_u32 m0, s61, 65536
v_mfma_f32_16x16x32_bf16 v[156:159], a[232:235], a[84:87], v[156:159]
ds_read_b64_tr_b16 a[226:227], v1 offset:17928
buffer_load_dwordx4 v0, s[44:47], s68 offen sc1 lds
v_mfma_f32_16x16x32_bf16 v[140:143], a[236:239], a[80:83], v[140:143]
ds_read_b64_tr_b16 a[228:229], v1 offset:15376
v_mfma_f32_16x16x32_bf16 v[160:163], a[236:239], a[84:87], v[160:163]
ds_read_b64_tr_b16 a[230:231], v1 offset:17936
s_waitcnt lgkmcnt(4)
v_mfma_f32_16x16x32_bf16 v[144:147], a[240:243], a[80:83], v[144:147]
ds_read_b64_tr_b16 a[232:233], v1 offset:15384
v_mfma_f32_16x16x32_bf16 v[164:167], a[240:243], a[84:87], v[164:167]
ds_read_b64_tr_b16 a[234:235], v1 offset:17944
v_mfma_f32_16x16x32_bf16 v[148:151], a[244:247], a[80:83], v[148:151]
ds_read_b64_tr_b16 a[236:237], v1 offset:15392
s_barrier
v_mfma_f32_16x16x32_bf16 v[168:171], a[244:247], a[84:87], v[168:171]
ds_read_b64_tr_b16 a[238:239], v1 offset:17952
s_add_u32 s68, s40, 23502848
v_mfma_f32_16x16x32_bf16 v[152:155], a[248:251], a[80:83], v[152:155]
ds_read_b64_tr_b16 a[240:241], v1 offset:20480
s_add_u32 m0, s61, 81920
v_mfma_f32_16x16x32_bf16 v[172:175], a[248:251], a[84:87], v[172:175]
ds_read_b64_tr_b16 a[242:243], v1 offset:23040
s_waitcnt vmcnt(6)
v_mfma_f32_16x16x32_bf16 v[136:139], a[252:255], a[88:91], v[136:139]
ds_read_b64_tr_b16 a[244:245], v1 offset:20488
buffer_load_dwordx4 v0, s[44:47], s68 offen sc1 lds
v_mfma_f32_16x16x32_bf16 v[156:159], a[252:255], a[92:95], v[156:159]
ds_read_b64_tr_b16 a[246:247], v1 offset:23048
s_waitcnt lgkmcnt(4)
v_mfma_f32_16x16x32_bf16 v[140:143], a[224:227], a[88:91], v[140:143]
ds_read_b64_tr_b16 a[248:249], v1 offset:20496
buffer_load_dwordx4 v0, s[44:47], s68 offen offset:1024 sc1 lds
v_mfma_f32_16x16x32_bf16 v[160:163], a[224:227], a[92:95], v[160:163]
ds_read_b64_tr_b16 a[250:251], v1 offset:23056
buffer_load_dwordx4 v0, s[44:47], s68 offen offset:2048 sc1 lds
v_mfma_f32_16x16x32_bf16 v[144:147], a[228:231], a[88:91], v[144:147]
ds_read_b64_tr_b16 a[252:253], v1 offset:20504
v_mfma_f32_16x16x32_bf16 v[164:167], a[228:231], a[92:95], v[164:167]
ds_read_b64_tr_b16 a[254:255], v1 offset:23064
v_mfma_f32_16x16x32_bf16 v[148:151], a[232:235], a[88:91], v[148:151]
ds_read_b64_tr_b16 a[224:225], v1 offset:20512
v_accvgpr_read_b32 v228, a210
v_mfma_f32_16x16x32_bf16 v[168:171], a[232:235], a[92:95], v[168:171]
ds_read_b64_tr_b16 a[226:227], v1 offset:23072
s_mov_b32 s65, 19660800
v_mfma_f32_16x16x32_bf16 v[152:155], a[236:239], a[88:91], v[152:155]
ds_read_b64_tr_b16 a[228:229], v1 offset:25600
buffer_load_dword v229, v228, s[44:47], s65 offen sc1
v_mfma_f32_16x16x32_bf16 v[172:175], a[236:239], a[92:95], v[172:175]
ds_read_b64_tr_b16 a[230:231], v1 offset:28160
s_waitcnt lgkmcnt(4)
v_mfma_f32_16x16x32_bf16 v[96:99], a[240:243], a[64:67], v[96:99]
ds_read_b64_tr_b16 a[232:233], v1 offset:25608
buffer_load_dwordx4 v0, s[44:47], s68 offen offset:3072 sc1 lds
v_mfma_f32_16x16x32_bf16 v[116:119], a[240:243], a[68:71], v[116:119]
ds_read_b64_tr_b16 a[234:235], v1 offset:28168
s_add_u32 s68, s40, 23506944
v_mfma_f32_16x16x32_bf16 v[100:103], a[244:247], a[64:67], v[100:103]
ds_read_b64_tr_b16 a[236:237], v1 offset:25616
s_add_u32 m0, s61, 86016
v_mfma_f32_16x16x32_bf16 v[120:123], a[244:247], a[68:71], v[120:123]
ds_read_b64_tr_b16 a[238:239], v1 offset:28176
buffer_load_dwordx4 v0, s[44:47], s68 offen sc1 lds
v_mfma_f32_16x16x32_bf16 v[104:107], a[248:251], a[64:67], v[104:107]
ds_read_b64_tr_b16 a[240:241], v1 offset:25624
v_mfma_f32_16x16x32_bf16 v[124:127], a[248:251], a[68:71], v[124:127]
ds_read_b64_tr_b16 a[242:243], v1 offset:28184
v_mfma_f32_16x16x32_bf16 v[108:111], a[252:255], a[64:67], v[108:111]
ds_read_b64_tr_b16 a[244:245], v1 offset:25632
v_mfma_f32_16x16x32_bf16 v[128:131], a[252:255], a[68:71], v[128:131]
ds_read_b64_tr_b16 a[246:247], v1 offset:28192
s_waitcnt lgkmcnt(4)
v_mfma_f32_16x16x32_bf16 v[112:115], a[224:227], a[64:67], v[112:115]
ds_read_b64_tr_b16 a[248:249], v1 offset:30720
v_mfma_f32_16x16x32_bf16 v[132:135], a[224:227], a[68:71], v[132:135]
ds_read_b64_tr_b16 a[250:251], v1 offset:33280
v_mfma_f32_16x16x32_bf16 v[96:99], a[228:231], a[72:75], v[96:99]
ds_read_b64_tr_b16 a[252:253], v1 offset:30728
v_mfma_f32_16x16x32_bf16 v[116:119], a[228:231], a[76:79], v[116:119]
ds_read_b64_tr_b16 a[254:255], v1 offset:33288
v_mfma_f32_16x16x32_bf16 v[100:103], a[232:235], a[72:75], v[100:103]
ds_read_b64_tr_b16 a[224:225], v1 offset:30736
v_mfma_f32_16x16x32_bf16 v[120:123], a[232:235], a[76:79], v[120:123]
ds_read_b64_tr_b16 a[226:227], v1 offset:33296
v_mfma_f32_16x16x32_bf16 v[104:107], a[236:239], a[72:75], v[104:107]
ds_read_b64_tr_b16 a[228:229], v1 offset:30744
v_mfma_f32_16x16x32_bf16 v[124:127], a[236:239], a[76:79], v[124:127]
ds_read_b64_tr_b16 a[230:231], v1 offset:33304
s_waitcnt lgkmcnt(4)
v_mfma_f32_16x16x32_bf16 v[108:111], a[240:243], a[72:75], v[108:111]
ds_read_b64_tr_b16 a[232:233], v1 offset:30752
v_mfma_f32_16x16x32_bf16 v[128:131], a[240:243], a[76:79], v[128:131]
ds_read_b64_tr_b16 a[234:235], v1 offset:33312
v_mfma_f32_16x16x32_bf16 v[112:115], a[244:247], a[72:75], v[112:115]
ds_read_b64_tr_b16 a[236:237], v1 offset:35840
v_mfma_f32_16x16x32_bf16 v[132:135], a[244:247], a[76:79], v[132:135]
ds_read_b64_tr_b16 a[238:239], v1 offset:38400
v_mfma_f32_16x16x32_bf16 v[96:99], a[248:251], a[80:83], v[96:99]
ds_read_b64_tr_b16 a[240:241], v1 offset:35848
s_waitcnt vmcnt(2)
v_mfma_f32_16x16x32_bf16 v[116:119], a[248:251], a[84:87], v[116:119]
ds_read_b64_tr_b16 a[242:243], v1 offset:38408
v_cmp_ne_u32 vcc, s62, v229
v_mfma_f32_16x16x32_bf16 v[100:103], a[252:255], a[80:83], v[100:103]
ds_read_b64_tr_b16 a[244:245], v1 offset:35856
s_cbranch_vccnz .Lpk_4s
.Lpk_4:
v_mfma_f32_16x16x32_bf16 v[120:123], a[252:255], a[84:87], v[120:123]
ds_read_b64_tr_b16 a[246:247], v1 offset:38416
s_waitcnt lgkmcnt(4)
v_mfma_f32_16x16x32_bf16 v[104:107], a[224:227], a[80:83], v[104:107]
ds_read_b64_tr_b16 a[248:249], v1 offset:35864
buffer_load_dwordx4 a[16:19], v0, s[44:47], s15 offen sc1
v_mfma_f32_16x16x32_bf16 v[124:127], a[224:227], a[84:87], v[124:127]
ds_read_b64_tr_b16 a[250:251], v1 offset:38424
buffer_load_dwordx4 a[20:23], v0, s[44:47], s15 offen offset:1024 sc1
v_mfma_f32_16x16x32_bf16 v[108:111], a[228:231], a[80:83], v[108:111]
ds_read_b64_tr_b16 a[252:253], v1 offset:35872
s_barrier
v_mfma_f32_16x16x32_bf16 v[128:131], a[228:231], a[84:87], v[128:131]
ds_read_b64_tr_b16 a[254:255], v1 offset:38432
v_mfma_f32_16x16x32_bf16 v[112:115], a[232:235], a[80:83], v[112:115]
ds_read_b64_tr_b16 a[224:225], v2 offset:40960
buffer_load_dwordx4 a[24:27], v0, s[44:47], s15 offen offset:2048 sc1
v_mfma_f32_16x16x32_bf16 v[132:135], a[232:235], a[84:87], v[132:135]
ds_read_b64_tr_b16 a[226:227], v2 offset:43520
v_mfma_f32_16x16x32_bf16 v[96:99], a[236:239], a[88:91], v[96:99]
ds_read_b64_tr_b16 a[228:229], v2 offset:40968
v_mfma_f32_16x16x32_bf16 v[116:119], a[236:239], a[92:95], v[116:119]
ds_read_b64_tr_b16 a[230:231], v2 offset:43528
s_waitcnt lgkmcnt(4)
v_mfma_f32_16x16x32_bf16 v[100:103], a[240:243], a[88:91], v[100:103]
ds_read_b64_tr_b16 a[232:233], v2 offset:40976
buffer_load_dwordx4 a[28:31], v0, s[44:47], s15 offen offset:3072 sc1
v_mfma_f32_16x16x32_bf16 v[120:123], a[240:243], a[92:95], v[120:123]
ds_read_b64_tr_b16 a[234:235], v2 offset:43536
v_mfma_f32_16x16x32_bf16 v[104:107], a[244:247], a[88:91], v[104:107]
ds_read_b64_tr_b16 a[236:237], v2 offset:40984
v_mfma_f32_16x16x32_bf16 v[124:127], a[244:247], a[92:95], v[124:127]
ds_read_b64_tr_b16 a[238:239], v2 offset:43544
v_mfma_f32_16x16x32_bf16 v[108:111], a[248:251], a[88:91], v[108:111]
ds_read_b64_tr_b16 a[240:241], v2 offset:40992
s_add_u32 s68, s40, 23523328
v_mfma_f32_16x16x32_bf16 v[128:131], a[248:251], a[92:95], v[128:131]
ds_read_b64_tr_b16 a[242:243], v2 offset:43552
s_add_u32 m0, s61, 0
v_mfma_f32_16x16x32_bf16 v[112:115], a[252:255], a[88:91], v[112:115]
ds_read_b64_tr_b16 a[244:245], v2 offset:46080
buffer_load_dwordx4 v0, s[44:47], s68 offen sc1 lds
v_mfma_f32_16x16x32_bf16 v[132:135], a[252:255], a[92:95], v[132:135]
ds_read_b64_tr_b16 a[246:247], v2 offset:48640
s_waitcnt lgkmcnt(4)
v_mfma_f32_16x16x32_bf16 v[136:139], a[224:227], v[176:179], v[136:139]
ds_read_b64_tr_b16 a[248:249], v2 offset:46088
buffer_load_dwordx4 v0, s[44:47], s68 offen offset:1024 sc1 lds
v_mfma_f32_16x16x32_bf16 v[156:159], a[224:227], v[180:183], v[156:159]
ds_read_b64_tr_b16 a[250:251], v2 offset:48648
v_mfma_f32_16x16x32_bf16 v[140:143], a[228:231], v[176:179], v[140:143]
ds_read_b64_tr_b16 a[252:253], v2 offset:46096
v_mfma_f32_16x16x32_bf16 v[160:163], a[228:231], v[180:183], v[160:163]
ds_read_b64_tr_b16 a[254:255], v2 offset:48656
v_mfma_f32_16x16x32_bf16 v[144:147], a[232:235], v[176:179], v[144:147]
ds_read_b64_tr_b16 a[224:225], v2 offset:46104
buffer_load_dwordx4 v0, s[44:47], s68 offen offset:2048 sc1 lds
v_mfma_f32_16x16x32_bf16 v[164:167], a[232:235], v[180:183], v[164:167]
ds_read_b64_tr_b16 a[226:227], v2 offset:48664
v_mfma_f32_16x16x32_bf16 v[148:151], a[236:239], v[176:179], v[148:151]
ds_read_b64_tr_b16 a[228:229], v2 offset:46112
v_mfma_f32_16x16x32_bf16 v[168:171], a[236:239], v[180:183], v[168:171]
ds_read_b64_tr_b16 a[230:231], v2 offset:48672
s_waitcnt lgkmcnt(4)
v_mfma_f32_16x16x32_bf16 v[152:155], a[240:243], v[176:179], v[152:155]
ds_read_b64_tr_b16 a[232:233], v2 offset:51200
buffer_load_dwordx4 v0, s[44:47], s68 offen offset:3072 sc1 lds
v_mfma_f32_16x16x32_bf16 v[172:175], a[240:243], v[180:183], v[172:175]
ds_read_b64_tr_b16 a[234:235], v2 offset:53760
v_mfma_f32_16x16x32_bf16 v[136:139], a[244:247], v[184:187], v[136:139]
ds_read_b64_tr_b16 a[236:237], v2 offset:51208
v_mfma_f32_16x16x32_bf16 v[156:159], a[244:247], v[188:191], v[156:159]
ds_read_b64_tr_b16 a[238:239], v2 offset:53768
s_add_u32 s68, s40, 23527424
v_mfma_f32_16x16x32_bf16 v[140:143], a[248:251], v[184:187], v[140:143]
ds_read_b64_tr_b16 a[240:241], v2 offset:51216
s_add_u32 m0, s61, 4096
v_mfma_f32_16x16x32_bf16 v[160:163], a[248:251], v[188:191], v[160:163]
ds_read_b64_tr_b16 a[242:243], v2 offset:53776
buffer_load_dwordx4 v0, s[44:47], s68 offen sc1 lds
v_mfma_f32_16x16x32_bf16 v[144:147], a[252:255], v[184:187], v[144:147]
ds_read_b64_tr_b16 a[244:245], v2 offset:51224
v_mfma_f32_16x16x32_bf16 v[164:167], a[252:255], v[188:191], v[164:167]
ds_read_b64_tr_b16 a[246:247], v2 offset:53784
s_waitcnt lgkmcnt(4)
v_mfma_f32_16x16x32_bf16 v[148:151], a[224:227], v[184:187], v[148:151]
ds_read_b64_tr_b16 a[248:249], v2 offset:51232
v_mfma_f32_16x16x32_bf16 v[168:171], a[224:227], v[188:191], v[168:171]
ds_read_b64_tr_b16 a[250:251], v2 offset:53792
v_mfma_f32_16x16x32_bf16 v[152:155], a[228:231], v[184:187], v[152:155]
ds_read_b64_tr_b16 a[252:253], v2 offset:56320
v_mfma_f32_16x16x32_bf16 v[172:175], a[228:231], v[188:191], v[172:175]
ds_read_b64_tr_b16 a[254:255], v2 offset:58880
s_waitcnt vmcnt(7)
v_mfma_f32_16x16x32_bf16 v[136:139], a[232:235], a[16:19], v[136:139]
ds_read_b64_tr_b16 a[224:225], v2 offset:56328
v_mfma_f32_16x16x32_bf16 v[156:159], a[232:235], a[20:23], v[156:159]
ds_read_b64_tr_b16 a[226:227], v2 offset:58888
v_mfma_f32_16x16x32_bf16 v[140:143], a[236:239], a[16:19], v[140:143]
ds_read_b64_tr_b16 a[228:229], v2 offset:56336
v_mfma_f32_16x16x32_bf16 v[160:163], a[236:239], a[20:23], v[160:163]
ds_read_b64_tr_b16 a[230:231], v2 offset:58896
s_waitcnt lgkmcnt(4)
v_mfma_f32_16x16x32_bf16 v[144:147], a[240:243], a[16:19], v[144:147]
ds_read_b64_tr_b16 a[232:233], v2 offset:56344
v_mfma_f32_16x16x32_bf16 v[164:167], a[240:243], a[20:23], v[164:167]
ds_read_b64_tr_b16 a[234:235], v2 offset:58904
v_mfma_f32_16x16x32_bf16 v[148:151], a[244:247], a[16:19], v[148:151]
ds_read_b64_tr_b16 a[236:237], v2 offset:56352
s_barrier
v_mfma_f32_16x16x32_bf16 v[168:171], a[244:247], a[20:23], v[168:171]
ds_read_b64_tr_b16 a[238:239], v2 offset:58912
s_add_u32 s68, s40, 23543808
v_mfma_f32_16x16x32_bf16 v[152:155], a[248:251], a[16:19], v[152:155]
ds_read_b64_tr_b16 a[240:241], v3
s_add_u32 m0, s61, 20480
v_mfma_f32_16x16x32_bf16 v[172:175], a[248:251], a[20:23], v[172:175]
ds_read_b64_tr_b16 a[242:243], v3 offset:2560
s_waitcnt vmcnt(5)
v_mfma_f32_16x16x32_bf16 v[136:139], a[252:255], a[24:27], v[136:139]
ds_read_b64_tr_b16 a[244:245], v3 offset:8
buffer_load_dwordx4 v0, s[44:47], s68 offen sc1 lds
v_mfma_f32_16x16x32_bf16 v[156:159], a[252:255], a[28:31], v[156:159]
ds_read_b64_tr_b16 a[246:247], v3 offset:2568
s_waitcnt lgkmcnt(4)
v_mfma_f32_16x16x32_bf16 v[140:143], a[224:227], a[24:27], v[140:143]
ds_read_b64_tr_b16 a[248:249], v3 offset:16
buffer_load_dwordx4 v0, s[44:47], s68 offen offset:1024 sc1 lds
v_mfma_f32_16x16x32_bf16 v[160:163], a[224:227], a[28:31], v[160:163]
ds_read_b64_tr_b16 a[250:251], v3 offset:2576
buffer_load_dwordx4 v0, s[44:47], s68 offen offset:2048 sc1 lds
v_mfma_f32_16x16x32_bf16 v[144:147], a[228:231], a[24:27], v[144:147]
ds_read_b64_tr_b16 a[252:253], v3 offset:24
v_mfma_f32_16x16x32_bf16 v[164:167], a[228:231], a[28:31], v[164:167]
ds_read_b64_tr_b16 a[254:255], v3 offset:2584
v_mfma_f32_16x16x32_bf16 v[148:151], a[232:235], a[24:27], v[148:151]
ds_read_b64_tr_b16 a[224:225], v3 offset:32
buffer_load_dwordx4 v0, s[44:47], s68 offen offset:3072 sc1 lds
v_mfma_f32_16x16x32_bf16 v[168:171], a[232:235], a[28:31], v[168:171]
ds_read_b64_tr_b16 a[226:227], v3 offset:2592
v_mfma_f32_16x16x32_bf16 v[152:155], a[236:239], a[24:27], v[152:155]
ds_read_b64_tr_b16 a[228:229], v3 offset:5120
v_mfma_f32_16x16x32_bf16 v[172:175], a[236:239], a[28:31], v[172:175]
ds_read_b64_tr_b16 a[230:231], v3 offset:7680
s_waitcnt lgkmcnt(4)
v_mfma_f32_16x16x32_bf16 v[96:99], a[240:243], v[176:179], v[96:99]
ds_read_b64_tr_b16 a[232:233], v3 offset:5128
s_add_u32 s68, s40, 23547904
v_mfma_f32_16x16x32_bf16 v[116:119], a[240:243], v[180:183], v[116:119]
ds_read_b64_tr_b16 a[234:235], v3 offset:7688
s_add_u32 m0, s61, 24576
v_mfma_f32_16x16x32_bf16 v[100:103], a[244:247], v[176:179], v[100:103]
ds_read_b64_tr_b16 a[236:237], v3 offset:5136
buffer_load_dwordx4 v0, s[44:47], s68 offen sc1 lds
v_mfma_f32_16x16x32_bf16 v[120:123], a[244:247], v[180:183], v[120:123]
ds_read_b64_tr_b16 a[238:239], v3 offset:7696
v_mfma_f32_16x16x32_bf16 v[104:107], a[248:251], v[176:179], v[104:107]
ds_read_b64_tr_b16 a[240:241], v3 offset:5144
v_cvt_pk_bf16_f32 v46, v136, v137
v_mfma_f32_16x16x32_bf16 v[124:127], a[248:251], v[180:183], v[124:127]
ds_read_b64_tr_b16 a[242:243], v3 offset:7704
v_cvt_pk_bf16_f32 v47, v138, v139
v_mfma_f32_16x16x32_bf16 v[108:111], a[252:255], v[176:179], v[108:111]
ds_read_b64_tr_b16 a[244:245], v3 offset:5152
v_cvt_pk_bf16_f32 v48, v140, v141
v_mfma_f32_16x16x32_bf16 v[128:131], a[252:255], v[180:183], v[128:131]
ds_read_b64_tr_b16 a[246:247], v3 offset:7712
s_waitcnt lgkmcnt(4)
v_mfma_f32_16x16x32_bf16 v[112:115], a[224:227], v[176:179], v[112:115]
ds_read_b64_tr_b16 a[248:249], v3 offset:10240
v_cvt_pk_bf16_f32 v49, v142, v143
v_mfma_f32_16x16x32_bf16 v[132:135], a[224:227], v[180:183], v[132:135]
ds_read_b64_tr_b16 a[250:251], v3 offset:12800
buffer_store_dwordx4 v[46:49], v0, s[44:47], s53 offen sc1
v_mfma_f32_16x16x32_bf16 v[96:99], a[228:231], v[184:187], v[96:99]
ds_read_b64_tr_b16 a[252:253], v3 offset:10248
v_cvt_pk_bf16_f32 v50, v144, v145
)ASM"
R"ASM(
v_mfma_f32_16x16x32_bf16 v[116:119], a[228:231], v[188:191], v[116:119]
ds_read_b64_tr_b16 a[254:255], v3 offset:12808
v_cvt_pk_bf16_f32 v51, v146, v147
v_mfma_f32_16x16x32_bf16 v[100:103], a[232:235], v[184:187], v[100:103]
ds_read_b64_tr_b16 a[224:225], v3 offset:10256
v_cvt_pk_bf16_f32 v52, v148, v149
v_mfma_f32_16x16x32_bf16 v[120:123], a[232:235], v[188:191], v[120:123]
ds_read_b64_tr_b16 a[226:227], v3 offset:12816
v_cvt_pk_bf16_f32 v53, v150, v151
v_mfma_f32_16x16x32_bf16 v[104:107], a[236:239], v[184:187], v[104:107]
ds_read_b64_tr_b16 a[228:229], v3 offset:10264
buffer_store_dwordx4 v[50:53], v0, s[44:47], s53 offen offset:1024 sc1
v_mfma_f32_16x16x32_bf16 v[124:127], a[236:239], v[188:191], v[124:127]
ds_read_b64_tr_b16 a[230:231], v3 offset:12824
s_waitcnt lgkmcnt(4)
v_mfma_f32_16x16x32_bf16 v[108:111], a[240:243], v[184:187], v[108:111]
ds_read_b64_tr_b16 a[232:233], v3 offset:10272
v_cvt_pk_bf16_f32 v54, v152, v153
v_mfma_f32_16x16x32_bf16 v[128:131], a[240:243], v[188:191], v[128:131]
ds_read_b64_tr_b16 a[234:235], v3 offset:12832
v_cvt_pk_bf16_f32 v55, v154, v155
v_mfma_f32_16x16x32_bf16 v[112:115], a[244:247], v[184:187], v[112:115]
ds_read_b64_tr_b16 a[236:237], v3 offset:15360
buffer_store_dwordx2 v[54:55], v12, s[44:47], s53 offen offset:2048 sc1
v_mfma_f32_16x16x32_bf16 v[132:135], a[244:247], v[188:191], v[132:135]
ds_read_b64_tr_b16 a[238:239], v3 offset:17920
v_cvt_pk_bf16_f32 v56, v156, v157
v_mfma_f32_16x16x32_bf16 v[96:99], a[248:251], a[16:19], v[96:99]
ds_read_b64_tr_b16 a[240:241], v3 offset:15368
v_cvt_pk_bf16_f32 v57, v158, v159
v_mfma_f32_16x16x32_bf16 v[116:119], a[248:251], a[20:23], v[116:119]
ds_read_b64_tr_b16 a[242:243], v3 offset:17928
v_cvt_pk_bf16_f32 v58, v160, v161
v_mfma_f32_16x16x32_bf16 v[100:103], a[252:255], a[16:19], v[100:103]
ds_read_b64_tr_b16 a[244:245], v3 offset:15376
v_cvt_pk_bf16_f32 v59, v162, v163
v_mfma_f32_16x16x32_bf16 v[120:123], a[252:255], a[20:23], v[120:123]
ds_read_b64_tr_b16 a[246:247], v3 offset:17936
s_waitcnt lgkmcnt(4)
v_mfma_f32_16x16x32_bf16 v[104:107], a[224:227], a[16:19], v[104:107]
ds_read_b64_tr_b16 a[248:249], v3 offset:15384
buffer_store_dwordx4 v[56:59], v0, s[44:47], s54 offen sc1
v_mfma_f32_16x16x32_bf16 v[124:127], a[224:227], a[20:23], v[124:127]
ds_read_b64_tr_b16 a[250:251], v3 offset:17944
v_cvt_pk_bf16_f32 v60, v164, v165
v_mfma_f32_16x16x32_bf16 v[108:111], a[228:231], a[16:19], v[108:111]
ds_read_b64_tr_b16 a[252:253], v3 offset:15392
v_cvt_pk_bf16_f32 v61, v166, v167
v_mfma_f32_16x16x32_bf16 v[128:131], a[228:231], a[20:23], v[128:131]
ds_read_b64_tr_b16 a[254:255], v3 offset:17952
v_cvt_pk_bf16_f32 v62, v168, v169
v_mfma_f32_16x16x32_bf16 v[112:115], a[232:235], a[16:19], v[112:115]
v_cvt_pk_bf16_f32 v63, v170, v171
buffer_store_dwordx4 v[60:63], v0, s[44:47], s54 offen offset:1024 sc1
v_mfma_f32_16x16x32_bf16 v[132:135], a[232:235], a[20:23], v[132:135]
v_cvt_pk_bf16_f32 v64, v172, v173
v_cvt_pk_bf16_f32 v65, v174, v175
v_mfma_f32_16x16x32_bf16 v[96:99], a[236:239], a[24:27], v[96:99]
buffer_store_dwordx2 v[64:65], v12, s[44:47], s54 offen offset:2048 sc1
v_mfma_f32_16x16x32_bf16 v[116:119], a[236:239], a[28:31], v[116:119]
s_waitcnt lgkmcnt(0)
v_mfma_f32_16x16x32_bf16 v[100:103], a[240:243], a[24:27], v[100:103]
v_mfma_f32_16x16x32_bf16 v[120:123], a[240:243], a[28:31], v[120:123]
v_mfma_f32_16x16x32_bf16 v[104:107], a[244:247], a[24:27], v[104:107]
v_mfma_f32_16x16x32_bf16 v[124:127], a[244:247], a[28:31], v[124:127]
v_mfma_f32_16x16x32_bf16 v[108:111], a[248:251], a[24:27], v[108:111]
v_mfma_f32_16x16x32_bf16 v[128:131], a[248:251], a[28:31], v[128:131]
v_mfma_f32_16x16x32_bf16 v[112:115], a[252:255], a[24:27], v[112:115]
v_mfma_f32_16x16x32_bf16 v[132:135], a[252:255], a[28:31], v[132:135]
v_mfma_f32_16x16x16_bf16 v[96:99], a[222:223], a[160:161], v[96:99]
v_cvt_pk_bf16_f32 v36, v116, v117
v_cvt_pk_bf16_f32 v37, v118, v119
v_mfma_f32_16x16x16_bf16 v[100:103], a[222:223], a[162:163], v[100:103]
v_cvt_pk_bf16_f32 v38, v120, v121
v_cvt_pk_bf16_f32 v39, v122, v123
v_mfma_f32_16x16x16_bf16 v[104:107], a[222:223], a[164:165], v[104:107]
buffer_store_dwordx4 v[36:39], v0, s[44:47], s52 offen sc1
v_cvt_pk_bf16_f32 v40, v124, v125
v_mfma_f32_16x16x16_bf16 v[108:111], a[222:223], a[166:167], v[108:111]
v_cvt_pk_bf16_f32 v41, v126, v127
v_cvt_pk_bf16_f32 v42, v128, v129
v_mfma_f32_16x16x16_bf16 v[112:115], a[222:223], a[168:169], v[112:115]
v_cvt_pk_bf16_f32 v43, v130, v131
buffer_store_dwordx4 v[40:43], v0, s[44:47], s52 offen offset:1024 sc1
v_mfma_f32_16x16x16_bf16 v[96:99], a[222:223], a[170:171], v[96:99]
v_cvt_pk_bf16_f32 v44, v132, v133
v_cvt_pk_bf16_f32 v45, v134, v135
v_mfma_f32_16x16x16_bf16 v[100:103], a[222:223], a[172:173], v[100:103]
buffer_store_dwordx2 v[44:45], v12, s[44:47], s52 offen offset:2048 sc1
v_mfma_f32_16x16x16_bf16 v[104:107], a[222:223], a[174:175], v[104:107]
v_mfma_f32_16x16x16_bf16 v[108:111], a[222:223], a[176:177], v[108:111]
v_mfma_f32_16x16x16_bf16 v[112:115], a[222:223], a[178:179], v[112:115]
v_add_u32 v1, s70, v1
v_add_u32 v2, s70, v2
v_add_u32 v3, s70, v3
s_barrier
ds_read_b64_tr_b16 a[224:225], v1 offset:40960
ds_read_b64_tr_b16 a[226:227], v1 offset:43520
ds_read_b64_tr_b16 a[228:229], v1 offset:40968
ds_read_b64_tr_b16 a[230:231], v1 offset:43528
ds_read_b64_tr_b16 a[232:233], v1 offset:40976
ds_read_b64_tr_b16 a[234:235], v1 offset:43536
ds_read_b64_tr_b16 a[236:237], v1 offset:40984
ds_read_b64_tr_b16 a[238:239], v1 offset:43544
ds_read_b64_tr_b16 a[240:241], v1 offset:40992
ds_read_b64_tr_b16 a[242:243], v1 offset:43552
ds_read_b64_tr_b16 a[244:245], v1 offset:46080
ds_read_b64_tr_b16 a[246:247], v1 offset:48640
s_add_u32 s65, s64, 786432
s_waitcnt vmcnt(0)
buffer_store_dword v224, v225, s[44:47], s65 offen sc1
s_lshl_b32 s65, s56, 8
s_or_b32 s65, s63, s65
s_cmp_eq_u32 s65, 1
s_cbranch_scc1 .Lek_5s
.Lek_5:
v_accvgpr_read_b32 v230, a212
s_mov_b32 s65, 19922944
buffer_load_dword v231, v230, s[44:47], s65 offen sc1
s_waitcnt vmcnt(0)
v_cmp_ne_u32 vcc, s62, v231
s_cbranch_vccnz .Lpk_6s
.Lpk_6:
s_add_u32 s17, s19, 5120
s_add_u32 s18, s20, 5120
buffer_load_dwordx4 a[96:99], v0, s[44:47], s19 offen sc1
buffer_load_dwordx4 a[100:103], v0, s[44:47], s20 offen sc1
buffer_load_dwordx4 a[104:107], v0, s[44:47], s17 offen sc1
buffer_load_dwordx4 a[108:111], v0, s[44:47], s18 offen sc1
buffer_load_dwordx4 a[112:115], v0, s[44:47], s19 offen offset:1024 sc1
buffer_load_dwordx4 a[116:119], v0, s[44:47], s20 offen offset:1024 sc1
buffer_load_dwordx4 a[120:123], v0, s[44:47], s17 offen offset:1024 sc1
buffer_load_dwordx4 a[124:127], v0, s[44:47], s18 offen offset:1024 sc1
s_waitcnt vmcnt(6) lgkmcnt(8)
v_mfma_f32_16x16x32_bf16 v[96:99], a[224:227], a[96:99], v[96:99]
ds_read_b64_tr_b16 a[248:249], v1 offset:46088
buffer_load_dwordx4 a[128:131], v0, s[44:47], s19 offen offset:2560 sc1
v_mfma_f32_16x16x32_bf16 v[116:119], a[224:227], a[100:103], 0
ds_read_b64_tr_b16 a[250:251], v1 offset:48648
v_mfma_f32_16x16x32_bf16 v[100:103], a[228:231], a[96:99], v[100:103]
ds_read_b64_tr_b16 a[252:253], v1 offset:46096
buffer_load_dwordx4 a[132:135], v0, s[44:47], s20 offen offset:2560 sc1
v_mfma_f32_16x16x32_bf16 v[120:123], a[228:231], a[100:103], 0
ds_read_b64_tr_b16 a[254:255], v1 offset:48656
s_waitcnt lgkmcnt(8)
v_mfma_f32_16x16x32_bf16 v[104:107], a[232:235], a[96:99], v[104:107]
ds_read_b64_tr_b16 a[224:225], v1 offset:46104
buffer_load_dwordx4 a[136:139], v0, s[44:47], s17 offen offset:2560 sc1
v_mfma_f32_16x16x32_bf16 v[124:127], a[232:235], a[100:103], 0
ds_read_b64_tr_b16 a[226:227], v1 offset:48664
v_mfma_f32_16x16x32_bf16 v[108:111], a[236:239], a[96:99], v[108:111]
ds_read_b64_tr_b16 a[228:229], v1 offset:46112
s_barrier
v_mfma_f32_16x16x32_bf16 v[128:131], a[236:239], a[100:103], 0
ds_read_b64_tr_b16 a[230:231], v1 offset:48672
s_waitcnt lgkmcnt(4)
v_mfma_f32_16x16x32_bf16 v[112:115], a[240:243], a[96:99], v[112:115]
ds_read_b64_tr_b16 a[232:233], v2
buffer_load_dwordx4 a[140:143], v0, s[44:47], s18 offen offset:2560 sc1
v_mfma_f32_16x16x32_bf16 v[132:135], a[240:243], a[100:103], 0
ds_read_b64_tr_b16 a[234:235], v2 offset:2560
s_waitcnt vmcnt(8)
v_mfma_f32_16x16x32_bf16 v[96:99], a[244:247], a[104:107], v[96:99]
ds_read_b64_tr_b16 a[236:237], v2 offset:8
buffer_load_dwordx4 a[144:147], v0, s[44:47], s19 offen offset:3584 sc1
v_mfma_f32_16x16x32_bf16 v[116:119], a[244:247], a[108:111], v[116:119]
ds_read_b64_tr_b16 a[238:239], v2 offset:2568
buffer_load_dwordx4 a[148:151], v0, s[44:47], s20 offen offset:3584 sc1
v_mfma_f32_16x16x32_bf16 v[100:103], a[248:251], a[104:107], v[100:103]
ds_read_b64_tr_b16 a[240:241], v2 offset:16
v_mfma_f32_16x16x32_bf16 v[120:123], a[248:251], a[108:111], v[120:123]
ds_read_b64_tr_b16 a[242:243], v2 offset:2576
buffer_load_dwordx4 a[152:155], v0, s[44:47], s17 offen offset:3584 sc1
v_mfma_f32_16x16x32_bf16 v[104:107], a[252:255], a[104:107], v[104:107]
ds_read_b64_tr_b16 a[244:245], v2 offset:24
v_mfma_f32_16x16x32_bf16 v[124:127], a[252:255], a[108:111], v[124:127]
ds_read_b64_tr_b16 a[246:247], v2 offset:2584
s_waitcnt lgkmcnt(4)
v_mfma_f32_16x16x32_bf16 v[108:111], a[224:227], a[104:107], v[108:111]
ds_read_b64_tr_b16 a[248:249], v2 offset:32
buffer_load_dwordx4 a[156:159], v0, s[44:47], s18 offen offset:3584 sc1
v_mfma_f32_16x16x32_bf16 v[128:131], a[224:227], a[108:111], v[128:131]
ds_read_b64_tr_b16 a[250:251], v2 offset:2592
buffer_load_dwordx2 v[192:193], v12, s[44:47], s19 offen offset:2048 sc1
v_mfma_f32_16x16x32_bf16 v[112:115], a[228:231], a[104:107], v[112:115]
ds_read_b64_tr_b16 a[252:253], v2 offset:5120
s_add_u32 s67, s19, 4096
v_mfma_f32_16x16x32_bf16 v[132:135], a[228:231], a[108:111], v[132:135]
ds_read_b64_tr_b16 a[254:255], v2 offset:7680
s_waitcnt vmcnt(11)
v_mfma_f32_16x16x32_bf16 v[96:99], a[232:235], a[112:115], v[96:99]
ds_read_b64_tr_b16 a[224:225], v2 offset:5128
buffer_load_dwordx2 v[194:195], v12, s[44:47], s67 offen offset:512 sc1
v_mfma_f32_16x16x32_bf16 v[116:119], a[232:235], a[116:119], v[116:119]
ds_read_b64_tr_b16 a[226:227], v2 offset:7688
buffer_load_dwordx2 v[196:197], v12, s[44:47], s20 offen offset:2048 sc1
v_mfma_f32_16x16x32_bf16 v[100:103], a[236:239], a[112:115], v[100:103]
ds_read_b64_tr_b16 a[228:229], v2 offset:5136
s_add_u32 s67, s20, 4096
v_mfma_f32_16x16x32_bf16 v[120:123], a[236:239], a[116:119], v[120:123]
ds_read_b64_tr_b16 a[230:231], v2 offset:7696
s_waitcnt lgkmcnt(4)
v_mfma_f32_16x16x32_bf16 v[104:107], a[240:243], a[112:115], v[104:107]
ds_read_b64_tr_b16 a[232:233], v2 offset:5144
buffer_load_dwordx2 v[198:199], v12, s[44:47], s67 offen offset:512 sc1
v_mfma_f32_16x16x32_bf16 v[124:127], a[240:243], a[116:119], v[124:127]
ds_read_b64_tr_b16 a[234:235], v2 offset:7704
buffer_load_dwordx2 v[200:201], v12, s[44:47], s17 offen offset:2048 sc1
v_mfma_f32_16x16x32_bf16 v[108:111], a[244:247], a[112:115], v[108:111]
ds_read_b64_tr_b16 a[236:237], v2 offset:5152
s_barrier
v_mfma_f32_16x16x32_bf16 v[128:131], a[244:247], a[116:119], v[128:131]
ds_read_b64_tr_b16 a[238:239], v2 offset:7712
s_add_u32 s67, s17, 4096
v_mfma_f32_16x16x32_bf16 v[112:115], a[248:251], a[112:115], v[112:115]
ds_read_b64_tr_b16 a[240:241], v2 offset:20480
buffer_load_dwordx2 v[202:203], v12, s[44:47], s67 offen offset:512 sc1
v_mfma_f32_16x16x32_bf16 v[132:135], a[248:251], a[116:119], v[132:135]
ds_read_b64_tr_b16 a[242:243], v2 offset:23040
s_waitcnt vmcnt(14)
v_mfma_f32_16x16x32_bf16 v[96:99], a[252:255], a[120:123], v[96:99]
ds_read_b64_tr_b16 a[244:245], v2 offset:20488
buffer_load_dwordx2 v[204:205], v12, s[44:47], s18 offen offset:2048 sc1
v_mfma_f32_16x16x32_bf16 v[116:119], a[252:255], a[124:127], v[116:119]
ds_read_b64_tr_b16 a[246:247], v2 offset:23048
s_waitcnt lgkmcnt(4)
v_mfma_f32_16x16x32_bf16 v[100:103], a[224:227], a[120:123], v[100:103]
ds_read_b64_tr_b16 a[248:249], v2 offset:20496
s_add_u32 s67, s18, 4096
v_mfma_f32_16x16x32_bf16 v[120:123], a[224:227], a[124:127], v[120:123]
ds_read_b64_tr_b16 a[250:251], v2 offset:23056
buffer_load_dwordx2 v[206:207], v12, s[44:47], s67 offen offset:512 sc1
v_mfma_f32_16x16x32_bf16 v[104:107], a[228:231], a[120:123], v[104:107]
ds_read_b64_tr_b16 a[252:253], v2 offset:20504
v_mfma_f32_16x16x32_bf16 v[124:127], a[228:231], a[124:127], v[124:127]
ds_read_b64_tr_b16 a[254:255], v2 offset:23064
v_mfma_f32_16x16x32_bf16 v[108:111], a[232:235], a[120:123], v[108:111]
ds_read_b64_tr_b16 a[224:225], v2 offset:20512
v_mfma_f32_16x16x32_bf16 v[128:131], a[232:235], a[124:127], v[128:131]
ds_read_b64_tr_b16 a[226:227], v2 offset:23072
v_mfma_f32_16x16x32_bf16 v[112:115], a[236:239], a[120:123], v[112:115]
ds_read_b64_tr_b16 a[228:229], v2 offset:25600
v_mfma_f32_16x16x32_bf16 v[132:135], a[236:239], a[124:127], v[132:135]
ds_read_b64_tr_b16 a[230:231], v2 offset:28160
s_waitcnt vmcnt(14) lgkmcnt(4)
v_mfma_f32_16x16x32_bf16 v[96:99], a[240:243], a[128:131], v[96:99]
ds_read_b64_tr_b16 a[232:233], v2 offset:25608
v_mfma_f32_16x16x32_bf16 v[116:119], a[240:243], a[132:135], v[116:119]
ds_read_b64_tr_b16 a[234:235], v2 offset:28168
v_mfma_f32_16x16x32_bf16 v[100:103], a[244:247], a[128:131], v[100:103]
ds_read_b64_tr_b16 a[236:237], v2 offset:25616
v_mfma_f32_16x16x32_bf16 v[120:123], a[244:247], a[132:135], v[120:123]
ds_read_b64_tr_b16 a[238:239], v2 offset:28176
v_mfma_f32_16x16x32_bf16 v[104:107], a[248:251], a[128:131], v[104:107]
ds_read_b64_tr_b16 a[240:241], v2 offset:25624
v_accvgpr_read_b32 v228, a211
v_mfma_f32_16x16x32_bf16 v[124:127], a[248:251], a[132:135], v[124:127]
ds_read_b64_tr_b16 a[242:243], v2 offset:28184
s_mov_b32 s65, 20185088
v_mfma_f32_16x16x32_bf16 v[108:111], a[252:255], a[128:131], v[108:111]
ds_read_b64_tr_b16 a[244:245], v2 offset:25632
s_barrier
v_mfma_f32_16x16x32_bf16 v[128:131], a[252:255], a[132:135], v[128:131]
ds_read_b64_tr_b16 a[246:247], v2 offset:28192
s_waitcnt lgkmcnt(4)
v_mfma_f32_16x16x32_bf16 v[112:115], a[224:227], a[128:131], v[112:115]
ds_read_b64_tr_b16 a[248:249], v1
buffer_load_dword v229, v228, s[44:47], s65 offen sc1
v_mfma_f32_16x16x32_bf16 v[132:135], a[224:227], a[132:135], v[132:135]
ds_read_b64_tr_b16 a[250:251], v1 offset:2560
s_waitcnt vmcnt(13)
v_mfma_f32_16x16x32_bf16 v[96:99], a[228:231], a[136:139], v[96:99]
ds_read_b64_tr_b16 a[252:253], v1 offset:8
v_mfma_f32_16x16x32_bf16 v[116:119], a[228:231], a[140:143], v[116:119]
ds_read_b64_tr_b16 a[254:255], v1 offset:2568
v_mfma_f32_16x16x32_bf16 v[100:103], a[232:235], a[136:139], v[100:103]
ds_read_b64_tr_b16 a[224:225], v1 offset:16
v_mfma_f32_16x16x32_bf16 v[120:123], a[232:235], a[140:143], v[120:123]
ds_read_b64_tr_b16 a[226:227], v1 offset:2576
v_mfma_f32_16x16x32_bf16 v[104:107], a[236:239], a[136:139], v[104:107]
ds_read_b64_tr_b16 a[228:229], v1 offset:24
v_mfma_f32_16x16x32_bf16 v[124:127], a[236:239], a[140:143], v[124:127]
ds_read_b64_tr_b16 a[230:231], v1 offset:2584
s_waitcnt lgkmcnt(4)
v_mfma_f32_16x16x32_bf16 v[108:111], a[240:243], a[136:139], v[108:111]
ds_read_b64_tr_b16 a[232:233], v1 offset:32
v_mfma_f32_16x16x32_bf16 v[128:131], a[240:243], a[140:143], v[128:131]
ds_read_b64_tr_b16 a[234:235], v1 offset:2592
v_mfma_f32_16x16x32_bf16 v[112:115], a[244:247], a[136:139], v[112:115]
ds_read_b64_tr_b16 a[236:237], v1 offset:5120
v_mfma_f32_16x16x32_bf16 v[132:135], a[244:247], a[140:143], v[132:135]
ds_read_b64_tr_b16 a[238:239], v1 offset:7680
s_waitcnt vmcnt(11)
v_mfma_f32_16x16x32_bf16 v[96:99], a[248:251], a[144:147], v[96:99]
ds_read_b64_tr_b16 a[240:241], v1 offset:5128
v_mfma_f32_16x16x32_bf16 v[116:119], a[248:251], a[148:151], v[116:119]
ds_read_b64_tr_b16 a[242:243], v1 offset:7688
v_mfma_f32_16x16x32_bf16 v[100:103], a[252:255], a[144:147], v[100:103]
ds_read_b64_tr_b16 a[244:245], v1 offset:5136
v_mfma_f32_16x16x32_bf16 v[120:123], a[252:255], a[148:151], v[120:123]
ds_read_b64_tr_b16 a[246:247], v1 offset:7696
s_waitcnt lgkmcnt(4)
v_mfma_f32_16x16x32_bf16 v[104:107], a[224:227], a[144:147], v[104:107]
ds_read_b64_tr_b16 a[248:249], v1 offset:5144
v_mfma_f32_16x16x32_bf16 v[124:127], a[224:227], a[148:151], v[124:127]
ds_read_b64_tr_b16 a[250:251], v1 offset:7704
v_mfma_f32_16x16x32_bf16 v[108:111], a[228:231], a[144:147], v[108:111]
ds_read_b64_tr_b16 a[252:253], v1 offset:5152
s_barrier
v_mfma_f32_16x16x32_bf16 v[128:131], a[228:231], a[148:151], v[128:131]
ds_read_b64_tr_b16 a[254:255], v1 offset:7712
v_mfma_f32_16x16x32_bf16 v[112:115], a[232:235], a[144:147], v[112:115]
ds_read_b64_tr_b16 a[224:225], v1 offset:20480
s_waitcnt vmcnt(0)
v_mfma_f32_16x16x32_bf16 v[132:135], a[232:235], a[148:151], v[132:135]
ds_read_b64_tr_b16 a[226:227], v1 offset:23040
v_cmp_ne_u32 vcc, s62, v229
v_mfma_f32_16x16x32_bf16 v[96:99], a[236:239], a[152:155], v[96:99]
ds_read_b64_tr_b16 a[228:229], v1 offset:20488
s_cbranch_vccnz .Lpk_7s
.Lpk_7:
v_mfma_f32_16x16x32_bf16 v[116:119], a[236:239], a[156:159], v[116:119]
ds_read_b64_tr_b16 a[230:231], v1 offset:23048
s_waitcnt lgkmcnt(4)
v_mfma_f32_16x16x32_bf16 v[100:103], a[240:243], a[152:155], v[100:103]
ds_read_b64_tr_b16 a[232:233], v1 offset:20496
buffer_load_dwordx4 v[36:39], v0, s[44:47], s21 offen sc1
v_mfma_f32_16x16x32_bf16 v[120:123], a[240:243], a[156:159], v[120:123]
ds_read_b64_tr_b16 a[234:235], v1 offset:23056
buffer_load_dwordx4 v[40:43], v0, s[44:47], s21 offen offset:1024 sc1
v_mfma_f32_16x16x32_bf16 v[104:107], a[244:247], a[152:155], v[104:107]
ds_read_b64_tr_b16 a[236:237], v1 offset:20504
buffer_load_dwordx2 v[44:45], v12, s[44:47], s21 offen offset:2048 sc1
v_mfma_f32_16x16x32_bf16 v[124:127], a[244:247], a[156:159], v[124:127]
ds_read_b64_tr_b16 a[238:239], v1 offset:23064
buffer_load_dwordx4 v[46:49], v0, s[44:47], s21 offen offset:2560 sc1
v_mfma_f32_16x16x32_bf16 v[108:111], a[248:251], a[152:155], v[108:111]
ds_read_b64_tr_b16 a[240:241], v1 offset:20512
buffer_load_dwordx4 v[50:53], v0, s[44:47], s21 offen offset:3584 sc1
v_mfma_f32_16x16x32_bf16 v[128:131], a[248:251], a[156:159], v[128:131]
ds_read_b64_tr_b16 a[242:243], v1 offset:23072
s_add_u32 s67, s21, 4096
v_mfma_f32_16x16x32_bf16 v[112:115], a[252:255], a[152:155], v[112:115]
ds_read_b64_tr_b16 a[244:245], v1 offset:25600
buffer_load_dwordx2 v[54:55], v12, s[44:47], s67 offen offset:512 sc1
v_mfma_f32_16x16x32_bf16 v[132:135], a[252:255], a[156:159], v[132:135]
ds_read_b64_tr_b16 a[246:247], v1 offset:28160
s_waitcnt lgkmcnt(4)
v_mfma_f32_16x16x32_bf16 v[96:99], a[224:227], v[192:195], v[96:99]
ds_read_b64_tr_b16 a[248:249], v1 offset:25608
buffer_load_dwordx4 v[56:59], v0, s[44:47], s67 offen offset:1024 sc1
v_mfma_f32_16x16x32_bf16 v[116:119], a[224:227], v[196:199], v[116:119]
ds_read_b64_tr_b16 a[250:251], v1 offset:28168
buffer_load_dwordx4 v[60:63], v0, s[44:47], s67 offen offset:2048 sc1
v_mfma_f32_16x16x32_bf16 v[100:103], a[228:231], v[192:195], v[100:103]
ds_read_b64_tr_b16 a[252:253], v1 offset:25616
buffer_load_dwordx2 v[64:65], v12, s[44:47], s67 offen offset:3072 sc1
v_mfma_f32_16x16x32_bf16 v[120:123], a[228:231], v[196:199], v[120:123]
ds_read_b64_tr_b16 a[254:255], v1 offset:28176
v_add_u32 v16, s61, v0
v_mfma_f32_16x16x32_bf16 v[104:107], a[232:235], v[192:195], v[104:107]
ds_read_b64_tr_b16 a[224:225], v1 offset:25624
v_add_u32 v17, s72, v0
v_mfma_f32_16x16x32_bf16 v[124:127], a[232:235], v[196:199], v[124:127]
ds_read_b64_tr_b16 a[226:227], v1 offset:28184
v_accvgpr_read_b32 v7, a180
v_mfma_f32_16x16x32_bf16 v[108:111], a[236:239], v[192:195], v[108:111]
ds_read_b64_tr_b16 a[228:229], v1 offset:25632
v_accvgpr_read_b32 v8, a182
v_mfma_f32_16x16x32_bf16 v[128:131], a[236:239], v[196:199], v[128:131]
ds_read_b64_tr_b16 a[230:231], v1 offset:28192
s_waitcnt lgkmcnt(4)
v_mfma_f32_16x16x32_bf16 v[112:115], a[240:243], v[192:195], v[112:115]
v_accvgpr_read_b32 v9, a184
v_add_u32 v18, 143360, v16
v_mfma_f32_16x16x32_bf16 v[132:135], a[240:243], v[196:199], v[132:135]
v_accvgpr_read_b32 v19, a206
v_add_u32 v19, 143360, v19
v_mfma_f32_16x16x32_bf16 v[96:99], a[244:247], v[200:203], v[96:99]
v_mfma_f32_16x16x32_bf16 v[116:119], a[244:247], v[204:207], v[116:119]
v_mfma_f32_16x16x32_bf16 v[100:103], a[248:251], v[200:203], v[100:103]
v_mfma_f32_16x16x32_bf16 v[120:123], a[248:251], v[204:207], v[120:123]
v_mfma_f32_16x16x32_bf16 v[104:107], a[252:255], v[200:203], v[104:107]
v_mfma_f32_16x16x32_bf16 v[124:127], a[252:255], v[204:207], v[124:127]
s_waitcnt lgkmcnt(0)
v_mfma_f32_16x16x32_bf16 v[108:111], a[224:227], v[200:203], v[108:111]
v_mfma_f32_16x16x32_bf16 v[128:131], a[224:227], v[204:207], v[128:131]
v_mfma_f32_16x16x32_bf16 v[112:115], a[228:231], v[200:203], v[112:115]
v_mfma_f32_16x16x32_bf16 v[132:135], a[228:231], v[204:207], v[132:135]
v_add_u32 v16, 102400, v16
v_add_u32 v17, 102400, v17
ds_write_b128 v16, v[116:119]
ds_write_b128 v16, v[120:123] offset:1024
ds_write_b128 v16, v[124:127] offset:2048
s_nop 0
ds_write_b128 v16, v[128:131] offset:3072
s_nop 0
ds_write_b128 v16, v[132:135] offset:4096
s_waitcnt vmcnt(8)
v_mfma_f32_16x16x16_bf16 v[96:99], a[222:223], v[36:37], v[96:99]
)ASM"
R"ASM(
v_mfma_f32_16x16x16_bf16 v[100:103], a[222:223], v[38:39], v[100:103]
s_waitcnt vmcnt(7)
v_mfma_f32_16x16x16_bf16 v[104:107], a[222:223], v[40:41], v[104:107]
v_mfma_f32_16x16x16_bf16 v[108:111], a[222:223], v[42:43], v[108:111]
s_waitcnt vmcnt(6)
v_mfma_f32_16x16x16_bf16 v[112:115], a[222:223], v[44:45], v[112:115]
s_waitcnt vmcnt(5)
v_mfma_f32_16x16x16_bf16 v[96:99], a[222:223], v[46:47], v[96:99]
v_mfma_f32_16x16x16_bf16 v[100:103], a[222:223], v[48:49], v[100:103]
s_waitcnt vmcnt(4)
v_mfma_f32_16x16x16_bf16 v[104:107], a[222:223], v[50:51], v[104:107]
v_mfma_f32_16x16x16_bf16 v[108:111], a[222:223], v[52:53], v[108:111]
s_waitcnt vmcnt(3)
v_mfma_f32_16x16x16_bf16 v[112:115], a[222:223], v[54:55], v[112:115]
s_waitcnt vmcnt(2)
v_mfma_f32_16x16x16_bf16 v[96:99], a[222:223], v[56:57], v[96:99]
v_mfma_f32_16x16x16_bf16 v[100:103], a[222:223], v[58:59], v[100:103]
s_waitcnt vmcnt(1)
v_mfma_f32_16x16x16_bf16 v[104:107], a[222:223], v[60:61], v[104:107]
v_mfma_f32_16x16x16_bf16 v[108:111], a[222:223], v[62:63], v[108:111]
s_waitcnt vmcnt(0)
v_mfma_f32_16x16x16_bf16 v[112:115], a[222:223], v[64:65], v[112:115]
s_waitcnt lgkmcnt(0)
s_barrier
ds_read_b128 v[136:139], v17
ds_read_b128 v[140:143], v17 offset:1024
ds_read_b128 v[144:147], v17 offset:2048
ds_read_b128 v[148:151], v17 offset:3072
ds_read_b128 v[152:155], v17 offset:4096
s_waitcnt lgkmcnt(4)
v_pk_add_f32 v[96:97], v[96:97], v[136:137]
v_pk_add_f32 v[98:99], v[98:99], v[138:139]
v_cvt_pk_bf16_f32 v176, v96, v97
v_cvt_pk_bf16_f32 v177, v98, v99
ds_write_b64 v19, v[176:177]
s_waitcnt lgkmcnt(4)
v_pk_add_f32 v[100:101], v[100:101], v[140:141]
v_pk_add_f32 v[102:103], v[102:103], v[142:143]
v_cvt_pk_bf16_f32 v178, v100, v101
v_cvt_pk_bf16_f32 v179, v102, v103
ds_write_b64 v19, v[178:179] offset:8
s_waitcnt lgkmcnt(4)
v_pk_add_f32 v[104:105], v[104:105], v[144:145]
v_pk_add_f32 v[106:107], v[106:107], v[146:147]
v_cvt_pk_bf16_f32 v180, v104, v105
v_cvt_pk_bf16_f32 v181, v106, v107
ds_write_b64 v19, v[180:181] offset:16
s_waitcnt lgkmcnt(4)
v_pk_add_f32 v[108:109], v[108:109], v[148:149]
v_pk_add_f32 v[110:111], v[110:111], v[150:151]
v_cvt_pk_bf16_f32 v182, v108, v109
v_cvt_pk_bf16_f32 v183, v110, v111
ds_write_b64 v19, v[182:183] offset:24
s_waitcnt lgkmcnt(4)
v_pk_add_f32 v[112:113], v[112:113], v[152:153]
v_pk_add_f32 v[114:115], v[114:115], v[154:155]
v_cvt_pk_bf16_f32 v184, v112, v113
v_cvt_pk_bf16_f32 v185, v114, v115
ds_write_b64 v19, v[184:185] offset:32
s_waitcnt lgkmcnt(0)
ds_read_b128 v[188:191], v18
ds_read_b128 v[192:195], v18 offset:1024
ds_read_b128 v[196:199], v18 offset:2048
s_waitcnt lgkmcnt(2)
buffer_store_dwordx4 v[188:191], v7, s[48:51], 0 offen
s_waitcnt lgkmcnt(1)
buffer_store_dwordx4 v[192:195], v8, s[48:51], 0 offen offset:1024
s_waitcnt lgkmcnt(0)
buffer_store_dwordx4 v[196:199], v9, s[48:51], 0 offen offset:2048
s_endpgm
.Lpk_1s:
s_mov_b32 s69, 65536
s_mov_b32 s65, 20709376
.Lpk_1t:
buffer_load_dword v5, v4, s[44:47], s65 offen sc1
s_waitcnt vmcnt(0)
v_cmp_ne_u32 vcc, s62, v5
s_cbranch_vccz .Lpk_1
s_sub_u32 s69, s69, 1
s_cbranch_scc0 .Lpk_1t
s_branch .Lpk_1
.Lpk_2s:
s_mov_b32 s69, 65536
s_mov_b32 s65, 20447232
.Lpk_2t:
buffer_load_dword v75, v74, s[44:47], s65 offen sc1
s_waitcnt vmcnt(0)
v_cmp_ne_u32 vcc, s62, v75
s_cbranch_vccz .Lpk_2
s_sub_u32 s69, s69, 1
s_cbranch_scc0 .Lpk_2t
s_branch .Lpk_2
.Lpk_3s:
s_mov_b32 s69, 65536
s_mov_b32 s65, 19660800
.Lpk_3t:
buffer_load_dword v227, v226, s[44:47], s65 offen sc1
s_waitcnt vmcnt(0)
v_cmp_ne_u32 vcc, s62, v227
s_cbranch_vccz .Lpk_3
s_sub_u32 s69, s69, 1
s_cbranch_scc0 .Lpk_3t
s_branch .Lpk_3
.Lpk_4s:
s_mov_b32 s69, 65536
s_mov_b32 s65, 19660800
.Lpk_4t:
buffer_load_dword v229, v228, s[44:47], s65 offen sc1
s_waitcnt vmcnt(0)
v_cmp_ne_u32 vcc, s62, v229
s_cbranch_vccz .Lpk_4
s_sub_u32 s69, s69, 1
s_cbranch_scc0 .Lpk_4t
s_branch .Lpk_4
.Lek_5s:
s_mov_b32 s69, 65536
v_lshlrev_b32 v170, 6, v0
.Lek_5t:
s_mov_b32 s65, 19660800
buffer_load_dword v166, v170, s[44:47], s65 offen sc1
s_mov_b32 s65, 19726336
buffer_load_dword v167, v170, s[44:47], s65 offen sc1
s_mov_b32 s65, 19791872
buffer_load_dword v168, v170, s[44:47], s65 offen sc1
s_mov_b32 s65, 19857408
buffer_load_dword v169, v170, s[44:47], s65 offen sc1
s_waitcnt vmcnt(0)
v_xor_b32 v166, s62, v166
v_xor_b32 v167, s62, v167
v_xor_b32 v168, s62, v168
v_xor_b32 v169, s62, v169
v_or3_b32 v166, v166, v167, v168
v_or_b32 v166, v166, v169
v_cmp_ne_u32 vcc, 0, v166
s_cbranch_vccz .Lek_5d
s_sub_u32 s69, s69, 1
s_cbranch_scc0 .Lek_5t
.Lek_5d:
buffer_store_dword v224, v225, s[44:47], 0 offen sc1
s_waitcnt vmcnt(0)
s_branch .Lek_5
.Lpk_6s:
s_mov_b32 s69, 65536
s_mov_b32 s65, 19922944
.Lpk_6t:
buffer_load_dword v231, v230, s[44:47], s65 offen sc1
s_waitcnt vmcnt(0)
v_cmp_ne_u32 vcc, s62, v231
s_cbranch_vccz .Lpk_6
s_sub_u32 s69, s69, 1
s_cbranch_scc0 .Lpk_6t
s_branch .Lpk_6
.Lpk_7s:
s_mov_b32 s69, 65536
s_mov_b32 s65, 20185088
.Lpk_7t:
buffer_load_dword v229, v228, s[44:47], s65 offen sc1
s_waitcnt vmcnt(0)
v_cmp_ne_u32 vcc, s62, v229
s_cbranch_vccz .Lpk_7
s_sub_u32 s69, s69, 1
s_cbranch_scc0 .Lpk_7t
s_branch .Lpk_7
.Lroleq:
s_waitcnt lgkmcnt(8)
v_mfma_f32_16x16x32_bf16 v[16:19], a[224:227], v[96:99], 0
ds_read_b64_tr_b16 a[248:249], v1 offset:46088
v_cndmask_b32 v64, 0, v72, s[4:5]
v_mfma_f32_16x16x32_bf16 v[36:39], a[224:227], v[100:103], 0
ds_read_b64_tr_b16 a[250:251], v1 offset:48648
v_cndmask_b32 v64, v64, v73, s[6:7]
v_mfma_f32_16x16x32_bf16 v[20:23], a[228:231], v[96:99], 0
ds_read_b64_tr_b16 a[252:253], v1 offset:46096
v_cndmask_b32 v64, v64, v74, s[8:9]
v_mfma_f32_16x16x32_bf16 v[40:43], a[228:231], v[100:103], 0
ds_read_b64_tr_b16 a[254:255], v1 offset:48656
s_waitcnt lgkmcnt(8)
v_mfma_f32_16x16x32_bf16 v[24:27], a[232:235], v[96:99], 0
ds_read_b64_tr_b16 a[224:225], v1 offset:46104
buffer_load_dwordx4 v9, s[28:31], s68 offen offset:2048 lds
v_mfma_f32_16x16x32_bf16 v[44:47], a[232:235], v[100:103], 0
ds_read_b64_tr_b16 a[226:227], v1 offset:48664
v_cndmask_b32 v64, v64, v75, s[10:11]
v_mfma_f32_16x16x32_bf16 v[28:31], a[236:239], v[96:99], 0
ds_read_b64_tr_b16 a[228:229], v1 offset:46112
v_cndmask_b32 v65, 0, v76, s[4:5]
v_mfma_f32_16x16x32_bf16 v[48:51], a[236:239], v[100:103], 0
ds_read_b64_tr_b16 a[230:231], v1 offset:48672
s_waitcnt lgkmcnt(4)
v_mfma_f32_16x16x32_bf16 v[32:35], a[240:243], v[96:99], 0
ds_read_b64_tr_b16 a[232:233], v1 offset:51200
v_cndmask_b32 v65, v65, v77, s[6:7]
v_mfma_f32_16x16x32_bf16 v[52:55], a[240:243], v[100:103], 0
ds_read_b64_tr_b16 a[234:235], v1 offset:53760
v_cndmask_b32 v65, v65, v78, s[8:9]
v_mfma_f32_16x16x32_bf16 v[16:19], a[244:247], v[104:107], v[16:19]
ds_read_b64_tr_b16 a[236:237], v1 offset:51208
v_cndmask_b32 v65, v65, v79, s[10:11]
v_mfma_f32_16x16x32_bf16 v[36:39], a[244:247], v[108:111], v[36:39]
ds_read_b64_tr_b16 a[238:239], v1 offset:53768
buffer_load_dwordx4 v10, s[28:31], s68 offen offset:3072 lds
v_mfma_f32_16x16x32_bf16 v[20:23], a[248:251], v[104:107], v[20:23]
ds_read_b64_tr_b16 a[240:241], v1 offset:51216
v_mov_b32 v66, v64
v_mfma_f32_16x16x32_bf16 v[40:43], a[248:251], v[108:111], v[40:43]
ds_read_b64_tr_b16 a[242:243], v1 offset:53776
v_mov_b32 v67, v65
v_mfma_f32_16x16x32_bf16 v[24:27], a[252:255], v[104:107], v[24:27]
ds_read_b64_tr_b16 a[244:245], v1 offset:51224
v_permlane32_swap_b32 v64, v66
v_mfma_f32_16x16x32_bf16 v[44:47], a[252:255], v[108:111], v[44:47]
ds_read_b64_tr_b16 a[246:247], v1 offset:53784
s_waitcnt lgkmcnt(4)
v_mfma_f32_16x16x32_bf16 v[28:31], a[224:227], v[104:107], v[28:31]
ds_read_b64_tr_b16 a[248:249], v1 offset:51232
v_permlane32_swap_b32 v65, v67
v_mfma_f32_16x16x32_bf16 v[48:51], a[224:227], v[108:111], v[48:51]
ds_read_b64_tr_b16 a[250:251], v1 offset:53792
s_add_u32 m0, s61, 106496
v_mfma_f32_16x16x32_bf16 v[32:35], a[228:231], v[104:107], v[32:35]
ds_read_b64_tr_b16 a[252:253], v1 offset:56320
buffer_load_dwordx4 v11, s[28:31], s68 offen lds
v_mfma_f32_16x16x32_bf16 v[52:55], a[228:231], v[108:111], v[52:55]
ds_read_b64_tr_b16 a[254:255], v1 offset:58880
v_add_f32 v64, v64, v66
v_mfma_f32_16x16x32_bf16 v[16:19], a[232:235], v[112:115], v[16:19]
ds_read_b64_tr_b16 a[224:225], v1 offset:56328
v_add_f32 v65, v65, v67
v_mfma_f32_16x16x32_bf16 v[36:39], a[232:235], v[116:119], v[36:39]
ds_read_b64_tr_b16 a[226:227], v1 offset:58888
v_mov_b32 v66, v64
v_mfma_f32_16x16x32_bf16 v[20:23], a[236:239], v[112:115], v[20:23]
ds_read_b64_tr_b16 a[228:229], v1 offset:56336
v_mov_b32 v67, v65
v_mfma_f32_16x16x32_bf16 v[40:43], a[236:239], v[116:119], v[40:43]
ds_read_b64_tr_b16 a[230:231], v1 offset:58896
s_waitcnt lgkmcnt(4)
v_mfma_f32_16x16x32_bf16 v[24:27], a[240:243], v[112:115], v[24:27]
ds_read_b64_tr_b16 a[232:233], v1 offset:56344
s_mul_i32 s68, s77, 122880
v_mfma_f32_16x16x32_bf16 v[44:47], a[240:243], v[116:119], v[44:47]
ds_read_b64_tr_b16 a[234:235], v1 offset:58904
s_waitcnt vmcnt(10)
v_mfma_f32_16x16x32_bf16 v[28:31], a[244:247], v[112:115], v[28:31]
ds_read_b64_tr_b16 a[236:237], v1 offset:56352
s_barrier
v_mfma_f32_16x16x32_bf16 v[48:51], a[244:247], v[116:119], v[48:51]
ds_read_b64_tr_b16 a[238:239], v1 offset:58912
s_add_u32 m0, s61, 122880
v_mfma_f32_16x16x32_bf16 v[32:35], a[248:251], v[112:115], v[32:35]
ds_read_b64_tr_b16 a[240:241], v2
buffer_load_dwordx4 v7, s[28:31], s68 offen lds
v_mfma_f32_16x16x32_bf16 v[52:55], a[248:251], v[116:119], v[52:55]
ds_read_b64_tr_b16 a[242:243], v2 offset:2560
buffer_load_dwordx4 v8, s[28:31], s68 offen offset:1024 lds
v_mfma_f32_16x16x32_bf16 v[16:19], a[252:255], v[120:123], v[16:19]
ds_read_b64_tr_b16 a[244:245], v2 offset:8
v_permlane16_swap_b32 v64, v66
v_mfma_f32_16x16x32_bf16 v[36:39], a[252:255], v[124:127], v[36:39]
ds_read_b64_tr_b16 a[246:247], v2 offset:2568
s_waitcnt lgkmcnt(4)
v_mfma_f32_16x16x32_bf16 v[20:23], a[224:227], v[120:123], v[20:23]
ds_read_b64_tr_b16 a[248:249], v2 offset:16
v_permlane16_swap_b32 v65, v67
v_mfma_f32_16x16x32_bf16 v[40:43], a[224:227], v[124:127], v[40:43]
ds_read_b64_tr_b16 a[250:251], v2 offset:2576
v_add_f32 v64, v64, v66
v_mfma_f32_16x16x32_bf16 v[24:27], a[228:231], v[120:123], v[24:27]
ds_read_b64_tr_b16 a[252:253], v2 offset:24
v_add_f32 v65, v65, v67
v_mfma_f32_16x16x32_bf16 v[44:47], a[228:231], v[124:127], v[44:47]
ds_read_b64_tr_b16 a[254:255], v2 offset:2584
v_mul_f32 v64, 986500301, v64
v_mfma_f32_16x16x32_bf16 v[28:31], a[232:235], v[120:123], v[28:31]
ds_read_b64_tr_b16 a[224:225], v2 offset:32
buffer_load_dwordx4 v9, s[28:31], s68 offen offset:2048 lds
v_mfma_f32_16x16x32_bf16 v[48:51], a[232:235], v[124:127], v[48:51]
ds_read_b64_tr_b16 a[226:227], v2 offset:2592
v_fma_f32 v64, -v80, v80, v64
v_mfma_f32_16x16x32_bf16 v[32:35], a[236:239], v[120:123], v[32:35]
ds_read_b64_tr_b16 a[228:229], v2 offset:5120
v_max_f32 v64, 0, v64
v_mfma_f32_16x16x32_bf16 v[52:55], a[236:239], v[124:127], v[52:55]
ds_read_b64_tr_b16 a[230:231], v2 offset:7680
s_waitcnt lgkmcnt(4)
v_mfma_f32_16x16x32_bf16 v[16:19], a[240:243], v[128:131], v[16:19]
ds_read_b64_tr_b16 a[232:233], v2 offset:5128
v_mul_f32 v65, 986500301, v65
v_mfma_f32_16x16x32_bf16 v[36:39], a[240:243], v[132:135], v[36:39]
ds_read_b64_tr_b16 a[234:235], v2 offset:7688
v_fma_f32 v65, -v81, v81, v65
v_mfma_f32_16x16x32_bf16 v[20:23], a[244:247], v[128:131], v[20:23]
ds_read_b64_tr_b16 a[236:237], v2 offset:5136
buffer_load_dwordx4 v10, s[28:31], s68 offen offset:3072 lds
v_mfma_f32_16x16x32_bf16 v[40:43], a[244:247], v[132:135], v[40:43]
ds_read_b64_tr_b16 a[238:239], v2 offset:7696
v_max_f32 v65, 0, v65
v_mfma_f32_16x16x32_bf16 v[24:27], a[248:251], v[128:131], v[24:27]
ds_read_b64_tr_b16 a[240:241], v2 offset:5144
v_cvt_pk_bf16_f32 v68, v64, v65
v_mfma_f32_16x16x32_bf16 v[44:47], a[248:251], v[132:135], v[44:47]
ds_read_b64_tr_b16 a[242:243], v2 offset:7704
v_lshlrev_b32 v64, 16, v68
v_mfma_f32_16x16x32_bf16 v[28:31], a[252:255], v[128:131], v[28:31]
ds_read_b64_tr_b16 a[244:245], v2 offset:5152
v_and_b32 v65, 4294901760, v68
v_mfma_f32_16x16x32_bf16 v[48:51], a[252:255], v[132:135], v[48:51]
ds_read_b64_tr_b16 a[246:247], v2 offset:7712
s_waitcnt lgkmcnt(4)
v_mfma_f32_16x16x32_bf16 v[32:35], a[224:227], v[128:131], v[32:35]
ds_read_b64_tr_b16 a[248:249], v2 offset:10240
v_add_f32 v64, 925353388, v64
v_mfma_f32_16x16x32_bf16 v[52:55], a[224:227], v[132:135], v[52:55]
ds_read_b64_tr_b16 a[250:251], v2 offset:12800
s_add_u32 m0, s61, 126976
v_mfma_f32_16x16x32_bf16 v[16:19], a[228:231], v[136:139], v[16:19]
ds_read_b64_tr_b16 a[252:253], v2 offset:10248
buffer_load_dwordx4 v11, s[28:31], s68 offen lds
v_mfma_f32_16x16x32_bf16 v[36:39], a[228:231], v[140:143], v[36:39]
ds_read_b64_tr_b16 a[254:255], v2 offset:12808
v_add_f32 v65, 925353388, v65
v_mfma_f32_16x16x32_bf16 v[20:23], a[232:235], v[136:139], v[20:23]
ds_read_b64_tr_b16 a[224:225], v2 offset:10256
v_cvt_pk_bf16_f32 v68, v64, v65
v_mfma_f32_16x16x32_bf16 v[40:43], a[232:235], v[140:143], v[40:43]
ds_read_b64_tr_b16 a[226:227], v2 offset:12816
v_lshlrev_b32 v64, 16, v68
v_mfma_f32_16x16x32_bf16 v[24:27], a[236:239], v[136:139], v[24:27]
ds_read_b64_tr_b16 a[228:229], v2 offset:10264
v_and_b32 v65, 4294901760, v68
v_mfma_f32_16x16x32_bf16 v[44:47], a[236:239], v[140:143], v[44:47]
ds_read_b64_tr_b16 a[230:231], v2 offset:12824
s_waitcnt lgkmcnt(4)
v_mfma_f32_16x16x32_bf16 v[28:31], a[240:243], v[136:139], v[28:31]
ds_read_b64_tr_b16 a[232:233], v2 offset:10272
v_accvgpr_read_b32 v7, a188
v_mfma_f32_16x16x32_bf16 v[48:51], a[240:243], v[140:143], v[48:51]
ds_read_b64_tr_b16 a[234:235], v2 offset:12832
v_add_u32 v8, 2016, v7
v_mfma_f32_16x16x32_bf16 v[32:35], a[244:247], v[136:139], v[32:35]
ds_read_b64_tr_b16 a[236:237], v2 offset:15360
v_add_u32 v9, 4032, v7
v_mfma_f32_16x16x32_bf16 v[52:55], a[244:247], v[140:143], v[52:55]
ds_read_b64_tr_b16 a[238:239], v2 offset:17920
v_add_u32 v10, 6048, v7
v_mfma_f32_16x16x32_bf16 v[16:19], a[248:251], v[144:147], v[16:19]
ds_read_b64_tr_b16 a[240:241], v2 offset:15368
v_accvgpr_read_b32 v12, a181
v_mfma_f32_16x16x32_bf16 v[36:39], a[248:251], v[148:151], v[36:39]
ds_read_b64_tr_b16 a[242:243], v2 offset:17928
buffer_load_dwordx4 a[100:103], v12, s[36:39], 0 offen nt
v_mfma_f32_16x16x32_bf16 v[20:23], a[252:255], v[144:147], v[20:23]
ds_read_b64_tr_b16 a[244:245], v2 offset:15376
v_accvgpr_read_b32 v12, a183
v_mfma_f32_16x16x32_bf16 v[40:43], a[252:255], v[148:151], v[40:43]
ds_read_b64_tr_b16 a[246:247], v2 offset:17936
s_waitcnt lgkmcnt(4)
v_mfma_f32_16x16x32_bf16 v[24:27], a[224:227], v[144:147], v[24:27]
ds_read_b64_tr_b16 a[248:249], v2 offset:15384
buffer_load_dwordx4 a[104:107], v12, s[36:39], 0 offen offset:1024 nt
v_mfma_f32_16x16x32_bf16 v[44:47], a[224:227], v[148:151], v[44:47]
ds_read_b64_tr_b16 a[250:251], v2 offset:17944
s_waitcnt vmcnt(12)
v_mfma_f32_16x16x32_bf16 v[28:31], a[228:231], v[144:147], v[28:31]
ds_read_b64_tr_b16 a[252:253], v2 offset:15392
s_barrier
v_mfma_f32_16x16x32_bf16 v[48:51], a[228:231], v[148:151], v[48:51]
ds_read_b64_tr_b16 a[254:255], v2 offset:17952
v_accvgpr_read_b32 v12, a185
v_mfma_f32_16x16x32_bf16 v[32:35], a[232:235], v[144:147], v[32:35]
ds_read_b64_tr_b16 a[224:225], v2 offset:20480
buffer_load_dwordx4 a[108:111], v12, s[36:39], 0 offen offset:2048 nt
v_mfma_f32_16x16x32_bf16 v[52:55], a[232:235], v[148:151], v[52:55]
ds_read_b64_tr_b16 a[226:227], v2 offset:23040
v_accvgpr_read_b32 v12, a186
v_mfma_f32_16x16x32_bf16 v[16:19], a[236:239], v[152:155], v[16:19]
ds_read_b64_tr_b16 a[228:229], v2 offset:20488
buffer_load_dwordx4 a[112:115], v12, s[36:39], 0 offen offset:3072 nt
v_mfma_f32_16x16x32_bf16 v[36:39], a[236:239], v[156:159], v[36:39]
ds_read_b64_tr_b16 a[230:231], v2 offset:23048
s_waitcnt lgkmcnt(4)
v_mfma_f32_16x16x32_bf16 v[20:23], a[240:243], v[152:155], v[20:23]
ds_read_b64_tr_b16 a[232:233], v2 offset:20496
v_accvgpr_read_b32 v12, a187
v_mfma_f32_16x16x32_bf16 v[40:43], a[240:243], v[156:159], v[40:43]
ds_read_b64_tr_b16 a[234:235], v2 offset:23056
buffer_load_dwordx4 a[116:119], v12, s[36:39], 0 offen nt
v_mfma_f32_16x16x32_bf16 v[24:27], a[244:247], v[152:155], v[24:27]
ds_read_b64_tr_b16 a[236:237], v2 offset:20504
v_accvgpr_read_b32 v12, a207
v_mfma_f32_16x16x32_bf16 v[44:47], a[244:247], v[156:159], v[44:47]
ds_read_b64_tr_b16 a[238:239], v2 offset:23064
buffer_load_dwordx4 a[170:173], v12, s[24:27], s55 offen
v_mfma_f32_16x16x32_bf16 v[28:31], a[248:251], v[152:155], v[28:31]
ds_read_b64_tr_b16 a[240:241], v2 offset:20512
v_sqrt_f32 v64, v64
v_mfma_f32_16x16x32_bf16 v[48:51], a[248:251], v[156:159], v[48:51]
ds_read_b64_tr_b16 a[242:243], v2 offset:23072
v_sqrt_f32 v65, v65
v_mfma_f32_16x16x32_bf16 v[32:35], a[252:255], v[152:155], v[32:35]
)ASM"
R"ASM(
ds_read_b64_tr_b16 a[244:245], v2 offset:25600
buffer_load_dwordx4 a[174:177], v12, s[24:27], s55 offen offset:16
v_mfma_f32_16x16x32_bf16 v[52:55], a[252:255], v[156:159], v[52:55]
ds_read_b64_tr_b16 a[246:247], v2 offset:28160
s_waitcnt lgkmcnt(4)
v_mfma_f32_16x16x32_bf16 v[16:19], a[224:227], v[160:163], v[16:19]
ds_read_b64_tr_b16 a[248:249], v2 offset:25608
v_cvt_pk_bf16_f32 v68, v64, v65
v_mfma_f32_16x16x32_bf16 v[36:39], a[224:227], v[164:167], v[36:39]
ds_read_b64_tr_b16 a[250:251], v2 offset:28168
v_lshlrev_b32 v64, 16, v68
v_mfma_f32_16x16x32_bf16 v[20:23], a[228:231], v[160:163], v[20:23]
ds_read_b64_tr_b16 a[252:253], v2 offset:25616
v_and_b32 v65, 4294901760, v68
v_mfma_f32_16x16x32_bf16 v[40:43], a[228:231], v[164:167], v[40:43]
ds_read_b64_tr_b16 a[254:255], v2 offset:28176
v_rcp_f32 v14, v64
v_mfma_f32_16x16x32_bf16 v[24:27], a[232:235], v[160:163], v[24:27]
ds_read_b64_tr_b16 a[224:225], v2 offset:25624
v_rcp_f32 v15, v65
v_mfma_f32_16x16x32_bf16 v[44:47], a[232:235], v[164:167], v[44:47]
ds_read_b64_tr_b16 a[226:227], v2 offset:28184
v_accvgpr_read_b32 v13, a190
v_mfma_f32_16x16x32_bf16 v[28:31], a[236:239], v[160:163], v[28:31]
ds_read_b64_tr_b16 a[228:229], v2 offset:25632
buffer_load_dwordx2 a[178:179], v12, s[24:27], s55 offen offset:32
v_mfma_f32_16x16x32_bf16 v[48:51], a[236:239], v[164:167], v[48:51]
ds_read_b64_tr_b16 a[230:231], v2 offset:28192
s_waitcnt lgkmcnt(4)
v_mfma_f32_16x16x32_bf16 v[32:35], a[240:243], v[160:163], v[32:35]
ds_read_b64_tr_b16 a[232:233], v2 offset:30720
ds_read_b64_tr_b16 a[0:1], v13
v_mfma_f32_16x16x32_bf16 v[52:55], a[240:243], v[164:167], v[52:55]
ds_read_b64_tr_b16 a[234:235], v2 offset:33280
ds_read_b64_tr_b16 a[2:3], v13 offset:160
v_mfma_f32_16x16x32_bf16 v[16:19], a[244:247], v[168:171], v[16:19]
ds_read_b64_tr_b16 a[236:237], v2 offset:30728
ds_read_b64_tr_b16 a[4:5], v13 offset:320
v_mfma_f32_16x16x32_bf16 v[36:39], a[244:247], v[172:175], v[36:39]
ds_read_b64_tr_b16 a[238:239], v2 offset:33288
ds_read_b64_tr_b16 a[6:7], v13 offset:480
v_mfma_f32_16x16x32_bf16 v[20:23], a[248:251], v[168:171], v[20:23]
ds_read_b64_tr_b16 a[240:241], v2 offset:30736
ds_read_b64_tr_b16 a[8:9], v13 offset:640
v_mfma_f32_16x16x32_bf16 v[40:43], a[248:251], v[172:175], v[40:43]
ds_read_b64_tr_b16 a[242:243], v2 offset:33296
s_waitcnt lgkmcnt(14)
ds_read_b64_tr_b16 a[10:11], v13 offset:8
v_mfma_f32_16x16x32_bf16 v[24:27], a[252:255], v[168:171], v[24:27]
s_waitcnt lgkmcnt(14)
ds_read_b64_tr_b16 a[244:245], v2 offset:30744
s_waitcnt lgkmcnt(14)
ds_read_b64_tr_b16 a[12:13], v13 offset:168
v_mfma_f32_16x16x32_bf16 v[44:47], a[252:255], v[172:175], v[44:47]
s_waitcnt lgkmcnt(14)
ds_read_b64_tr_b16 a[246:247], v2 offset:33304
s_waitcnt lgkmcnt(8)
v_mfma_f32_16x16x32_bf16 v[28:31], a[224:227], v[168:171], v[28:31]
ds_read_b64_tr_b16 a[248:249], v2 offset:30752
ds_read_b64_tr_b16 a[14:15], v13 offset:328
v_mfma_f32_16x16x32_bf16 v[48:51], a[224:227], v[172:175], v[48:51]
ds_read_b64_tr_b16 a[250:251], v2 offset:33312
ds_read_b64_tr_b16 a[16:17], v13 offset:488
v_mfma_f32_16x16x32_bf16 v[32:35], a[228:231], v[168:171], v[32:35]
ds_read_b64_tr_b16 a[252:253], v2 offset:35840
ds_read_b64_tr_b16 a[18:19], v13 offset:648
v_mfma_f32_16x16x32_bf16 v[52:55], a[228:231], v[172:175], v[52:55]
ds_read_b64_tr_b16 a[254:255], v2 offset:38400
s_waitcnt lgkmcnt(14)
ds_read_b64_tr_b16 a[20:21], v13 offset:16
v_mfma_f32_16x16x32_bf16 v[16:19], a[232:235], v[176:179], v[16:19]
s_waitcnt lgkmcnt(14)
ds_read_b64_tr_b16 a[224:225], v2 offset:35848
s_waitcnt lgkmcnt(14)
ds_read_b64_tr_b16 a[22:23], v13 offset:176
v_mfma_f32_16x16x32_bf16 v[36:39], a[232:235], v[180:183], v[36:39]
s_waitcnt lgkmcnt(14)
ds_read_b64_tr_b16 a[226:227], v2 offset:38408
s_waitcnt lgkmcnt(14)
ds_read_b64_tr_b16 a[24:25], v13 offset:336
v_mfma_f32_16x16x32_bf16 v[20:23], a[236:239], v[176:179], v[20:23]
s_waitcnt lgkmcnt(14)
ds_read_b64_tr_b16 a[228:229], v2 offset:35856
s_waitcnt lgkmcnt(14)
ds_read_b64_tr_b16 a[26:27], v13 offset:496
v_mfma_f32_16x16x32_bf16 v[40:43], a[236:239], v[180:183], v[40:43]
s_waitcnt lgkmcnt(14)
ds_read_b64_tr_b16 a[230:231], v2 offset:38416
s_waitcnt lgkmcnt(8)
v_mfma_f32_16x16x32_bf16 v[24:27], a[240:243], v[176:179], v[24:27]
ds_read_b64_tr_b16 a[232:233], v2 offset:35864
ds_read_b64_tr_b16 a[28:29], v13 offset:656
v_mfma_f32_16x16x32_bf16 v[44:47], a[240:243], v[180:183], v[44:47]
ds_read_b64_tr_b16 a[234:235], v2 offset:38424
s_waitcnt vmcnt(13)
v_mfma_f32_16x16x32_bf16 v[28:31], a[244:247], v[176:179], v[28:31]
ds_read_b64_tr_b16 a[236:237], v2 offset:35872
s_barrier
v_mfma_f32_16x16x32_bf16 v[48:51], a[244:247], v[180:183], v[48:51]
ds_read_b64_tr_b16 a[238:239], v2 offset:38432
ds_read_b64_tr_b16 a[30:31], v13 offset:24
v_mfma_f32_16x16x32_bf16 v[32:35], a[248:251], v[176:179], v[32:35]
ds_read_b64_tr_b16 a[240:241], v2 offset:40960
s_waitcnt lgkmcnt(14)
ds_read_b64_tr_b16 a[32:33], v13 offset:184
v_mfma_f32_16x16x32_bf16 v[52:55], a[248:251], v[180:183], v[52:55]
s_waitcnt lgkmcnt(14)
ds_read_b64_tr_b16 a[242:243], v2 offset:43520
s_waitcnt lgkmcnt(14)
ds_read_b64_tr_b16 a[34:35], v13 offset:344
v_mfma_f32_16x16x32_bf16 v[16:19], a[252:255], v[184:187], v[16:19]
s_waitcnt lgkmcnt(14)
ds_read_b64_tr_b16 a[244:245], v2 offset:40968
s_waitcnt lgkmcnt(14)
ds_read_b64_tr_b16 a[36:37], v13 offset:504
v_mfma_f32_16x16x32_bf16 v[36:39], a[252:255], v[188:191], v[36:39]
s_waitcnt lgkmcnt(14)
ds_read_b64_tr_b16 a[246:247], v2 offset:43528
s_waitcnt lgkmcnt(8)
v_mfma_f32_16x16x32_bf16 v[20:23], a[224:227], v[184:187], v[20:23]
ds_read_b64_tr_b16 a[248:249], v2 offset:40976
ds_read_b64_tr_b16 a[38:39], v13 offset:664
v_mfma_f32_16x16x32_bf16 v[40:43], a[224:227], v[188:191], v[40:43]
ds_read_b64_tr_b16 a[250:251], v2 offset:43536
ds_read_b64_tr_b16 a[40:41], v13 offset:32
v_mfma_f32_16x16x32_bf16 v[24:27], a[228:231], v[184:187], v[24:27]
ds_read_b64_tr_b16 a[252:253], v2 offset:40984
ds_read_b64_tr_b16 a[42:43], v13 offset:192
v_mfma_f32_16x16x32_bf16 v[44:47], a[228:231], v[188:191], v[44:47]
ds_read_b64_tr_b16 a[254:255], v2 offset:43544
s_waitcnt lgkmcnt(14)
ds_read_b64_tr_b16 a[44:45], v13 offset:352
v_mfma_f32_16x16x32_bf16 v[28:31], a[232:235], v[184:187], v[28:31]
s_waitcnt lgkmcnt(14)
ds_read_b64_tr_b16 a[224:225], v2 offset:40992
s_waitcnt lgkmcnt(14)
ds_read_b64_tr_b16 a[46:47], v13 offset:512
v_mfma_f32_16x16x32_bf16 v[48:51], a[232:235], v[188:191], v[48:51]
s_waitcnt lgkmcnt(14)
ds_read_b64_tr_b16 a[226:227], v2 offset:43552
s_waitcnt lgkmcnt(14)
ds_read_b64_tr_b16 a[48:49], v13 offset:672
v_mfma_f32_16x16x32_bf16 v[32:35], a[236:239], v[184:187], v[32:35]
s_waitcnt lgkmcnt(14)
ds_read_b64_tr_b16 a[228:229], v2 offset:46080
s_and_b32 s70, s56, 1
v_mfma_f32_16x16x32_bf16 v[52:55], a[236:239], v[188:191], v[52:55]
s_waitcnt lgkmcnt(14)
ds_read_b64_tr_b16 a[230:231], v2 offset:48640
s_waitcnt lgkmcnt(7)
v_mfma_f32_16x16x32_bf16 v[16:19], a[240:243], v[192:195], v[16:19]
ds_read_b64_tr_b16 a[232:233], v2 offset:46088
s_mul_i32 s68, s73, 98304
v_mfma_f32_16x16x32_bf16 v[36:39], a[240:243], v[196:199], v[36:39]
ds_read_b64_tr_b16 a[234:235], v2 offset:48648
s_add_u32 m0, s61, 0
v_mfma_f32_16x16x32_bf16 v[20:23], a[244:247], v[192:195], v[20:23]
ds_read_b64_tr_b16 a[236:237], v2 offset:46096
buffer_load_dwordx4 v7, s[32:35], s68 offen lds
v_mfma_f32_16x16x32_bf16 v[40:43], a[244:247], v[196:199], v[40:43]
ds_read_b64_tr_b16 a[238:239], v2 offset:48656
s_mul_i32 s70, s70, 10240
v_mfma_f32_16x16x32_bf16 v[24:27], a[248:251], v[192:195], v[24:27]
ds_read_b64_tr_b16 a[240:241], v2 offset:46104
buffer_load_dwordx4 v8, s[32:35], s68 offen offset:1056 lds
v_mfma_f32_16x16x32_bf16 v[44:47], a[248:251], v[196:199], v[44:47]
ds_read_b64_tr_b16 a[242:243], v2 offset:48664
s_xor_b32 s72, s56, 1
v_mfma_f32_16x16x32_bf16 v[28:31], a[252:255], v[192:195], v[28:31]
ds_read_b64_tr_b16 a[244:245], v2 offset:46112
s_mul_i32 s72, s72, 5120
v_mfma_f32_16x16x32_bf16 v[48:51], a[252:255], v[196:199], v[48:51]
ds_read_b64_tr_b16 a[246:247], v2 offset:48672
s_waitcnt lgkmcnt(4)
v_mfma_f32_16x16x32_bf16 v[32:35], a[224:227], v[192:195], v[32:35]
ds_read_b64_tr_b16 a[248:249], v2 offset:51200
buffer_load_dwordx4 v9, s[32:35], s68 offen offset:2112 lds
v_mfma_f32_16x16x32_bf16 v[52:55], a[224:227], v[196:199], v[52:55]
ds_read_b64_tr_b16 a[250:251], v2 offset:53760
v_mfma_f32_16x16x32_bf16 v[16:19], a[228:231], v[200:203], v[16:19]
ds_read_b64_tr_b16 a[252:253], v2 offset:51208
v_mfma_f32_16x16x32_bf16 v[36:39], a[228:231], v[204:207], v[36:39]
ds_read_b64_tr_b16 a[254:255], v2 offset:53768
buffer_load_dwordx4 v10, s[32:35], s68 offen offset:3168 lds
v_mfma_f32_16x16x32_bf16 v[20:23], a[232:235], v[200:203], v[20:23]
ds_read_b64_tr_b16 a[224:225], v2 offset:51216
v_mfma_f32_16x16x32_bf16 v[40:43], a[232:235], v[204:207], v[40:43]
ds_read_b64_tr_b16 a[226:227], v2 offset:53776
v_mfma_f32_16x16x32_bf16 v[24:27], a[236:239], v[200:203], v[24:27]
ds_read_b64_tr_b16 a[228:229], v2 offset:51224
v_mfma_f32_16x16x32_bf16 v[44:47], a[236:239], v[204:207], v[44:47]
ds_read_b64_tr_b16 a[230:231], v2 offset:53784
s_waitcnt lgkmcnt(4)
v_mfma_f32_16x16x32_bf16 v[28:31], a[240:243], v[200:203], v[28:31]
ds_read_b64_tr_b16 a[232:233], v2 offset:51232
v_mfma_f32_16x16x32_bf16 v[48:51], a[240:243], v[204:207], v[48:51]
ds_read_b64_tr_b16 a[234:235], v2 offset:53792
v_mfma_f32_16x16x32_bf16 v[32:35], a[244:247], v[200:203], v[32:35]
ds_read_b64_tr_b16 a[236:237], v2 offset:56320
v_mfma_f32_16x16x32_bf16 v[52:55], a[244:247], v[204:207], v[52:55]
ds_read_b64_tr_b16 a[238:239], v2 offset:58880
v_mfma_f32_16x16x32_bf16 v[16:19], a[248:251], v[208:211], v[16:19]
ds_read_b64_tr_b16 a[240:241], v2 offset:56328
v_mfma_f32_16x16x32_bf16 v[36:39], a[248:251], v[212:215], v[36:39]
ds_read_b64_tr_b16 a[242:243], v2 offset:58888
v_mfma_f32_16x16x32_bf16 v[20:23], a[252:255], v[208:211], v[20:23]
ds_read_b64_tr_b16 a[244:245], v2 offset:56336
v_mfma_f32_16x16x32_bf16 v[40:43], a[252:255], v[212:215], v[40:43]
ds_read_b64_tr_b16 a[246:247], v2 offset:58896
s_waitcnt lgkmcnt(4)
v_mfma_f32_16x16x32_bf16 v[24:27], a[224:227], v[208:211], v[24:27]
ds_read_b64_tr_b16 a[248:249], v2 offset:56344
v_mfma_f32_16x16x32_bf16 v[44:47], a[224:227], v[212:215], v[44:47]
ds_read_b64_tr_b16 a[250:251], v2 offset:58904
s_waitcnt vmcnt(12)
v_mfma_f32_16x16x32_bf16 v[28:31], a[228:231], v[208:211], v[28:31]
ds_read_b64_tr_b16 a[252:253], v2 offset:56352
s_barrier
v_mfma_f32_16x16x32_bf16 v[48:51], a[228:231], v[212:215], v[48:51]
ds_read_b64_tr_b16 a[254:255], v2 offset:58912
v_mfma_f32_16x16x32_bf16 v[32:35], a[232:235], v[208:211], v[32:35]
ds_read_b64_tr_b16 a[224:225], v3
v_mfma_f32_16x16x32_bf16 v[52:55], a[232:235], v[212:215], v[52:55]
ds_read_b64_tr_b16 a[226:227], v3 offset:2560
v_mfma_f32_16x16x32_bf16 v[16:19], a[236:239], v[216:219], v[16:19]
ds_read_b64_tr_b16 a[228:229], v3 offset:8
v_mfma_f32_16x16x32_bf16 v[36:39], a[236:239], v[220:223], v[36:39]
ds_read_b64_tr_b16 a[230:231], v3 offset:2568
s_waitcnt lgkmcnt(4)
v_mfma_f32_16x16x32_bf16 v[20:23], a[240:243], v[216:219], v[20:23]
ds_read_b64_tr_b16 a[232:233], v3 offset:16
v_mfma_f32_16x16x32_bf16 v[40:43], a[240:243], v[220:223], v[40:43]
ds_read_b64_tr_b16 a[234:235], v3 offset:2576
v_mfma_f32_16x16x32_bf16 v[24:27], a[244:247], v[216:219], v[24:27]
ds_read_b64_tr_b16 a[236:237], v3 offset:24
v_mfma_f32_16x16x32_bf16 v[44:47], a[244:247], v[220:223], v[44:47]
ds_read_b64_tr_b16 a[238:239], v3 offset:2584
v_mfma_f32_16x16x32_bf16 v[28:31], a[248:251], v[216:219], v[28:31]
ds_read_b64_tr_b16 a[240:241], v3 offset:32
v_mfma_f32_16x16x32_bf16 v[48:51], a[248:251], v[220:223], v[48:51]
ds_read_b64_tr_b16 a[242:243], v3 offset:2592
v_mfma_f32_16x16x32_bf16 v[32:35], a[252:255], v[216:219], v[32:35]
ds_read_b64_tr_b16 a[244:245], v3 offset:5120
v_mfma_f32_16x16x32_bf16 v[52:55], a[252:255], v[220:223], v[52:55]
ds_read_b64_tr_b16 a[246:247], v3 offset:7680
s_waitcnt lgkmcnt(4)
v_mfma_f32_16x16x32_bf16 v[16:19], a[224:227], v[224:227], v[16:19]
ds_read_b64_tr_b16 a[248:249], v3 offset:5128
v_mfma_f32_16x16x32_bf16 v[36:39], a[224:227], v[228:231], v[36:39]
ds_read_b64_tr_b16 a[250:251], v3 offset:7688
v_mfma_f32_16x16x32_bf16 v[20:23], a[228:231], v[224:227], v[20:23]
ds_read_b64_tr_b16 a[252:253], v3 offset:5136
v_mfma_f32_16x16x32_bf16 v[40:43], a[228:231], v[228:231], v[40:43]
ds_read_b64_tr_b16 a[254:255], v3 offset:7696
v_mfma_f32_16x16x32_bf16 v[24:27], a[232:235], v[224:227], v[24:27]
ds_read_b64_tr_b16 a[224:225], v3 offset:5144
v_mfma_f32_16x16x32_bf16 v[44:47], a[232:235], v[228:231], v[44:47]
ds_read_b64_tr_b16 a[226:227], v3 offset:7704
v_mfma_f32_16x16x32_bf16 v[28:31], a[236:239], v[224:227], v[28:31]
ds_read_b64_tr_b16 a[228:229], v3 offset:5152
v_mfma_f32_16x16x32_bf16 v[48:51], a[236:239], v[228:231], v[48:51]
ds_read_b64_tr_b16 a[230:231], v3 offset:7712
s_waitcnt lgkmcnt(4)
v_mfma_f32_16x16x32_bf16 v[32:35], a[240:243], v[224:227], v[32:35]
ds_read_b64_tr_b16 a[232:233], v3 offset:10240
v_mfma_f32_16x16x32_bf16 v[52:55], a[240:243], v[228:231], v[52:55]
ds_read_b64_tr_b16 a[234:235], v3 offset:12800
v_mfma_f32_16x16x32_bf16 v[16:19], a[244:247], v[232:235], v[16:19]
ds_read_b64_tr_b16 a[236:237], v3 offset:10248
v_mfma_f32_16x16x32_bf16 v[36:39], a[244:247], v[236:239], v[36:39]
ds_read_b64_tr_b16 a[238:239], v3 offset:12808
v_mfma_f32_16x16x32_bf16 v[20:23], a[248:251], v[232:235], v[20:23]
ds_read_b64_tr_b16 a[240:241], v3 offset:10256
v_mfma_f32_16x16x32_bf16 v[40:43], a[248:251], v[236:239], v[40:43]
ds_read_b64_tr_b16 a[242:243], v3 offset:12816
v_mfma_f32_16x16x32_bf16 v[24:27], a[252:255], v[232:235], v[24:27]
ds_read_b64_tr_b16 a[244:245], v3 offset:10264
s_mul_i32 s68, s74, 98304
v_mfma_f32_16x16x32_bf16 v[44:47], a[252:255], v[236:239], v[44:47]
ds_read_b64_tr_b16 a[246:247], v3 offset:12824
s_waitcnt lgkmcnt(4)
v_mfma_f32_16x16x32_bf16 v[28:31], a[224:227], v[232:235], v[28:31]
ds_read_b64_tr_b16 a[248:249], v3 offset:10272
s_add_u32 m0, s61, 20480
v_mfma_f32_16x16x32_bf16 v[48:51], a[224:227], v[236:239], v[48:51]
ds_read_b64_tr_b16 a[250:251], v3 offset:12832
buffer_load_dwordx4 v7, s[32:35], s68 offen lds
v_mfma_f32_16x16x32_bf16 v[32:35], a[228:231], v[232:235], v[32:35]
ds_read_b64_tr_b16 a[252:253], v3 offset:15360
buffer_load_dwordx4 v8, s[32:35], s68 offen offset:1056 lds
v_mfma_f32_16x16x32_bf16 v[52:55], a[228:231], v[236:239], v[52:55]
ds_read_b64_tr_b16 a[254:255], v3 offset:17920
v_mfma_f32_16x16x32_bf16 v[16:19], a[232:235], v[240:243], v[16:19]
ds_read_b64_tr_b16 a[224:225], v3 offset:15368
s_add_u32 s66, s3, 4096
v_mfma_f32_16x16x32_bf16 v[36:39], a[232:235], v[244:247], v[36:39]
ds_read_b64_tr_b16 a[226:227], v3 offset:17928
s_waitcnt vmcnt(9)
v_mfma_f32_16x16x32_bf16 v[20:23], a[236:239], v[240:243], v[20:23]
ds_read_b64_tr_b16 a[228:229], v3 offset:15376
buffer_store_dwordx4 a[100:103], v0, s[44:47], s3 offen sc1
v_mfma_f32_16x16x32_bf16 v[40:43], a[236:239], v[244:247], v[40:43]
ds_read_b64_tr_b16 a[230:231], v3 offset:17936
s_waitcnt lgkmcnt(4)
v_mfma_f32_16x16x32_bf16 v[24:27], a[240:243], v[240:243], v[24:27]
ds_read_b64_tr_b16 a[232:233], v3 offset:15384
buffer_store_dwordx4 a[104:107], v0, s[44:47], s3 offen offset:1024 sc1
v_mfma_f32_16x16x32_bf16 v[44:47], a[240:243], v[244:247], v[44:47]
ds_read_b64_tr_b16 a[234:235], v3 offset:17944
buffer_store_dwordx4 a[108:111], v0, s[44:47], s3 offen offset:2048 sc1
v_mfma_f32_16x16x32_bf16 v[28:31], a[244:247], v[240:243], v[28:31]
ds_read_b64_tr_b16 a[236:237], v3 offset:15392
buffer_store_dwordx4 a[112:115], v0, s[44:47], s3 offen offset:3072 sc1
v_mfma_f32_16x16x32_bf16 v[48:51], a[244:247], v[244:247], v[48:51]
ds_read_b64_tr_b16 a[238:239], v3 offset:17952
buffer_store_dwordx4 a[116:119], v0, s[44:47], s66 offen sc1
v_mfma_f32_16x16x32_bf16 v[32:35], a[248:251], v[240:243], v[32:35]
buffer_load_dwordx4 v9, s[32:35], s68 offen offset:2112 lds
buffer_load_dwordx4 v10, s[32:35], s68 offen offset:3168 lds
v_mfma_f32_16x16x32_bf16 v[52:55], a[248:251], v[244:247], v[52:55]
v_mfma_f32_16x16x32_bf16 v[16:19], a[252:255], v[248:251], v[16:19]
v_mfma_f32_16x16x32_bf16 v[36:39], a[252:255], v[252:255], v[36:39]
s_waitcnt lgkmcnt(0)
v_mfma_f32_16x16x32_bf16 v[20:23], a[224:227], v[248:251], v[20:23]
s_mul_i32 s68, s75, 98304
s_add_u32 m0, s61, 40960
v_mfma_f32_16x16x32_bf16 v[40:43], a[224:227], v[252:255], v[40:43]
buffer_load_dwordx4 v7, s[32:35], s68 offen lds
v_mfma_f32_16x16x32_bf16 v[24:27], a[228:231], v[248:251], v[24:27]
v_mfma_f32_16x16x32_bf16 v[44:47], a[228:231], v[252:255], v[44:47]
v_mfma_f32_16x16x32_bf16 v[28:31], a[232:235], v[248:251], v[28:31]
v_mfma_f32_16x16x32_bf16 v[48:51], a[232:235], v[252:255], v[48:51]
buffer_load_dwordx4 v8, s[32:35], s68 offen offset:1056 lds
v_mfma_f32_16x16x32_bf16 v[32:35], a[236:239], v[248:251], v[32:35]
v_mfma_f32_16x16x32_bf16 v[52:55], a[236:239], v[252:255], v[52:55]
s_barrier
v_pk_mul_f32 v[16:17], v[16:17], v[14:15] op_sel:[0,0] op_sel_hi:[1,0]
v_pk_mul_f32 v[18:19], v[18:19], v[14:15] op_sel:[0,0] op_sel_hi:[1,0]
v_pk_mul_f32 v[20:21], v[20:21], v[14:15] op_sel:[0,0] op_sel_hi:[1,0]
v_pk_mul_f32 v[22:23], v[22:23], v[14:15] op_sel:[0,0] op_sel_hi:[1,0]
v_pk_mul_f32 v[24:25], v[24:25], v[14:15] op_sel:[0,0] op_sel_hi:[1,0]
v_pk_mul_f32 v[26:27], v[26:27], v[14:15] op_sel:[0,0] op_sel_hi:[1,0]
v_pk_mul_f32 v[28:29], v[28:29], v[14:15] op_sel:[0,0] op_sel_hi:[1,0]
v_pk_mul_f32 v[30:31], v[30:31], v[14:15] op_sel:[0,0] op_sel_hi:[1,0]
v_pk_mul_f32 v[32:33], v[32:33], v[14:15] op_sel:[0,0] op_sel_hi:[1,0]
v_pk_mul_f32 v[34:35], v[34:35], v[14:15] op_sel:[0,0] op_sel_hi:[1,0]
v_cvt_pk_bf16_f32 v76, v16, v17
v_cvt_pk_bf16_f32 v77, v18, v19
v_cvt_pk_bf16_f32 v78, v20, v21
v_cvt_pk_bf16_f32 v79, v22, v23
v_cvt_pk_bf16_f32 v80, v24, v25
v_cvt_pk_bf16_f32 v81, v26, v27
v_cvt_pk_bf16_f32 v82, v28, v29
v_cvt_pk_bf16_f32 v83, v30, v31
v_cvt_pk_bf16_f32 v84, v32, v33
v_cvt_pk_bf16_f32 v85, v34, v35
v_mfma_f32_16x16x32_bf16 v[16:19], a[0:3], v[76:79], 0
v_mul_f32 v36, v15, v36
v_mul_f32 v37, v15, v37
v_mfma_f32_16x16x32_bf16 v[20:23], a[10:13], v[76:79], 0
v_cvt_pk_bf16_f32 v86, v36, v37
v_mul_f32 v38, v15, v38
v_mfma_f32_16x16x32_bf16 v[24:27], a[20:23], v[76:79], 0
v_mul_f32 v39, v15, v39
v_cvt_pk_bf16_f32 v87, v38, v39
v_mfma_f32_16x16x32_bf16 v[28:31], a[30:33], v[76:79], 0
v_mul_f32 v40, v15, v40
v_mul_f32 v41, v15, v41
v_mfma_f32_16x16x32_bf16 v[32:35], a[40:43], v[76:79], 0
v_cvt_pk_bf16_f32 v88, v40, v41
v_mul_f32 v42, v15, v42
v_mfma_f32_16x16x32_bf16 v[16:19], a[4:7], v[80:83], v[16:19]
v_mul_f32 v43, v15, v43
v_cvt_pk_bf16_f32 v89, v42, v43
v_mfma_f32_16x16x32_bf16 v[20:23], a[14:17], v[80:83], v[20:23]
v_mul_f32 v44, v15, v44
v_mul_f32 v45, v15, v45
v_mfma_f32_16x16x32_bf16 v[24:27], a[24:27], v[80:83], v[24:27]
v_cvt_pk_bf16_f32 v90, v44, v45
v_mul_f32 v46, v15, v46
v_mfma_f32_16x16x32_bf16 v[28:31], a[34:37], v[80:83], v[28:31]
v_mul_f32 v47, v15, v47
v_cvt_pk_bf16_f32 v91, v46, v47
v_mfma_f32_16x16x32_bf16 v[32:35], a[44:47], v[80:83], v[32:35]
v_mul_f32 v48, v15, v48
v_mul_f32 v49, v15, v49
v_mfma_f32_16x16x16_bf16 v[16:19], a[8:9], v[84:85], v[16:19]
v_cvt_pk_bf16_f32 v92, v48, v49
v_mul_f32 v50, v15, v50
v_mfma_f32_16x16x16_bf16 v[20:23], a[18:19], v[84:85], v[20:23]
v_mul_f32 v51, v15, v51
v_cvt_pk_bf16_f32 v93, v50, v51
v_mfma_f32_16x16x16_bf16 v[24:27], a[28:29], v[84:85], v[24:27]
v_mul_f32 v52, v15, v52
v_mul_f32 v53, v15, v53
v_mfma_f32_16x16x16_bf16 v[28:31], a[38:39], v[84:85], v[28:31]
v_cvt_pk_bf16_f32 v94, v52, v53
v_mul_f32 v54, v15, v54
v_mfma_f32_16x16x16_bf16 v[32:35], a[48:49], v[84:85], v[32:35]
v_mul_f32 v55, v15, v55
)ASM"
R"ASM(
v_cvt_pk_bf16_f32 v95, v54, v55
v_mfma_f32_16x16x32_bf16 v[36:39], a[0:3], v[86:89], 0
v_mfma_f32_16x16x32_bf16 v[40:43], a[10:13], v[86:89], 0
v_mfma_f32_16x16x32_bf16 v[44:47], a[20:23], v[86:89], 0
v_mfma_f32_16x16x32_bf16 v[48:51], a[30:33], v[86:89], 0
v_max3_f32 v4, v16, v17, v18
v_min3_f32 v5, v16, v17, v18
v_mfma_f32_16x16x32_bf16 v[52:55], a[40:43], v[86:89], 0
v_max3_f32 v4, v4, v19, v20
v_min3_f32 v5, v5, v19, v20
v_mfma_f32_16x16x32_bf16 v[36:39], a[4:7], v[90:93], v[36:39]
v_max3_f32 v4, v4, v21, v22
v_min3_f32 v5, v5, v21, v22
v_mfma_f32_16x16x32_bf16 v[40:43], a[14:17], v[90:93], v[40:43]
v_max3_f32 v4, v4, v23, v24
v_min3_f32 v5, v5, v23, v24
v_mfma_f32_16x16x32_bf16 v[44:47], a[24:27], v[90:93], v[44:47]
v_max3_f32 v4, v4, v25, v26
v_min3_f32 v5, v5, v25, v26
v_mfma_f32_16x16x32_bf16 v[48:51], a[34:37], v[90:93], v[48:51]
v_max3_f32 v4, v4, v27, v28
v_min3_f32 v5, v5, v27, v28
v_mfma_f32_16x16x32_bf16 v[52:55], a[44:47], v[90:93], v[52:55]
v_max3_f32 v4, v4, v29, v30
v_min3_f32 v5, v5, v29, v30
v_mfma_f32_16x16x16_bf16 v[36:39], a[8:9], v[94:95], v[36:39]
v_max3_f32 v4, v4, v31, v32
v_min3_f32 v5, v5, v31, v32
v_mfma_f32_16x16x16_bf16 v[40:43], a[18:19], v[94:95], v[40:43]
v_max3_f32 v4, v4, v33, v34
v_min3_f32 v5, v5, v33, v34
v_mfma_f32_16x16x16_bf16 v[44:47], a[28:29], v[94:95], v[44:47]
v_max_f32 v4, v4, v35
v_min_f32 v5, v5, v35
v_mfma_f32_16x16x16_bf16 v[48:51], a[38:39], v[94:95], v[48:51]
v_mul_f32 v4, s13, v4
v_mul_f32 v5, s12, v5
v_mfma_f32_16x16x16_bf16 v[52:55], a[48:49], v[94:95], v[52:55]
v_pk_fma_f32 v[56:57], v[16:17], s[12:13], v[4:5] op_sel:[0,0,0] op_sel_hi:[1,0,0]
v_pk_fma_f32 v[16:17], v[16:17], s[12:13], v[4:5] op_sel:[0,1,1] op_sel_hi:[1,1,1]
v_pk_fma_f32 v[58:59], v[18:19], s[12:13], v[4:5] op_sel:[0,0,0] op_sel_hi:[1,0,0]
v_pk_fma_f32 v[18:19], v[18:19], s[12:13], v[4:5] op_sel:[0,1,1] op_sel_hi:[1,1,1]
v_pk_fma_f32 v[60:61], v[20:21], s[12:13], v[4:5] op_sel:[0,0,0] op_sel_hi:[1,0,0]
v_pk_fma_f32 v[20:21], v[20:21], s[12:13], v[4:5] op_sel:[0,1,1] op_sel_hi:[1,1,1]
v_pk_fma_f32 v[62:63], v[22:23], s[12:13], v[4:5] op_sel:[0,0,0] op_sel_hi:[1,0,0]
v_pk_fma_f32 v[22:23], v[22:23], s[12:13], v[4:5] op_sel:[0,1,1] op_sel_hi:[1,1,1]
v_pk_fma_f32 v[64:65], v[24:25], s[12:13], v[4:5] op_sel:[0,0,0] op_sel_hi:[1,0,0]
v_pk_fma_f32 v[24:25], v[24:25], s[12:13], v[4:5] op_sel:[0,1,1] op_sel_hi:[1,1,1]
v_pk_fma_f32 v[66:67], v[26:27], s[12:13], v[4:5] op_sel:[0,0,0] op_sel_hi:[1,0,0]
v_pk_fma_f32 v[26:27], v[26:27], s[12:13], v[4:5] op_sel:[0,1,1] op_sel_hi:[1,1,1]
v_pk_fma_f32 v[68:69], v[28:29], s[12:13], v[4:5] op_sel:[0,0,0] op_sel_hi:[1,0,0]
v_pk_fma_f32 v[28:29], v[28:29], s[12:13], v[4:5] op_sel:[0,1,1] op_sel_hi:[1,1,1]
v_pk_fma_f32 v[70:71], v[30:31], s[12:13], v[4:5] op_sel:[0,0,0] op_sel_hi:[1,0,0]
v_pk_fma_f32 v[30:31], v[30:31], s[12:13], v[4:5] op_sel:[0,1,1] op_sel_hi:[1,1,1]
v_pk_fma_f32 v[72:73], v[32:33], s[12:13], v[4:5] op_sel:[0,0,0] op_sel_hi:[1,0,0]
v_pk_fma_f32 v[32:33], v[32:33], s[12:13], v[4:5] op_sel:[0,1,1] op_sel_hi:[1,1,1]
v_pk_fma_f32 v[74:75], v[34:35], s[12:13], v[4:5] op_sel:[0,0,0] op_sel_hi:[1,0,0]
v_pk_fma_f32 v[34:35], v[34:35], s[12:13], v[4:5] op_sel:[0,1,1] op_sel_hi:[1,1,1]
buffer_load_dwordx4 v9, s[32:35], s68 offen offset:2112 lds
v_exp_f32 v56, v56
v_exp_f32 v16, v16
buffer_load_dwordx4 v10, s[32:35], s68 offen offset:3168 lds
v_exp_f32 v57, v57
v_exp_f32 v17, v17
v_exp_f32 v58, v58
v_exp_f32 v18, v18
v_exp_f32 v59, v59
v_exp_f32 v19, v19
s_mul_i32 s68, s76, 98304
s_add_u32 m0, s61, 61440
s_nop 0
buffer_load_dwordx4 v7, s[32:35], s68 offen lds
v_exp_f32 v60, v60
v_exp_f32 v20, v20
v_exp_f32 v61, v61
v_exp_f32 v21, v21
v_exp_f32 v62, v62
v_exp_f32 v22, v22
buffer_load_dwordx4 v8, s[32:35], s68 offen offset:1056 lds
v_exp_f32 v63, v63
v_exp_f32 v23, v23
v_exp_f32 v64, v64
v_exp_f32 v24, v24
v_exp_f32 v65, v65
v_exp_f32 v25, v25
buffer_load_dwordx4 v9, s[32:35], s68 offen offset:2112 lds
v_exp_f32 v66, v66
v_exp_f32 v26, v26
v_exp_f32 v67, v67
v_exp_f32 v27, v27
v_exp_f32 v68, v68
v_exp_f32 v28, v28
v_exp_f32 v69, v69
v_exp_f32 v29, v29
buffer_load_dwordx4 v10, s[32:35], s68 offen offset:3168 lds
v_exp_f32 v70, v70
v_exp_f32 v30, v30
v_exp_f32 v71, v71
v_exp_f32 v31, v31
v_exp_f32 v72, v72
v_exp_f32 v32, v32
v_exp_f32 v73, v73
v_exp_f32 v33, v33
s_mul_i32 s68, s77, 98304
s_add_u32 m0, s61, 81920
s_nop 0
buffer_load_dwordx4 v7, s[32:35], s68 offen lds
v_exp_f32 v74, v74
v_exp_f32 v34, v34
v_exp_f32 v75, v75
v_exp_f32 v35, v35
v_pk_add_f32 v[76:77], v[56:57], v[58:59]
v_pk_add_f32 v[78:79], v[16:17], v[18:19]
v_pk_add_f32 v[76:77], v[76:77], v[60:61]
v_pk_add_f32 v[78:79], v[78:79], v[20:21]
v_pk_add_f32 v[76:77], v[76:77], v[62:63]
v_pk_add_f32 v[78:79], v[78:79], v[22:23]
v_pk_add_f32 v[76:77], v[76:77], v[64:65]
v_pk_add_f32 v[78:79], v[78:79], v[24:25]
v_pk_add_f32 v[76:77], v[76:77], v[66:67]
v_pk_add_f32 v[78:79], v[78:79], v[26:27]
v_pk_add_f32 v[76:77], v[76:77], v[68:69]
v_pk_add_f32 v[78:79], v[78:79], v[28:29]
v_pk_add_f32 v[76:77], v[76:77], v[70:71]
v_pk_add_f32 v[78:79], v[78:79], v[30:31]
v_pk_add_f32 v[76:77], v[76:77], v[72:73]
v_pk_add_f32 v[78:79], v[78:79], v[32:33]
v_pk_add_f32 v[76:77], v[76:77], v[74:75]
v_pk_add_f32 v[78:79], v[78:79], v[34:35]
v_add_f32 v4, v76, v77
v_add_f32 v5, v78, v79
v_rcp_f32 v4, v4
v_rcp_f32 v5, v5
buffer_load_dwordx4 v8, s[32:35], s68 offen offset:1056 lds
v_pk_mul_f32 v[56:57], v[56:57], v[4:5] op_sel:[0,0] op_sel_hi:[1,0]
v_pk_mul_f32 v[16:17], v[16:17], v[4:5] op_sel:[0,1] op_sel_hi:[1,1]
v_pk_mul_f32 v[58:59], v[58:59], v[4:5] op_sel:[0,0] op_sel_hi:[1,0]
v_pk_mul_f32 v[18:19], v[18:19], v[4:5] op_sel:[0,1] op_sel_hi:[1,1]
v_pk_mul_f32 v[60:61], v[60:61], v[4:5] op_sel:[0,0] op_sel_hi:[1,0]
v_pk_mul_f32 v[20:21], v[20:21], v[4:5] op_sel:[0,1] op_sel_hi:[1,1]
v_pk_mul_f32 v[62:63], v[62:63], v[4:5] op_sel:[0,0] op_sel_hi:[1,0]
v_pk_mul_f32 v[22:23], v[22:23], v[4:5] op_sel:[0,1] op_sel_hi:[1,1]
v_pk_mul_f32 v[64:65], v[64:65], v[4:5] op_sel:[0,0] op_sel_hi:[1,0]
v_pk_mul_f32 v[24:25], v[24:25], v[4:5] op_sel:[0,1] op_sel_hi:[1,1]
v_pk_mul_f32 v[66:67], v[66:67], v[4:5] op_sel:[0,0] op_sel_hi:[1,0]
v_pk_mul_f32 v[26:27], v[26:27], v[4:5] op_sel:[0,1] op_sel_hi:[1,1]
v_pk_mul_f32 v[68:69], v[68:69], v[4:5] op_sel:[0,0] op_sel_hi:[1,0]
v_pk_mul_f32 v[28:29], v[28:29], v[4:5] op_sel:[0,1] op_sel_hi:[1,1]
v_pk_mul_f32 v[70:71], v[70:71], v[4:5] op_sel:[0,0] op_sel_hi:[1,0]
v_pk_mul_f32 v[30:31], v[30:31], v[4:5] op_sel:[0,1] op_sel_hi:[1,1]
v_pk_mul_f32 v[72:73], v[72:73], v[4:5] op_sel:[0,0] op_sel_hi:[1,0]
v_pk_mul_f32 v[32:33], v[32:33], v[4:5] op_sel:[0,1] op_sel_hi:[1,1]
v_pk_mul_f32 v[74:75], v[74:75], v[4:5] op_sel:[0,0] op_sel_hi:[1,0]
v_pk_mul_f32 v[34:35], v[34:35], v[4:5] op_sel:[0,1] op_sel_hi:[1,1]
buffer_load_dwordx4 v9, s[32:35], s68 offen offset:2112 lds
v_cvt_pk_bf16_f32 v76, v56, v57
v_cvt_pk_bf16_f32 v77, v58, v59
v_cvt_pk_bf16_f32 v78, v60, v61
v_cvt_pk_bf16_f32 v79, v62, v63
v_cvt_pk_bf16_f32 v80, v64, v65
v_cvt_pk_bf16_f32 v81, v66, v67
v_cvt_pk_bf16_f32 v82, v68, v69
v_cvt_pk_bf16_f32 v83, v70, v71
v_cvt_pk_bf16_f32 v84, v72, v73
v_cvt_pk_bf16_f32 v85, v74, v75
v_cvt_pk_bf16_f32 v86, v16, v17
v_cvt_pk_bf16_f32 v87, v18, v19
v_cvt_pk_bf16_f32 v88, v20, v21
v_cvt_pk_bf16_f32 v89, v22, v23
v_cvt_pk_bf16_f32 v90, v24, v25
v_cvt_pk_bf16_f32 v91, v26, v27
v_cvt_pk_bf16_f32 v92, v28, v29
v_cvt_pk_bf16_f32 v93, v30, v31
v_cvt_pk_bf16_f32 v94, v32, v33
v_cvt_pk_bf16_f32 v95, v34, v35
buffer_load_dwordx4 v10, s[32:35], s68 offen offset:3168 lds
v_mov_b32 v12, s62
v_lshrrev_b32 v13, 2, v0
s_add_u32 s65, s64, 1310720
s_waitcnt vmcnt(14)
buffer_store_dword v12, v13, s[44:47], s65 offen sc1
v_accvgpr_read_b32 v4, a189
v_add_u32 v5, 61440, v4
v_add_u32 v6, 122880, v4
v_max3_f32 v12, v36, v37, v38
v_min3_f32 v13, v36, v37, v38
v_max3_f32 v12, v12, v39, v40
v_min3_f32 v13, v13, v39, v40
v_max3_f32 v12, v12, v41, v42
v_min3_f32 v13, v13, v41, v42
v_max3_f32 v12, v12, v43, v44
v_min3_f32 v13, v13, v43, v44
v_max3_f32 v12, v12, v45, v46
v_min3_f32 v13, v13, v45, v46
v_max3_f32 v12, v12, v47, v48
v_min3_f32 v13, v13, v47, v48
v_max3_f32 v12, v12, v49, v50
v_min3_f32 v13, v13, v49, v50
v_max3_f32 v12, v12, v51, v52
v_min3_f32 v13, v13, v51, v52
v_max3_f32 v12, v12, v53, v54
v_min3_f32 v13, v13, v53, v54
v_max_f32 v12, v12, v55
v_min_f32 v13, v13, v55
v_mul_f32 v12, s13, v12
v_mul_f32 v13, s12, v13
v_pk_fma_f32 v[56:57], v[36:37], s[12:13], v[12:13] op_sel:[0,0,0] op_sel_hi:[1,0,0]
v_pk_fma_f32 v[36:37], v[36:37], s[12:13], v[12:13] op_sel:[0,1,1] op_sel_hi:[1,1,1]
v_pk_fma_f32 v[58:59], v[38:39], s[12:13], v[12:13] op_sel:[0,0,0] op_sel_hi:[1,0,0]
v_pk_fma_f32 v[38:39], v[38:39], s[12:13], v[12:13] op_sel:[0,1,1] op_sel_hi:[1,1,1]
v_pk_fma_f32 v[60:61], v[40:41], s[12:13], v[12:13] op_sel:[0,0,0] op_sel_hi:[1,0,0]
v_pk_fma_f32 v[40:41], v[40:41], s[12:13], v[12:13] op_sel:[0,1,1] op_sel_hi:[1,1,1]
v_pk_fma_f32 v[62:63], v[42:43], s[12:13], v[12:13] op_sel:[0,0,0] op_sel_hi:[1,0,0]
v_pk_fma_f32 v[42:43], v[42:43], s[12:13], v[12:13] op_sel:[0,1,1] op_sel_hi:[1,1,1]
v_pk_fma_f32 v[64:65], v[44:45], s[12:13], v[12:13] op_sel:[0,0,0] op_sel_hi:[1,0,0]
v_pk_fma_f32 v[44:45], v[44:45], s[12:13], v[12:13] op_sel:[0,1,1] op_sel_hi:[1,1,1]
v_pk_fma_f32 v[66:67], v[46:47], s[12:13], v[12:13] op_sel:[0,0,0] op_sel_hi:[1,0,0]
v_pk_fma_f32 v[46:47], v[46:47], s[12:13], v[12:13] op_sel:[0,1,1] op_sel_hi:[1,1,1]
v_pk_fma_f32 v[68:69], v[48:49], s[12:13], v[12:13] op_sel:[0,0,0] op_sel_hi:[1,0,0]
v_pk_fma_f32 v[48:49], v[48:49], s[12:13], v[12:13] op_sel:[0,1,1] op_sel_hi:[1,1,1]
v_pk_fma_f32 v[70:71], v[50:51], s[12:13], v[12:13] op_sel:[0,0,0] op_sel_hi:[1,0,0]
v_pk_fma_f32 v[50:51], v[50:51], s[12:13], v[12:13] op_sel:[0,1,1] op_sel_hi:[1,1,1]
v_pk_fma_f32 v[72:73], v[52:53], s[12:13], v[12:13] op_sel:[0,0,0] op_sel_hi:[1,0,0]
v_pk_fma_f32 v[52:53], v[52:53], s[12:13], v[12:13] op_sel:[0,1,1] op_sel_hi:[1,1,1]
v_pk_fma_f32 v[74:75], v[54:55], s[12:13], v[12:13] op_sel:[0,0,0] op_sel_hi:[1,0,0]
v_pk_fma_f32 v[54:55], v[54:55], s[12:13], v[12:13] op_sel:[0,1,1] op_sel_hi:[1,1,1]
s_mul_i32 s68, s73, 98304
s_add_u32 s68, s68, 128
s_add_u32 m0, s61, 102400
s_nop 0
buffer_load_dwordx4 v7, s[32:35], s68 offen lds
s_barrier
ds_read_b64_tr_b16 a[224:225], v4
ds_read_b64_tr_b16 a[226:227], v4 offset:512
ds_read_b64_tr_b16 a[228:229], v4 offset:32
ds_read_b64_tr_b16 a[230:231], v4 offset:544
ds_read_b64_tr_b16 a[232:233], v4 offset:64
ds_read_b64_tr_b16 a[234:235], v4 offset:576
ds_read_b64_tr_b16 a[236:237], v4 offset:96
ds_read_b64_tr_b16 a[238:239], v4 offset:608
ds_read_b64_tr_b16 a[240:241], v4 offset:5120
ds_read_b64_tr_b16 a[242:243], v4 offset:5632
ds_read_b64_tr_b16 a[244:245], v4 offset:5152
ds_read_b64_tr_b16 a[246:247], v4 offset:5664
v_exp_f32 v56, v56
v_exp_f32 v36, v36
buffer_load_dwordx4 v8, s[32:35], s68 offen offset:1056 lds
v_exp_f32 v57, v57
v_exp_f32 v37, v37
buffer_load_dwordx4 v9, s[32:35], s68 offen offset:2112 lds
v_exp_f32 v58, v58
v_exp_f32 v38, v38
v_exp_f32 v59, v59
v_exp_f32 v39, v39
v_exp_f32 v60, v60
v_exp_f32 v40, v40
buffer_load_dwordx4 v10, s[32:35], s68 offen offset:3168 lds
v_exp_f32 v61, v61
v_exp_f32 v41, v41
v_exp_f32 v62, v62
v_exp_f32 v42, v42
v_exp_f32 v63, v63
v_exp_f32 v43, v43
v_exp_f32 v64, v64
v_exp_f32 v44, v44
v_exp_f32 v65, v65
v_exp_f32 v45, v45
v_exp_f32 v66, v66
v_exp_f32 v46, v46
v_exp_f32 v67, v67
v_exp_f32 v47, v47
v_exp_f32 v68, v68
v_exp_f32 v48, v48
v_exp_f32 v69, v69
v_exp_f32 v49, v49
v_exp_f32 v70, v70
v_exp_f32 v50, v50
v_exp_f32 v71, v71
v_exp_f32 v51, v51
v_exp_f32 v72, v72
v_exp_f32 v52, v52
v_exp_f32 v73, v73
v_exp_f32 v53, v53
v_exp_f32 v74, v74
v_exp_f32 v54, v54
v_exp_f32 v75, v75
v_exp_f32 v55, v55
v_pk_add_f32 v[16:17], v[56:57], v[58:59]
v_pk_add_f32 v[18:19], v[36:37], v[38:39]
v_pk_add_f32 v[16:17], v[16:17], v[60:61]
v_pk_add_f32 v[18:19], v[18:19], v[40:41]
v_pk_add_f32 v[16:17], v[16:17], v[62:63]
v_pk_add_f32 v[18:19], v[18:19], v[42:43]
v_pk_add_f32 v[16:17], v[16:17], v[64:65]
v_pk_add_f32 v[18:19], v[18:19], v[44:45]
v_pk_add_f32 v[16:17], v[16:17], v[66:67]
v_pk_add_f32 v[18:19], v[18:19], v[46:47]
v_pk_add_f32 v[16:17], v[16:17], v[68:69]
v_pk_add_f32 v[18:19], v[18:19], v[48:49]
v_pk_add_f32 v[16:17], v[16:17], v[70:71]
v_pk_add_f32 v[18:19], v[18:19], v[50:51]
v_pk_add_f32 v[16:17], v[16:17], v[72:73]
v_pk_add_f32 v[18:19], v[18:19], v[52:53]
v_pk_add_f32 v[16:17], v[16:17], v[74:75]
v_pk_add_f32 v[18:19], v[18:19], v[54:55]
v_add_f32 v12, v16, v17
v_add_f32 v13, v18, v19
v_rcp_f32 v12, v12
v_rcp_f32 v13, v13
s_nop 0
v_pk_mul_f32 v[56:57], v[56:57], v[12:13] op_sel:[0,0] op_sel_hi:[1,0]
v_pk_mul_f32 v[36:37], v[36:37], v[12:13] op_sel:[0,1] op_sel_hi:[1,1]
v_pk_mul_f32 v[58:59], v[58:59], v[12:13] op_sel:[0,0] op_sel_hi:[1,0]
v_pk_mul_f32 v[38:39], v[38:39], v[12:13] op_sel:[0,1] op_sel_hi:[1,1]
v_pk_mul_f32 v[60:61], v[60:61], v[12:13] op_sel:[0,0] op_sel_hi:[1,0]
v_pk_mul_f32 v[40:41], v[40:41], v[12:13] op_sel:[0,1] op_sel_hi:[1,1]
v_pk_mul_f32 v[62:63], v[62:63], v[12:13] op_sel:[0,0] op_sel_hi:[1,0]
v_pk_mul_f32 v[42:43], v[42:43], v[12:13] op_sel:[0,1] op_sel_hi:[1,1]
v_pk_mul_f32 v[64:65], v[64:65], v[12:13] op_sel:[0,0] op_sel_hi:[1,0]
v_pk_mul_f32 v[44:45], v[44:45], v[12:13] op_sel:[0,1] op_sel_hi:[1,1]
v_pk_mul_f32 v[66:67], v[66:67], v[12:13] op_sel:[0,0] op_sel_hi:[1,0]
v_pk_mul_f32 v[46:47], v[46:47], v[12:13] op_sel:[0,1] op_sel_hi:[1,1]
v_pk_mul_f32 v[68:69], v[68:69], v[12:13] op_sel:[0,0] op_sel_hi:[1,0]
v_pk_mul_f32 v[48:49], v[48:49], v[12:13] op_sel:[0,1] op_sel_hi:[1,1]
v_pk_mul_f32 v[70:71], v[70:71], v[12:13] op_sel:[0,0] op_sel_hi:[1,0]
v_pk_mul_f32 v[50:51], v[50:51], v[12:13] op_sel:[0,1] op_sel_hi:[1,1]
v_pk_mul_f32 v[72:73], v[72:73], v[12:13] op_sel:[0,0] op_sel_hi:[1,0]
v_pk_mul_f32 v[52:53], v[52:53], v[12:13] op_sel:[0,1] op_sel_hi:[1,1]
v_pk_mul_f32 v[74:75], v[74:75], v[12:13] op_sel:[0,0] op_sel_hi:[1,0]
v_pk_mul_f32 v[54:55], v[54:55], v[12:13] op_sel:[0,1] op_sel_hi:[1,1]
v_cvt_pk_bf16_f32 v16, v56, v57
v_cvt_pk_bf16_f32 v17, v58, v59
v_cvt_pk_bf16_f32 v18, v60, v61
v_cvt_pk_bf16_f32 v19, v62, v63
v_cvt_pk_bf16_f32 v20, v64, v65
v_cvt_pk_bf16_f32 v21, v66, v67
v_cvt_pk_bf16_f32 v22, v68, v69
v_cvt_pk_bf16_f32 v23, v70, v71
v_cvt_pk_bf16_f32 v24, v72, v73
v_cvt_pk_bf16_f32 v25, v74, v75
v_cvt_pk_bf16_f32 v26, v36, v37
v_cvt_pk_bf16_f32 v27, v38, v39
v_cvt_pk_bf16_f32 v28, v40, v41
v_cvt_pk_bf16_f32 v29, v42, v43
v_cvt_pk_bf16_f32 v30, v44, v45
v_cvt_pk_bf16_f32 v31, v46, v47
v_cvt_pk_bf16_f32 v32, v48, v49
v_cvt_pk_bf16_f32 v33, v50, v51
v_cvt_pk_bf16_f32 v34, v52, v53
v_cvt_pk_bf16_f32 v35, v54, v55
s_waitcnt lgkmcnt(8)
v_mfma_f32_16x16x32_bf16 v[36:39], a[224:227], v[96:99], 0
ds_read_b64_tr_b16 a[248:249], v4 offset:5184
v_accvgpr_write_b32 a120, v76
v_mfma_f32_16x16x32_bf16 v[56:59], a[224:227], v[100:103], 0
ds_read_b64_tr_b16 a[250:251], v4 offset:5696
v_accvgpr_write_b32 a121, v77
v_mfma_f32_16x16x32_bf16 v[40:43], a[228:231], v[96:99], 0
ds_read_b64_tr_b16 a[252:253], v4 offset:5216
v_accvgpr_write_b32 a122, v78
v_mfma_f32_16x16x32_bf16 v[60:63], a[228:231], v[100:103], 0
ds_read_b64_tr_b16 a[254:255], v4 offset:5728
s_waitcnt lgkmcnt(8)
v_mfma_f32_16x16x32_bf16 v[44:47], a[232:235], v[96:99], 0
ds_read_b64_tr_b16 a[224:225], v4 offset:10240
v_accvgpr_write_b32 a123, v79
v_mfma_f32_16x16x32_bf16 v[64:67], a[232:235], v[100:103], 0
ds_read_b64_tr_b16 a[226:227], v4 offset:10752
v_accvgpr_write_b32 a124, v80
v_mfma_f32_16x16x32_bf16 v[48:51], a[236:239], v[96:99], 0
ds_read_b64_tr_b16 a[228:229], v4 offset:10272
v_accvgpr_write_b32 a125, v81
v_mfma_f32_16x16x32_bf16 v[68:71], a[236:239], v[100:103], 0
ds_read_b64_tr_b16 a[230:231], v4 offset:10784
s_waitcnt lgkmcnt(4)
v_mfma_f32_16x16x32_bf16 v[36:39], a[240:243], v[104:107], v[36:39]
ds_read_b64_tr_b16 a[232:233], v4 offset:10304
v_accvgpr_write_b32 a126, v82
v_mfma_f32_16x16x32_bf16 v[56:59], a[240:243], v[108:111], v[56:59]
ds_read_b64_tr_b16 a[234:235], v4 offset:10816
v_accvgpr_write_b32 a127, v83
v_mfma_f32_16x16x32_bf16 v[40:43], a[244:247], v[104:107], v[40:43]
ds_read_b64_tr_b16 a[236:237], v4 offset:10336
s_mul_i32 s68, s74, 98304
v_mfma_f32_16x16x32_bf16 v[60:63], a[244:247], v[108:111], v[60:63]
ds_read_b64_tr_b16 a[238:239], v4 offset:10848
s_add_u32 s68, s68, 128
v_mfma_f32_16x16x32_bf16 v[44:47], a[248:251], v[104:107], v[44:47]
ds_read_b64_tr_b16 a[240:241], v4 offset:15360
s_add_u32 m0, s61, 122880
v_mfma_f32_16x16x32_bf16 v[64:67], a[248:251], v[108:111], v[64:67]
ds_read_b64_tr_b16 a[242:243], v4 offset:15872
buffer_load_dwordx4 v7, s[32:35], s68 offen lds
v_mfma_f32_16x16x32_bf16 v[48:51], a[252:255], v[104:107], v[48:51]
ds_read_b64_tr_b16 a[244:245], v4 offset:15392
buffer_load_dwordx4 v8, s[32:35], s68 offen offset:1056 lds
v_mfma_f32_16x16x32_bf16 v[68:71], a[252:255], v[108:111], v[68:71]
ds_read_b64_tr_b16 a[246:247], v4 offset:15904
s_waitcnt lgkmcnt(4)
v_mfma_f32_16x16x32_bf16 v[36:39], a[224:227], v[112:115], v[36:39]
)ASM"
R"ASM(
ds_read_b64_tr_b16 a[248:249], v4 offset:15424
v_accvgpr_write_b32 a128, v84
v_mfma_f32_16x16x32_bf16 v[56:59], a[224:227], v[116:119], v[56:59]
ds_read_b64_tr_b16 a[250:251], v4 offset:15936
s_waitcnt vmcnt(19)
v_mfma_f32_16x16x32_bf16 v[40:43], a[228:231], v[112:115], v[40:43]
ds_read_b64_tr_b16 a[252:253], v4 offset:15456
s_barrier
v_mfma_f32_16x16x32_bf16 v[60:63], a[228:231], v[116:119], v[60:63]
ds_read_b64_tr_b16 a[254:255], v4 offset:15968
buffer_load_dwordx4 v9, s[32:35], s68 offen offset:2112 lds
v_mfma_f32_16x16x32_bf16 v[44:47], a[232:235], v[112:115], v[44:47]
ds_read_b64_tr_b16 a[224:225], v4 offset:20480
v_accvgpr_write_b32 a129, v85
v_mfma_f32_16x16x32_bf16 v[64:67], a[232:235], v[116:119], v[64:67]
ds_read_b64_tr_b16 a[226:227], v4 offset:20992
buffer_load_dwordx4 v10, s[32:35], s68 offen offset:3168 lds
v_mfma_f32_16x16x32_bf16 v[48:51], a[236:239], v[112:115], v[48:51]
ds_read_b64_tr_b16 a[228:229], v4 offset:20512
v_accvgpr_write_b32 a130, v86
v_mfma_f32_16x16x32_bf16 v[68:71], a[236:239], v[116:119], v[68:71]
ds_read_b64_tr_b16 a[230:231], v4 offset:21024
s_waitcnt lgkmcnt(4)
v_mfma_f32_16x16x32_bf16 v[36:39], a[240:243], v[120:123], v[36:39]
ds_read_b64_tr_b16 a[232:233], v4 offset:20544
v_accvgpr_write_b32 a131, v87
v_mfma_f32_16x16x32_bf16 v[56:59], a[240:243], v[124:127], v[56:59]
ds_read_b64_tr_b16 a[234:235], v4 offset:21056
v_accvgpr_write_b32 a132, v88
v_mfma_f32_16x16x32_bf16 v[40:43], a[244:247], v[120:123], v[40:43]
ds_read_b64_tr_b16 a[236:237], v4 offset:20576
v_accvgpr_write_b32 a133, v89
v_mfma_f32_16x16x32_bf16 v[60:63], a[244:247], v[124:127], v[60:63]
ds_read_b64_tr_b16 a[238:239], v4 offset:21088
v_accvgpr_write_b32 a134, v90
v_mfma_f32_16x16x32_bf16 v[44:47], a[248:251], v[120:123], v[44:47]
ds_read_b64_tr_b16 a[240:241], v4 offset:25600
v_accvgpr_write_b32 a135, v91
v_mfma_f32_16x16x32_bf16 v[64:67], a[248:251], v[124:127], v[64:67]
ds_read_b64_tr_b16 a[242:243], v4 offset:26112
v_accvgpr_write_b32 a136, v92
v_mfma_f32_16x16x32_bf16 v[48:51], a[252:255], v[120:123], v[48:51]
ds_read_b64_tr_b16 a[244:245], v4 offset:25632
v_accvgpr_write_b32 a137, v93
v_mfma_f32_16x16x32_bf16 v[68:71], a[252:255], v[124:127], v[68:71]
ds_read_b64_tr_b16 a[246:247], v4 offset:26144
s_waitcnt lgkmcnt(4)
v_mfma_f32_16x16x32_bf16 v[36:39], a[224:227], v[128:131], v[36:39]
ds_read_b64_tr_b16 a[248:249], v4 offset:25664
v_accvgpr_write_b32 a138, v94
v_mfma_f32_16x16x32_bf16 v[56:59], a[224:227], v[132:135], v[56:59]
ds_read_b64_tr_b16 a[250:251], v4 offset:26176
v_accvgpr_write_b32 a139, v95
v_mfma_f32_16x16x32_bf16 v[40:43], a[228:231], v[128:131], v[40:43]
ds_read_b64_tr_b16 a[252:253], v4 offset:25696
v_accvgpr_write_b32 a140, v16
v_mfma_f32_16x16x32_bf16 v[60:63], a[228:231], v[132:135], v[60:63]
ds_read_b64_tr_b16 a[254:255], v4 offset:26208
v_accvgpr_write_b32 a141, v17
v_mfma_f32_16x16x32_bf16 v[44:47], a[232:235], v[128:131], v[44:47]
ds_read_b64_tr_b16 a[224:225], v4 offset:30720
v_accvgpr_write_b32 a142, v18
v_mfma_f32_16x16x32_bf16 v[64:67], a[232:235], v[132:135], v[64:67]
ds_read_b64_tr_b16 a[226:227], v4 offset:31232
v_accvgpr_write_b32 a143, v19
v_mfma_f32_16x16x32_bf16 v[48:51], a[236:239], v[128:131], v[48:51]
ds_read_b64_tr_b16 a[228:229], v4 offset:30752
v_accvgpr_write_b32 a144, v20
v_mfma_f32_16x16x32_bf16 v[68:71], a[236:239], v[132:135], v[68:71]
ds_read_b64_tr_b16 a[230:231], v4 offset:31264
s_waitcnt lgkmcnt(4)
v_mfma_f32_16x16x32_bf16 v[36:39], a[240:243], v[136:139], v[36:39]
ds_read_b64_tr_b16 a[232:233], v4 offset:30784
v_accvgpr_write_b32 a145, v21
v_mfma_f32_16x16x32_bf16 v[56:59], a[240:243], v[140:143], v[56:59]
ds_read_b64_tr_b16 a[234:235], v4 offset:31296
v_accvgpr_write_b32 a146, v22
v_mfma_f32_16x16x32_bf16 v[40:43], a[244:247], v[136:139], v[40:43]
ds_read_b64_tr_b16 a[236:237], v4 offset:30816
v_accvgpr_write_b32 a147, v23
v_mfma_f32_16x16x32_bf16 v[60:63], a[244:247], v[140:143], v[60:63]
ds_read_b64_tr_b16 a[238:239], v4 offset:31328
v_accvgpr_write_b32 a148, v24
v_mfma_f32_16x16x32_bf16 v[44:47], a[248:251], v[136:139], v[44:47]
ds_read_b64_tr_b16 a[240:241], v4 offset:35840
v_accvgpr_write_b32 a149, v25
v_mfma_f32_16x16x32_bf16 v[64:67], a[248:251], v[140:143], v[64:67]
ds_read_b64_tr_b16 a[242:243], v4 offset:36352
v_accvgpr_write_b32 a150, v26
v_mfma_f32_16x16x32_bf16 v[48:51], a[252:255], v[136:139], v[48:51]
ds_read_b64_tr_b16 a[244:245], v4 offset:35872
v_accvgpr_write_b32 a151, v27
v_mfma_f32_16x16x32_bf16 v[68:71], a[252:255], v[140:143], v[68:71]
ds_read_b64_tr_b16 a[246:247], v4 offset:36384
s_waitcnt lgkmcnt(4)
v_mfma_f32_16x16x32_bf16 v[36:39], a[224:227], v[144:147], v[36:39]
ds_read_b64_tr_b16 a[248:249], v4 offset:35904
v_accvgpr_write_b32 a152, v28
v_mfma_f32_16x16x32_bf16 v[56:59], a[224:227], v[148:151], v[56:59]
ds_read_b64_tr_b16 a[250:251], v4 offset:36416
s_waitcnt vmcnt(17)
v_mfma_f32_16x16x32_bf16 v[40:43], a[228:231], v[144:147], v[40:43]
ds_read_b64_tr_b16 a[252:253], v4 offset:35936
s_barrier
v_mfma_f32_16x16x32_bf16 v[60:63], a[228:231], v[148:151], v[60:63]
ds_read_b64_tr_b16 a[254:255], v4 offset:36448
s_mul_i32 s68, s75, 98304
v_mfma_f32_16x16x32_bf16 v[44:47], a[232:235], v[144:147], v[44:47]
ds_read_b64_tr_b16 a[224:225], v4 offset:40960
s_add_u32 s68, s68, 128
v_mfma_f32_16x16x32_bf16 v[64:67], a[232:235], v[148:151], v[64:67]
ds_read_b64_tr_b16 a[226:227], v4 offset:41472
s_add_u32 m0, s61, 0
v_mfma_f32_16x16x32_bf16 v[48:51], a[236:239], v[144:147], v[48:51]
ds_read_b64_tr_b16 a[228:229], v4 offset:40992
buffer_load_dwordx4 v7, s[32:35], s68 offen lds
v_mfma_f32_16x16x32_bf16 v[68:71], a[236:239], v[148:151], v[68:71]
ds_read_b64_tr_b16 a[230:231], v4 offset:41504
s_waitcnt lgkmcnt(4)
v_mfma_f32_16x16x32_bf16 v[36:39], a[240:243], v[152:155], v[36:39]
ds_read_b64_tr_b16 a[232:233], v4 offset:41024
buffer_load_dwordx4 v8, s[32:35], s68 offen offset:1056 lds
v_mfma_f32_16x16x32_bf16 v[56:59], a[240:243], v[156:159], v[56:59]
ds_read_b64_tr_b16 a[234:235], v4 offset:41536
buffer_load_dwordx4 v9, s[32:35], s68 offen offset:2112 lds
v_mfma_f32_16x16x32_bf16 v[40:43], a[244:247], v[152:155], v[40:43]
ds_read_b64_tr_b16 a[236:237], v4 offset:41056
v_accvgpr_write_b32 a153, v29
v_mfma_f32_16x16x32_bf16 v[60:63], a[244:247], v[156:159], v[60:63]
ds_read_b64_tr_b16 a[238:239], v4 offset:41568
v_accvgpr_write_b32 a154, v30
v_mfma_f32_16x16x32_bf16 v[44:47], a[248:251], v[152:155], v[44:47]
ds_read_b64_tr_b16 a[240:241], v4 offset:46080
buffer_load_dwordx4 v10, s[32:35], s68 offen offset:3168 lds
v_mfma_f32_16x16x32_bf16 v[64:67], a[248:251], v[156:159], v[64:67]
ds_read_b64_tr_b16 a[242:243], v4 offset:46592
v_accvgpr_write_b32 a155, v31
v_mfma_f32_16x16x32_bf16 v[48:51], a[252:255], v[152:155], v[48:51]
ds_read_b64_tr_b16 a[244:245], v4 offset:46112
v_accvgpr_write_b32 a156, v32
v_mfma_f32_16x16x32_bf16 v[68:71], a[252:255], v[156:159], v[68:71]
ds_read_b64_tr_b16 a[246:247], v4 offset:46624
s_waitcnt lgkmcnt(4)
v_mfma_f32_16x16x32_bf16 v[36:39], a[224:227], v[160:163], v[36:39]
ds_read_b64_tr_b16 a[248:249], v4 offset:46144
v_accvgpr_write_b32 a157, v33
v_mfma_f32_16x16x32_bf16 v[56:59], a[224:227], v[164:167], v[56:59]
ds_read_b64_tr_b16 a[250:251], v4 offset:46656
v_accvgpr_write_b32 a158, v34
v_mfma_f32_16x16x32_bf16 v[40:43], a[228:231], v[160:163], v[40:43]
ds_read_b64_tr_b16 a[252:253], v4 offset:46176
v_accvgpr_write_b32 a159, v35
v_mfma_f32_16x16x32_bf16 v[60:63], a[228:231], v[164:167], v[60:63]
ds_read_b64_tr_b16 a[254:255], v4 offset:46688
v_mfma_f32_16x16x32_bf16 v[44:47], a[232:235], v[160:163], v[44:47]
ds_read_b64_tr_b16 a[224:225], v4 offset:51200
v_mfma_f32_16x16x32_bf16 v[64:67], a[232:235], v[164:167], v[64:67]
ds_read_b64_tr_b16 a[226:227], v4 offset:51712
v_mfma_f32_16x16x32_bf16 v[48:51], a[236:239], v[160:163], v[48:51]
ds_read_b64_tr_b16 a[228:229], v4 offset:51232
v_mfma_f32_16x16x32_bf16 v[68:71], a[236:239], v[164:167], v[68:71]
ds_read_b64_tr_b16 a[230:231], v4 offset:51744
s_waitcnt lgkmcnt(4)
v_mfma_f32_16x16x32_bf16 v[36:39], a[240:243], v[168:171], v[36:39]
ds_read_b64_tr_b16 a[232:233], v4 offset:51264
v_mfma_f32_16x16x32_bf16 v[56:59], a[240:243], v[172:175], v[56:59]
ds_read_b64_tr_b16 a[234:235], v4 offset:51776
v_mfma_f32_16x16x32_bf16 v[40:43], a[244:247], v[168:171], v[40:43]
ds_read_b64_tr_b16 a[236:237], v4 offset:51296
v_mfma_f32_16x16x32_bf16 v[60:63], a[244:247], v[172:175], v[60:63]
ds_read_b64_tr_b16 a[238:239], v4 offset:51808
v_mfma_f32_16x16x32_bf16 v[44:47], a[248:251], v[168:171], v[44:47]
ds_read_b64_tr_b16 a[240:241], v4 offset:56320
v_mfma_f32_16x16x32_bf16 v[64:67], a[248:251], v[172:175], v[64:67]
ds_read_b64_tr_b16 a[242:243], v4 offset:56832
v_mfma_f32_16x16x32_bf16 v[48:51], a[252:255], v[168:171], v[48:51]
ds_read_b64_tr_b16 a[244:245], v4 offset:56352
v_mfma_f32_16x16x32_bf16 v[68:71], a[252:255], v[172:175], v[68:71]
ds_read_b64_tr_b16 a[246:247], v4 offset:56864
s_waitcnt lgkmcnt(4)
v_mfma_f32_16x16x32_bf16 v[36:39], a[224:227], v[176:179], v[36:39]
ds_read_b64_tr_b16 a[248:249], v4 offset:56384
v_mfma_f32_16x16x32_bf16 v[56:59], a[224:227], v[180:183], v[56:59]
ds_read_b64_tr_b16 a[250:251], v4 offset:56896
s_waitcnt vmcnt(17)
v_mfma_f32_16x16x32_bf16 v[40:43], a[228:231], v[176:179], v[40:43]
ds_read_b64_tr_b16 a[252:253], v4 offset:56416
s_barrier
v_mfma_f32_16x16x32_bf16 v[60:63], a[228:231], v[180:183], v[60:63]
ds_read_b64_tr_b16 a[254:255], v4 offset:56928
s_mul_i32 s68, s76, 98304
v_mfma_f32_16x16x32_bf16 v[44:47], a[232:235], v[176:179], v[44:47]
ds_read_b64_tr_b16 a[224:225], v5
s_add_u32 s68, s68, 128
v_mfma_f32_16x16x32_bf16 v[64:67], a[232:235], v[180:183], v[64:67]
ds_read_b64_tr_b16 a[226:227], v5 offset:512
s_add_u32 m0, s61, 20480
v_mfma_f32_16x16x32_bf16 v[48:51], a[236:239], v[176:179], v[48:51]
ds_read_b64_tr_b16 a[228:229], v5 offset:32
buffer_load_dwordx4 v7, s[32:35], s68 offen lds
v_mfma_f32_16x16x32_bf16 v[68:71], a[236:239], v[180:183], v[68:71]
ds_read_b64_tr_b16 a[230:231], v5 offset:544
s_waitcnt lgkmcnt(4)
v_mfma_f32_16x16x32_bf16 v[36:39], a[240:243], v[184:187], v[36:39]
ds_read_b64_tr_b16 a[232:233], v5 offset:64
buffer_load_dwordx4 v8, s[32:35], s68 offen offset:1056 lds
v_mfma_f32_16x16x32_bf16 v[56:59], a[240:243], v[188:191], v[56:59]
ds_read_b64_tr_b16 a[234:235], v5 offset:576
buffer_load_dwordx4 v9, s[32:35], s68 offen offset:2112 lds
v_mfma_f32_16x16x32_bf16 v[40:43], a[244:247], v[184:187], v[40:43]
ds_read_b64_tr_b16 a[236:237], v5 offset:96
v_mfma_f32_16x16x32_bf16 v[60:63], a[244:247], v[188:191], v[60:63]
ds_read_b64_tr_b16 a[238:239], v5 offset:608
v_mfma_f32_16x16x32_bf16 v[44:47], a[248:251], v[184:187], v[44:47]
ds_read_b64_tr_b16 a[240:241], v5 offset:5120
v_accvgpr_read_b32 v76, a213
v_mfma_f32_16x16x32_bf16 v[64:67], a[248:251], v[188:191], v[64:67]
ds_read_b64_tr_b16 a[242:243], v5 offset:5632
s_mov_b32 s65, 20709376
v_mfma_f32_16x16x32_bf16 v[48:51], a[252:255], v[184:187], v[48:51]
ds_read_b64_tr_b16 a[244:245], v5 offset:5152
buffer_load_dword v77, v76, s[44:47], s65 offen sc1
v_mfma_f32_16x16x32_bf16 v[68:71], a[252:255], v[188:191], v[68:71]
ds_read_b64_tr_b16 a[246:247], v5 offset:5664
s_waitcnt lgkmcnt(4)
v_mfma_f32_16x16x32_bf16 v[36:39], a[224:227], v[192:195], v[36:39]
ds_read_b64_tr_b16 a[248:249], v5 offset:5184
buffer_load_dwordx4 v10, s[32:35], s68 offen offset:3168 lds
v_mfma_f32_16x16x32_bf16 v[56:59], a[224:227], v[196:199], v[56:59]
ds_read_b64_tr_b16 a[250:251], v5 offset:5696
v_mfma_f32_16x16x32_bf16 v[40:43], a[228:231], v[192:195], v[40:43]
ds_read_b64_tr_b16 a[252:253], v5 offset:5216
v_mfma_f32_16x16x32_bf16 v[60:63], a[228:231], v[196:199], v[60:63]
ds_read_b64_tr_b16 a[254:255], v5 offset:5728
v_mfma_f32_16x16x32_bf16 v[44:47], a[232:235], v[192:195], v[44:47]
ds_read_b64_tr_b16 a[224:225], v5 offset:10240
v_mfma_f32_16x16x32_bf16 v[64:67], a[232:235], v[196:199], v[64:67]
ds_read_b64_tr_b16 a[226:227], v5 offset:10752
v_mfma_f32_16x16x32_bf16 v[48:51], a[236:239], v[192:195], v[48:51]
ds_read_b64_tr_b16 a[228:229], v5 offset:10272
v_mfma_f32_16x16x32_bf16 v[68:71], a[236:239], v[196:199], v[68:71]
ds_read_b64_tr_b16 a[230:231], v5 offset:10784
s_waitcnt lgkmcnt(4)
v_mfma_f32_16x16x32_bf16 v[36:39], a[240:243], v[200:203], v[36:39]
ds_read_b64_tr_b16 a[232:233], v5 offset:10304
v_mfma_f32_16x16x32_bf16 v[56:59], a[240:243], v[204:207], v[56:59]
ds_read_b64_tr_b16 a[234:235], v5 offset:10816
v_mfma_f32_16x16x32_bf16 v[40:43], a[244:247], v[200:203], v[40:43]
ds_read_b64_tr_b16 a[236:237], v5 offset:10336
v_mfma_f32_16x16x32_bf16 v[60:63], a[244:247], v[204:207], v[60:63]
ds_read_b64_tr_b16 a[238:239], v5 offset:10848
v_mfma_f32_16x16x32_bf16 v[44:47], a[248:251], v[200:203], v[44:47]
ds_read_b64_tr_b16 a[240:241], v5 offset:15360
v_mfma_f32_16x16x32_bf16 v[64:67], a[248:251], v[204:207], v[64:67]
ds_read_b64_tr_b16 a[242:243], v5 offset:15872
v_mfma_f32_16x16x32_bf16 v[48:51], a[252:255], v[200:203], v[48:51]
ds_read_b64_tr_b16 a[244:245], v5 offset:15392
v_mfma_f32_16x16x32_bf16 v[68:71], a[252:255], v[204:207], v[68:71]
ds_read_b64_tr_b16 a[246:247], v5 offset:15904
s_waitcnt lgkmcnt(4)
v_mfma_f32_16x16x32_bf16 v[36:39], a[224:227], v[208:211], v[36:39]
ds_read_b64_tr_b16 a[248:249], v5 offset:15424
v_mfma_f32_16x16x32_bf16 v[56:59], a[224:227], v[212:215], v[56:59]
ds_read_b64_tr_b16 a[250:251], v5 offset:15936
s_waitcnt vmcnt(18)
v_mfma_f32_16x16x32_bf16 v[40:43], a[228:231], v[208:211], v[40:43]
ds_read_b64_tr_b16 a[252:253], v5 offset:15456
s_barrier
v_mfma_f32_16x16x32_bf16 v[60:63], a[228:231], v[212:215], v[60:63]
ds_read_b64_tr_b16 a[254:255], v5 offset:15968
s_mul_i32 s68, s77, 98304
v_mfma_f32_16x16x32_bf16 v[44:47], a[232:235], v[208:211], v[44:47]
ds_read_b64_tr_b16 a[224:225], v5 offset:20480
s_add_u32 s68, s68, 128
v_mfma_f32_16x16x32_bf16 v[64:67], a[232:235], v[212:215], v[64:67]
ds_read_b64_tr_b16 a[226:227], v5 offset:20992
s_add_u32 m0, s61, 40960
v_mfma_f32_16x16x32_bf16 v[48:51], a[236:239], v[208:211], v[48:51]
ds_read_b64_tr_b16 a[228:229], v5 offset:20512
buffer_load_dwordx4 v7, s[32:35], s68 offen lds
v_mfma_f32_16x16x32_bf16 v[68:71], a[236:239], v[212:215], v[68:71]
ds_read_b64_tr_b16 a[230:231], v5 offset:21024
s_waitcnt lgkmcnt(4)
v_mfma_f32_16x16x32_bf16 v[36:39], a[240:243], v[216:219], v[36:39]
ds_read_b64_tr_b16 a[232:233], v5 offset:20544
s_waitcnt vmcnt(2)
v_mfma_f32_16x16x32_bf16 v[56:59], a[240:243], v[220:223], v[56:59]
ds_read_b64_tr_b16 a[234:235], v5 offset:21056
v_cmp_ne_u32 vcc, s62, v77
v_mfma_f32_16x16x32_bf16 v[40:43], a[244:247], v[216:219], v[40:43]
ds_read_b64_tr_b16 a[236:237], v5 offset:20576
s_cbranch_vccnz .Lpq_8s
.Lpq_8:
v_mfma_f32_16x16x32_bf16 v[60:63], a[244:247], v[220:223], v[60:63]
ds_read_b64_tr_b16 a[238:239], v5 offset:21088
buffer_load_dwordx4 v8, s[32:35], s68 offen offset:1056 lds
v_mfma_f32_16x16x32_bf16 v[44:47], a[248:251], v[216:219], v[44:47]
ds_read_b64_tr_b16 a[240:241], v5 offset:25600
buffer_load_dwordx4 v9, s[32:35], s68 offen offset:2112 lds
v_mfma_f32_16x16x32_bf16 v[64:67], a[248:251], v[220:223], v[64:67]
ds_read_b64_tr_b16 a[242:243], v5 offset:26112
v_mfma_f32_16x16x32_bf16 v[48:51], a[252:255], v[216:219], v[48:51]
ds_read_b64_tr_b16 a[244:245], v5 offset:25632
v_mfma_f32_16x16x32_bf16 v[68:71], a[252:255], v[220:223], v[68:71]
ds_read_b64_tr_b16 a[246:247], v5 offset:26144
s_waitcnt lgkmcnt(4)
v_mfma_f32_16x16x32_bf16 v[36:39], a[224:227], v[224:227], v[36:39]
ds_read_b64_tr_b16 a[248:249], v5 offset:25664
buffer_load_dwordx4 v10, s[32:35], s68 offen offset:3168 lds
v_mfma_f32_16x16x32_bf16 v[56:59], a[224:227], v[228:231], v[56:59]
ds_read_b64_tr_b16 a[250:251], v5 offset:26176
v_mfma_f32_16x16x32_bf16 v[40:43], a[228:231], v[224:227], v[40:43]
ds_read_b64_tr_b16 a[252:253], v5 offset:25696
v_mfma_f32_16x16x32_bf16 v[60:63], a[228:231], v[228:231], v[60:63]
ds_read_b64_tr_b16 a[254:255], v5 offset:26208
v_mfma_f32_16x16x32_bf16 v[44:47], a[232:235], v[224:227], v[44:47]
ds_read_b64_tr_b16 a[224:225], v5 offset:30720
v_accvgpr_read_b32 v13, a193
v_mfma_f32_16x16x32_bf16 v[64:67], a[232:235], v[228:231], v[64:67]
ds_read_b64_tr_b16 a[226:227], v5 offset:31232
ds_read_b128 v[52:55], v13
v_mfma_f32_16x16x32_bf16 v[48:51], a[236:239], v[224:227], v[48:51]
ds_read_b64_tr_b16 a[228:229], v5 offset:30752
ds_read_b128 v[72:75], v13 offset:64
v_mfma_f32_16x16x32_bf16 v[68:71], a[236:239], v[228:231], v[68:71]
ds_read_b64_tr_b16 a[230:231], v5 offset:31264
s_waitcnt lgkmcnt(6)
v_mfma_f32_16x16x32_bf16 v[36:39], a[240:243], v[232:235], v[36:39]
ds_read_b64_tr_b16 a[232:233], v5 offset:30784
v_mfma_f32_16x16x32_bf16 v[56:59], a[240:243], v[236:239], v[56:59]
ds_read_b64_tr_b16 a[234:235], v5 offset:31296
v_mfma_f32_16x16x32_bf16 v[40:43], a[244:247], v[232:235], v[40:43]
ds_read_b64_tr_b16 a[236:237], v5 offset:30816
v_mfma_f32_16x16x32_bf16 v[60:63], a[244:247], v[236:239], v[60:63]
ds_read_b64_tr_b16 a[238:239], v5 offset:31328
v_mfma_f32_16x16x32_bf16 v[44:47], a[248:251], v[232:235], v[44:47]
ds_read_b64_tr_b16 a[240:241], v5 offset:35840
v_mfma_f32_16x16x32_bf16 v[64:67], a[248:251], v[236:239], v[64:67]
ds_read_b64_tr_b16 a[242:243], v5 offset:36352
v_mfma_f32_16x16x32_bf16 v[48:51], a[252:255], v[232:235], v[48:51]
ds_read_b64_tr_b16 a[244:245], v5 offset:35872
v_mfma_f32_16x16x32_bf16 v[68:71], a[252:255], v[236:239], v[68:71]
ds_read_b64_tr_b16 a[246:247], v5 offset:36384
s_waitcnt lgkmcnt(4)
v_mfma_f32_16x16x32_bf16 v[36:39], a[224:227], v[240:243], v[36:39]
ds_read_b64_tr_b16 a[248:249], v5 offset:35904
v_mfma_f32_16x16x32_bf16 v[56:59], a[224:227], v[244:247], v[56:59]
ds_read_b64_tr_b16 a[250:251], v5 offset:36416
v_mfma_f32_16x16x32_bf16 v[40:43], a[228:231], v[240:243], v[40:43]
ds_read_b64_tr_b16 a[252:253], v5 offset:35936
v_mfma_f32_16x16x32_bf16 v[60:63], a[228:231], v[244:247], v[60:63]
ds_read_b64_tr_b16 a[254:255], v5 offset:36448
v_mfma_f32_16x16x32_bf16 v[44:47], a[232:235], v[240:243], v[44:47]
v_mfma_f32_16x16x32_bf16 v[64:67], a[232:235], v[244:247], v[64:67]
v_mfma_f32_16x16x32_bf16 v[48:51], a[236:239], v[240:243], v[48:51]
v_mfma_f32_16x16x32_bf16 v[68:71], a[236:239], v[244:247], v[68:71]
s_waitcnt lgkmcnt(0)
v_mfma_f32_16x16x32_bf16 v[36:39], a[240:243], v[248:251], v[36:39]
v_mfma_f32_16x16x32_bf16 v[56:59], a[240:243], v[252:255], v[56:59]
v_mfma_f32_16x16x32_bf16 v[40:43], a[244:247], v[248:251], v[40:43]
v_mfma_f32_16x16x32_bf16 v[60:63], a[244:247], v[252:255], v[60:63]
v_mfma_f32_16x16x32_bf16 v[44:47], a[248:251], v[248:251], v[44:47]
v_mfma_f32_16x16x32_bf16 v[64:67], a[248:251], v[252:255], v[64:67]
v_mfma_f32_16x16x32_bf16 v[48:51], a[252:255], v[248:251], v[48:51]
v_mfma_f32_16x16x32_bf16 v[68:71], a[252:255], v[252:255], v[68:71]
s_nop 0
v_pk_fma_f32 v[36:37], v[36:37], v[14:15], v[52:53] op_sel:[0,0,0] op_sel_hi:[1,0,1]
v_pk_fma_f32 v[38:39], v[38:39], v[14:15], v[54:55] op_sel:[0,0,0] op_sel_hi:[1,0,1]
v_pk_fma_f32 v[56:57], v[56:57], v[14:15], v[52:53] op_sel:[0,1,0] op_sel_hi:[1,1,1]
v_pk_fma_f32 v[58:59], v[58:59], v[14:15], v[54:55] op_sel:[0,1,0] op_sel_hi:[1,1,1]
ds_read_b128 v[52:55], v13 offset:128
v_max_f32 v36, 0, v36
v_max_f32 v37, 0, v37
v_max_f32 v38, 0, v38
v_max_f32 v39, 0, v39
v_pk_mul_f32 v[36:37], v[36:37], v[36:37]
v_pk_mul_f32 v[38:39], v[38:39], v[38:39]
v_cvt_pk_bf16_f32 v36, v36, v37
v_cvt_pk_bf16_f32 v37, v38, v39
v_max_f32 v56, 0, v56
v_max_f32 v57, 0, v57
v_max_f32 v58, 0, v58
v_max_f32 v59, 0, v59
v_pk_mul_f32 v[56:57], v[56:57], v[56:57]
v_pk_mul_f32 v[58:59], v[58:59], v[58:59]
v_cvt_pk_bf16_f32 v56, v56, v57
v_cvt_pk_bf16_f32 v57, v58, v59
v_pk_fma_f32 v[40:41], v[40:41], v[14:15], v[72:73] op_sel:[0,0,0] op_sel_hi:[1,0,1]
v_pk_fma_f32 v[42:43], v[42:43], v[14:15], v[74:75] op_sel:[0,0,0] op_sel_hi:[1,0,1]
v_pk_fma_f32 v[60:61], v[60:61], v[14:15], v[72:73] op_sel:[0,1,0] op_sel_hi:[1,1,1]
v_pk_fma_f32 v[62:63], v[62:63], v[14:15], v[74:75] op_sel:[0,1,0] op_sel_hi:[1,1,1]
ds_read_b128 v[72:75], v13 offset:192
v_max_f32 v40, 0, v40
v_max_f32 v41, 0, v41
v_max_f32 v42, 0, v42
v_max_f32 v43, 0, v43
v_pk_mul_f32 v[40:41], v[40:41], v[40:41]
v_pk_mul_f32 v[42:43], v[42:43], v[42:43]
v_cvt_pk_bf16_f32 v38, v40, v41
v_cvt_pk_bf16_f32 v39, v42, v43
)ASM"
R"ASM(
v_max_f32 v60, 0, v60
v_max_f32 v61, 0, v61
v_max_f32 v62, 0, v62
v_max_f32 v63, 0, v63
v_pk_mul_f32 v[60:61], v[60:61], v[60:61]
v_pk_mul_f32 v[62:63], v[62:63], v[62:63]
v_cvt_pk_bf16_f32 v58, v60, v61
v_cvt_pk_bf16_f32 v59, v62, v63
s_add_u32 s66, s14, 4096
buffer_store_dwordx4 v[36:39], v0, s[44:47], s66 offen sc1
buffer_store_dwordx4 v[56:59], v0, s[44:47], s66 offen offset:1024 sc1
s_barrier
ds_read_b64_tr_b16 a[224:225], v5 offset:40960
ds_read_b64_tr_b16 a[226:227], v5 offset:41472
ds_read_b64_tr_b16 a[228:229], v5 offset:40992
ds_read_b64_tr_b16 a[230:231], v5 offset:41504
ds_read_b64_tr_b16 a[232:233], v5 offset:41024
ds_read_b64_tr_b16 a[234:235], v5 offset:41536
ds_read_b64_tr_b16 a[236:237], v5 offset:41056
ds_read_b64_tr_b16 a[238:239], v5 offset:41568
ds_read_b64_tr_b16 a[240:241], v5 offset:46080
ds_read_b64_tr_b16 a[242:243], v5 offset:46592
ds_read_b64_tr_b16 a[244:245], v5 offset:46112
ds_read_b64_tr_b16 a[246:247], v5 offset:46624
s_waitcnt lgkmcnt(13)
v_pk_fma_f32 v[44:45], v[44:45], v[14:15], v[52:53] op_sel:[0,0,0] op_sel_hi:[1,0,1]
v_pk_fma_f32 v[46:47], v[46:47], v[14:15], v[54:55] op_sel:[0,0,0] op_sel_hi:[1,0,1]
v_pk_fma_f32 v[64:65], v[64:65], v[14:15], v[52:53] op_sel:[0,1,0] op_sel_hi:[1,1,1]
v_pk_fma_f32 v[66:67], v[66:67], v[14:15], v[54:55] op_sel:[0,1,0] op_sel_hi:[1,1,1]
v_max_f32 v44, 0, v44
v_max_f32 v45, 0, v45
v_max_f32 v46, 0, v46
v_max_f32 v47, 0, v47
v_pk_mul_f32 v[44:45], v[44:45], v[44:45]
v_pk_mul_f32 v[46:47], v[46:47], v[46:47]
v_cvt_pk_bf16_f32 v44, v44, v45
v_cvt_pk_bf16_f32 v45, v46, v47
v_max_f32 v64, 0, v64
v_max_f32 v65, 0, v65
v_max_f32 v66, 0, v66
v_max_f32 v67, 0, v67
v_pk_mul_f32 v[64:65], v[64:65], v[64:65]
v_pk_mul_f32 v[66:67], v[66:67], v[66:67]
v_cvt_pk_bf16_f32 v64, v64, v65
v_cvt_pk_bf16_f32 v65, v66, v67
s_waitcnt lgkmcnt(12)
v_pk_fma_f32 v[48:49], v[48:49], v[14:15], v[72:73] op_sel:[0,0,0] op_sel_hi:[1,0,1]
v_pk_fma_f32 v[50:51], v[50:51], v[14:15], v[74:75] op_sel:[0,0,0] op_sel_hi:[1,0,1]
v_pk_fma_f32 v[68:69], v[68:69], v[14:15], v[72:73] op_sel:[0,1,0] op_sel_hi:[1,1,1]
v_pk_fma_f32 v[70:71], v[70:71], v[14:15], v[74:75] op_sel:[0,1,0] op_sel_hi:[1,1,1]
v_max_f32 v48, 0, v48
v_max_f32 v49, 0, v49
v_max_f32 v50, 0, v50
v_max_f32 v51, 0, v51
v_pk_mul_f32 v[48:49], v[48:49], v[48:49]
v_pk_mul_f32 v[50:51], v[50:51], v[50:51]
v_cvt_pk_bf16_f32 v46, v48, v49
v_cvt_pk_bf16_f32 v47, v50, v51
v_max_f32 v68, 0, v68
v_max_f32 v69, 0, v69
v_max_f32 v70, 0, v70
v_max_f32 v71, 0, v71
v_pk_mul_f32 v[68:69], v[68:69], v[68:69]
v_pk_mul_f32 v[70:71], v[70:71], v[70:71]
v_cvt_pk_bf16_f32 v66, v68, v69
v_cvt_pk_bf16_f32 v67, v70, v71
buffer_store_dwordx4 v[44:47], v0, s[44:47], s66 offen offset:2048 sc1
buffer_store_dwordx4 v[64:67], v0, s[44:47], s66 offen offset:3072 sc1
s_waitcnt lgkmcnt(8)
v_mfma_f32_16x16x32_bf16 v[16:19], a[224:227], v[96:99], 0
ds_read_b64_tr_b16 a[248:249], v5 offset:46144
s_add_u32 s68, s23, 21506048
v_mfma_f32_16x16x32_bf16 v[76:79], a[224:227], v[100:103], 0
ds_read_b64_tr_b16 a[250:251], v5 offset:46656
s_add_u32 m0, s61, 61440
v_mfma_f32_16x16x32_bf16 v[20:23], a[228:231], v[96:99], 0
ds_read_b64_tr_b16 a[252:253], v5 offset:46176
buffer_load_dwordx4 v0, s[44:47], s68 offen sc1 lds
v_mfma_f32_16x16x32_bf16 v[80:83], a[228:231], v[100:103], 0
ds_read_b64_tr_b16 a[254:255], v5 offset:46688
s_waitcnt lgkmcnt(8)
v_mfma_f32_16x16x32_bf16 v[24:27], a[232:235], v[96:99], 0
ds_read_b64_tr_b16 a[224:225], v5 offset:51200
buffer_load_dwordx4 v0, s[44:47], s68 offen offset:1024 sc1 lds
v_mfma_f32_16x16x32_bf16 v[84:87], a[232:235], v[100:103], 0
ds_read_b64_tr_b16 a[226:227], v5 offset:51712
buffer_load_dwordx4 v0, s[44:47], s68 offen offset:2048 sc1 lds
v_mfma_f32_16x16x32_bf16 v[28:31], a[236:239], v[96:99], 0
ds_read_b64_tr_b16 a[228:229], v5 offset:51232
v_mov_b32 v40, s62
v_mfma_f32_16x16x32_bf16 v[88:91], a[236:239], v[100:103], 0
ds_read_b64_tr_b16 a[230:231], v5 offset:51744
s_waitcnt lgkmcnt(4)
v_mfma_f32_16x16x32_bf16 v[16:19], a[240:243], v[104:107], v[16:19]
ds_read_b64_tr_b16 a[232:233], v5 offset:51264
buffer_load_dwordx4 v0, s[44:47], s68 offen offset:3072 sc1 lds
v_mfma_f32_16x16x32_bf16 v[76:79], a[240:243], v[108:111], v[76:79]
ds_read_b64_tr_b16 a[234:235], v5 offset:51776
v_lshrrev_b32 v41, 2, v0
v_mfma_f32_16x16x32_bf16 v[20:23], a[244:247], v[104:107], v[20:23]
ds_read_b64_tr_b16 a[236:237], v5 offset:51296
v_accvgpr_write_b32 a32, v36
v_mfma_f32_16x16x32_bf16 v[80:83], a[244:247], v[108:111], v[80:83]
ds_read_b64_tr_b16 a[238:239], v5 offset:51808
v_accvgpr_write_b32 a33, v37
v_mfma_f32_16x16x32_bf16 v[24:27], a[248:251], v[104:107], v[24:27]
ds_read_b64_tr_b16 a[240:241], v5 offset:56320
s_add_u32 s68, s23, 21510144
v_mfma_f32_16x16x32_bf16 v[84:87], a[248:251], v[108:111], v[84:87]
ds_read_b64_tr_b16 a[242:243], v5 offset:56832
s_add_u32 m0, s61, 65536
v_mfma_f32_16x16x32_bf16 v[28:31], a[252:255], v[104:107], v[28:31]
ds_read_b64_tr_b16 a[244:245], v5 offset:56352
buffer_load_dwordx4 v0, s[44:47], s68 offen sc1 lds
v_mfma_f32_16x16x32_bf16 v[88:91], a[252:255], v[108:111], v[88:91]
ds_read_b64_tr_b16 a[246:247], v5 offset:56864
s_waitcnt lgkmcnt(4)
v_mfma_f32_16x16x32_bf16 v[16:19], a[224:227], v[112:115], v[16:19]
ds_read_b64_tr_b16 a[248:249], v5 offset:56384
s_add_u32 s68, s22, 21506048
v_mfma_f32_16x16x32_bf16 v[76:79], a[224:227], v[116:119], v[76:79]
ds_read_b64_tr_b16 a[250:251], v5 offset:56896
s_add_u32 m0, s61, 81920
v_mfma_f32_16x16x32_bf16 v[20:23], a[228:231], v[112:115], v[20:23]
ds_read_b64_tr_b16 a[252:253], v5 offset:56416
s_barrier
v_mfma_f32_16x16x32_bf16 v[80:83], a[228:231], v[116:119], v[80:83]
ds_read_b64_tr_b16 a[254:255], v5 offset:56928
buffer_load_dwordx4 v0, s[44:47], s68 offen sc1 lds
v_mfma_f32_16x16x32_bf16 v[24:27], a[232:235], v[112:115], v[24:27]
ds_read_b64_tr_b16 a[224:225], v6
buffer_load_dwordx4 v0, s[44:47], s68 offen offset:1024 sc1 lds
v_mfma_f32_16x16x32_bf16 v[84:87], a[232:235], v[116:119], v[84:87]
ds_read_b64_tr_b16 a[226:227], v6 offset:512
v_accvgpr_write_b32 a34, v38
v_mfma_f32_16x16x32_bf16 v[28:31], a[236:239], v[112:115], v[28:31]
ds_read_b64_tr_b16 a[228:229], v6 offset:32
v_accvgpr_write_b32 a35, v39
v_mfma_f32_16x16x32_bf16 v[88:91], a[236:239], v[116:119], v[88:91]
ds_read_b64_tr_b16 a[230:231], v6 offset:544
s_waitcnt lgkmcnt(4)
v_mfma_f32_16x16x32_bf16 v[16:19], a[240:243], v[120:123], v[16:19]
ds_read_b64_tr_b16 a[232:233], v6 offset:64
buffer_load_dwordx4 v0, s[44:47], s68 offen offset:2048 sc1 lds
v_mfma_f32_16x16x32_bf16 v[76:79], a[240:243], v[124:127], v[76:79]
ds_read_b64_tr_b16 a[234:235], v6 offset:576
v_accvgpr_write_b32 a36, v56
v_mfma_f32_16x16x32_bf16 v[20:23], a[244:247], v[120:123], v[20:23]
ds_read_b64_tr_b16 a[236:237], v6 offset:96
v_accvgpr_write_b32 a37, v57
v_mfma_f32_16x16x32_bf16 v[80:83], a[244:247], v[124:127], v[80:83]
ds_read_b64_tr_b16 a[238:239], v6 offset:608
v_accvgpr_write_b32 a38, v58
v_mfma_f32_16x16x32_bf16 v[24:27], a[248:251], v[120:123], v[24:27]
ds_read_b64_tr_b16 a[240:241], v6 offset:5120
buffer_load_dwordx4 v0, s[44:47], s68 offen offset:3072 sc1 lds
v_mfma_f32_16x16x32_bf16 v[84:87], a[248:251], v[124:127], v[84:87]
ds_read_b64_tr_b16 a[242:243], v6 offset:5632
v_accvgpr_write_b32 a39, v59
v_mfma_f32_16x16x32_bf16 v[28:31], a[252:255], v[120:123], v[28:31]
ds_read_b64_tr_b16 a[244:245], v6 offset:5152
v_accvgpr_write_b32 a40, v44
v_mfma_f32_16x16x32_bf16 v[88:91], a[252:255], v[124:127], v[88:91]
ds_read_b64_tr_b16 a[246:247], v6 offset:5664
s_waitcnt lgkmcnt(4)
v_mfma_f32_16x16x32_bf16 v[16:19], a[224:227], v[128:131], v[16:19]
ds_read_b64_tr_b16 a[248:249], v6 offset:5184
s_add_u32 s68, s22, 21510144
v_mfma_f32_16x16x32_bf16 v[76:79], a[224:227], v[132:135], v[76:79]
ds_read_b64_tr_b16 a[250:251], v6 offset:5696
s_add_u32 m0, s61, 86016
v_mfma_f32_16x16x32_bf16 v[20:23], a[228:231], v[128:131], v[20:23]
ds_read_b64_tr_b16 a[252:253], v6 offset:5216
buffer_load_dwordx4 v0, s[44:47], s68 offen sc1 lds
v_mfma_f32_16x16x32_bf16 v[80:83], a[228:231], v[132:135], v[80:83]
ds_read_b64_tr_b16 a[254:255], v6 offset:5728
v_accvgpr_write_b32 a41, v45
v_mfma_f32_16x16x32_bf16 v[24:27], a[232:235], v[128:131], v[24:27]
ds_read_b64_tr_b16 a[224:225], v6 offset:10240
v_accvgpr_write_b32 a42, v46
v_mfma_f32_16x16x32_bf16 v[84:87], a[232:235], v[132:135], v[84:87]
ds_read_b64_tr_b16 a[226:227], v6 offset:10752
v_accvgpr_write_b32 a43, v47
v_mfma_f32_16x16x32_bf16 v[28:31], a[236:239], v[128:131], v[28:31]
ds_read_b64_tr_b16 a[228:229], v6 offset:10272
v_accvgpr_write_b32 a44, v64
v_mfma_f32_16x16x32_bf16 v[88:91], a[236:239], v[132:135], v[88:91]
ds_read_b64_tr_b16 a[230:231], v6 offset:10784
s_waitcnt lgkmcnt(4)
v_mfma_f32_16x16x32_bf16 v[16:19], a[240:243], v[136:139], v[16:19]
ds_read_b64_tr_b16 a[232:233], v6 offset:10304
s_add_u32 s65, s64, 1048576
v_mfma_f32_16x16x32_bf16 v[76:79], a[240:243], v[140:143], v[76:79]
ds_read_b64_tr_b16 a[234:235], v6 offset:10816
s_waitcnt vmcnt(10)
v_mfma_f32_16x16x32_bf16 v[20:23], a[244:247], v[136:139], v[20:23]
ds_read_b64_tr_b16 a[236:237], v6 offset:10336
buffer_store_dword v40, v41, s[44:47], s65 offen sc1
v_mfma_f32_16x16x32_bf16 v[80:83], a[244:247], v[140:143], v[80:83]
ds_read_b64_tr_b16 a[238:239], v6 offset:10848
v_accvgpr_write_b32 a45, v65
v_mfma_f32_16x16x32_bf16 v[24:27], a[248:251], v[136:139], v[24:27]
ds_read_b64_tr_b16 a[240:241], v6 offset:15360
v_accvgpr_write_b32 a46, v66
v_mfma_f32_16x16x32_bf16 v[84:87], a[248:251], v[140:143], v[84:87]
ds_read_b64_tr_b16 a[242:243], v6 offset:15872
v_accvgpr_write_b32 a47, v67
v_mfma_f32_16x16x32_bf16 v[28:31], a[252:255], v[136:139], v[28:31]
ds_read_b64_tr_b16 a[244:245], v6 offset:15392
v_accvgpr_read_b32 v61, a201
v_mfma_f32_16x16x32_bf16 v[88:91], a[252:255], v[140:143], v[88:91]
ds_read_b64_tr_b16 a[246:247], v6 offset:15904
s_waitcnt lgkmcnt(4)
v_mfma_f32_16x16x32_bf16 v[16:19], a[224:227], v[144:147], v[16:19]
ds_read_b64_tr_b16 a[248:249], v6 offset:15424
v_add_u32 v60, s16, v0
v_mfma_f32_16x16x32_bf16 v[76:79], a[224:227], v[148:151], v[76:79]
ds_read_b64_tr_b16 a[250:251], v6 offset:15936
v_add_u32 v61, s16, v61
v_mfma_f32_16x16x32_bf16 v[20:23], a[228:231], v[144:147], v[20:23]
ds_read_b64_tr_b16 a[252:253], v6 offset:15456
s_barrier
v_mfma_f32_16x16x32_bf16 v[80:83], a[228:231], v[148:151], v[80:83]
ds_read_b64_tr_b16 a[254:255], v6 offset:15968
s_add_u32 s68, s23, 21516288
v_mfma_f32_16x16x32_bf16 v[24:27], a[232:235], v[144:147], v[24:27]
ds_read_b64_tr_b16 a[224:225], v4
s_add_u32 m0, s61, 102400
v_mfma_f32_16x16x32_bf16 v[84:87], a[232:235], v[148:151], v[84:87]
ds_read_b64_tr_b16 a[226:227], v4 offset:512
buffer_load_dwordx4 v0, s[44:47], s68 offen sc1 lds
v_mfma_f32_16x16x32_bf16 v[28:31], a[236:239], v[144:147], v[28:31]
ds_read_b64_tr_b16 a[228:229], v4 offset:32
buffer_load_dwordx4 v0, s[44:47], s68 offen offset:1024 sc1 lds
v_mfma_f32_16x16x32_bf16 v[88:91], a[236:239], v[148:151], v[88:91]
ds_read_b64_tr_b16 a[230:231], v4 offset:544
s_waitcnt lgkmcnt(4)
v_mfma_f32_16x16x32_bf16 v[16:19], a[240:243], v[152:155], v[16:19]
ds_read_b64_tr_b16 a[232:233], v4 offset:64
buffer_load_dwordx4 v0, s[44:47], s68 offen offset:2048 sc1 lds
v_mfma_f32_16x16x32_bf16 v[76:79], a[240:243], v[156:159], v[76:79]
ds_read_b64_tr_b16 a[234:235], v4 offset:576
s_and_b32 s16, s57, 7
v_mfma_f32_16x16x32_bf16 v[20:23], a[244:247], v[152:155], v[20:23]
ds_read_b64_tr_b16 a[236:237], v4 offset:96
s_lshl_b32 s16, s16, 12
v_mfma_f32_16x16x32_bf16 v[80:83], a[244:247], v[156:159], v[80:83]
ds_read_b64_tr_b16 a[238:239], v4 offset:608
buffer_load_dwordx4 v0, s[44:47], s68 offen offset:3072 sc1 lds
v_mfma_f32_16x16x32_bf16 v[24:27], a[248:251], v[152:155], v[24:27]
ds_read_b64_tr_b16 a[240:241], v4 offset:5120
v_mfma_f32_16x16x32_bf16 v[84:87], a[248:251], v[156:159], v[84:87]
ds_read_b64_tr_b16 a[242:243], v4 offset:5632
v_mfma_f32_16x16x32_bf16 v[28:31], a[252:255], v[152:155], v[28:31]
ds_read_b64_tr_b16 a[244:245], v4 offset:5152
v_mfma_f32_16x16x32_bf16 v[88:91], a[252:255], v[156:159], v[88:91]
ds_read_b64_tr_b16 a[246:247], v4 offset:5664
s_waitcnt lgkmcnt(4)
v_mfma_f32_16x16x32_bf16 v[16:19], a[224:227], v[160:163], v[16:19]
ds_read_b64_tr_b16 a[248:249], v4 offset:5184
s_add_u32 s68, s23, 21520384
v_mfma_f32_16x16x32_bf16 v[76:79], a[224:227], v[164:167], v[76:79]
ds_read_b64_tr_b16 a[250:251], v4 offset:5696
s_add_u32 m0, s61, 106496
v_mfma_f32_16x16x32_bf16 v[20:23], a[228:231], v[160:163], v[20:23]
ds_read_b64_tr_b16 a[252:253], v4 offset:5216
buffer_load_dwordx4 v0, s[44:47], s68 offen sc1 lds
v_mfma_f32_16x16x32_bf16 v[80:83], a[228:231], v[164:167], v[80:83]
ds_read_b64_tr_b16 a[254:255], v4 offset:5728
v_mfma_f32_16x16x32_bf16 v[24:27], a[232:235], v[160:163], v[24:27]
ds_read_b64_tr_b16 a[224:225], v4 offset:10240
v_mfma_f32_16x16x32_bf16 v[84:87], a[232:235], v[164:167], v[84:87]
ds_read_b64_tr_b16 a[226:227], v4 offset:10752
v_mfma_f32_16x16x32_bf16 v[28:31], a[236:239], v[160:163], v[28:31]
ds_read_b64_tr_b16 a[228:229], v4 offset:10272
v_mfma_f32_16x16x32_bf16 v[88:91], a[236:239], v[164:167], v[88:91]
ds_read_b64_tr_b16 a[230:231], v4 offset:10784
s_waitcnt lgkmcnt(4)
v_mfma_f32_16x16x32_bf16 v[16:19], a[240:243], v[168:171], v[16:19]
ds_read_b64_tr_b16 a[232:233], v4 offset:10304
v_mfma_f32_16x16x32_bf16 v[76:79], a[240:243], v[172:175], v[76:79]
ds_read_b64_tr_b16 a[234:235], v4 offset:10816
v_mfma_f32_16x16x32_bf16 v[20:23], a[244:247], v[168:171], v[20:23]
ds_read_b64_tr_b16 a[236:237], v4 offset:10336
v_mfma_f32_16x16x32_bf16 v[80:83], a[244:247], v[172:175], v[80:83]
ds_read_b64_tr_b16 a[238:239], v4 offset:10848
v_mfma_f32_16x16x32_bf16 v[24:27], a[248:251], v[168:171], v[24:27]
ds_read_b64_tr_b16 a[240:241], v4 offset:15360
v_mfma_f32_16x16x32_bf16 v[84:87], a[248:251], v[172:175], v[84:87]
ds_read_b64_tr_b16 a[242:243], v4 offset:15872
v_mfma_f32_16x16x32_bf16 v[28:31], a[252:255], v[168:171], v[28:31]
ds_read_b64_tr_b16 a[244:245], v4 offset:15392
v_mfma_f32_16x16x32_bf16 v[88:91], a[252:255], v[172:175], v[88:91]
ds_read_b64_tr_b16 a[246:247], v4 offset:15904
s_waitcnt lgkmcnt(4)
v_mfma_f32_16x16x32_bf16 v[16:19], a[224:227], v[176:179], v[16:19]
ds_read_b64_tr_b16 a[248:249], v4 offset:15424
v_mfma_f32_16x16x32_bf16 v[76:79], a[224:227], v[180:183], v[76:79]
ds_read_b64_tr_b16 a[250:251], v4 offset:15936
v_mfma_f32_16x16x32_bf16 v[20:23], a[228:231], v[176:179], v[20:23]
ds_read_b64_tr_b16 a[252:253], v4 offset:15456
s_barrier
v_mfma_f32_16x16x32_bf16 v[80:83], a[228:231], v[180:183], v[80:83]
ds_read_b64_tr_b16 a[254:255], v4 offset:15968
s_add_u32 s68, s23, 21495808
v_mfma_f32_16x16x32_bf16 v[24:27], a[232:235], v[176:179], v[24:27]
ds_read_b64_tr_b16 a[224:225], v4 offset:20480
s_add_u32 m0, s61, 122880
v_mfma_f32_16x16x32_bf16 v[84:87], a[232:235], v[180:183], v[84:87]
ds_read_b64_tr_b16 a[226:227], v4 offset:20992
buffer_load_dwordx4 v0, s[44:47], s68 offen sc1 lds
v_mfma_f32_16x16x32_bf16 v[28:31], a[236:239], v[176:179], v[28:31]
ds_read_b64_tr_b16 a[228:229], v4 offset:20512
buffer_load_dwordx4 v0, s[44:47], s68 offen offset:1024 sc1 lds
v_mfma_f32_16x16x32_bf16 v[88:91], a[236:239], v[180:183], v[88:91]
ds_read_b64_tr_b16 a[230:231], v4 offset:21024
s_waitcnt lgkmcnt(4)
v_mfma_f32_16x16x32_bf16 v[16:19], a[240:243], v[184:187], v[16:19]
ds_read_b64_tr_b16 a[232:233], v4 offset:20544
buffer_load_dwordx4 v0, s[44:47], s68 offen offset:2048 sc1 lds
v_mfma_f32_16x16x32_bf16 v[76:79], a[240:243], v[188:191], v[76:79]
ds_read_b64_tr_b16 a[234:235], v4 offset:21056
v_mfma_f32_16x16x32_bf16 v[20:23], a[244:247], v[184:187], v[20:23]
ds_read_b64_tr_b16 a[236:237], v4 offset:20576
v_mfma_f32_16x16x32_bf16 v[80:83], a[244:247], v[188:191], v[80:83]
ds_read_b64_tr_b16 a[238:239], v4 offset:21088
buffer_load_dwordx4 v0, s[44:47], s68 offen offset:3072 sc1 lds
v_mfma_f32_16x16x32_bf16 v[24:27], a[248:251], v[184:187], v[24:27]
ds_read_b64_tr_b16 a[240:241], v4 offset:25600
v_mfma_f32_16x16x32_bf16 v[84:87], a[248:251], v[188:191], v[84:87]
ds_read_b64_tr_b16 a[242:243], v4 offset:26112
v_mfma_f32_16x16x32_bf16 v[28:31], a[252:255], v[184:187], v[28:31]
ds_read_b64_tr_b16 a[244:245], v4 offset:25632
v_mfma_f32_16x16x32_bf16 v[88:91], a[252:255], v[188:191], v[88:91]
ds_read_b64_tr_b16 a[246:247], v4 offset:26144
s_waitcnt lgkmcnt(4)
v_mfma_f32_16x16x32_bf16 v[16:19], a[224:227], v[192:195], v[16:19]
ds_read_b64_tr_b16 a[248:249], v4 offset:25664
s_add_u32 s68, s23, 21499904
v_mfma_f32_16x16x32_bf16 v[76:79], a[224:227], v[196:199], v[76:79]
ds_read_b64_tr_b16 a[250:251], v4 offset:26176
s_add_u32 m0, s61, 126976
v_mfma_f32_16x16x32_bf16 v[20:23], a[228:231], v[192:195], v[20:23]
ds_read_b64_tr_b16 a[252:253], v4 offset:25696
buffer_load_dwordx4 v0, s[44:47], s68 offen sc1 lds
v_mfma_f32_16x16x32_bf16 v[80:83], a[228:231], v[196:199], v[80:83]
ds_read_b64_tr_b16 a[254:255], v4 offset:26208
v_mfma_f32_16x16x32_bf16 v[24:27], a[232:235], v[192:195], v[24:27]
ds_read_b64_tr_b16 a[224:225], v4 offset:30720
v_mfma_f32_16x16x32_bf16 v[84:87], a[232:235], v[196:199], v[84:87]
ds_read_b64_tr_b16 a[226:227], v4 offset:31232
v_mfma_f32_16x16x32_bf16 v[28:31], a[236:239], v[192:195], v[28:31]
ds_read_b64_tr_b16 a[228:229], v4 offset:30752
v_mfma_f32_16x16x32_bf16 v[88:91], a[236:239], v[196:199], v[88:91]
ds_read_b64_tr_b16 a[230:231], v4 offset:31264
s_waitcnt lgkmcnt(4)
v_mfma_f32_16x16x32_bf16 v[16:19], a[240:243], v[200:203], v[16:19]
ds_read_b64_tr_b16 a[232:233], v4 offset:30784
v_mfma_f32_16x16x32_bf16 v[76:79], a[240:243], v[204:207], v[76:79]
ds_read_b64_tr_b16 a[234:235], v4 offset:31296
v_mfma_f32_16x16x32_bf16 v[20:23], a[244:247], v[200:203], v[20:23]
ds_read_b64_tr_b16 a[236:237], v4 offset:30816
v_mfma_f32_16x16x32_bf16 v[80:83], a[244:247], v[204:207], v[80:83]
ds_read_b64_tr_b16 a[238:239], v4 offset:31328
v_mfma_f32_16x16x32_bf16 v[24:27], a[248:251], v[200:203], v[24:27]
ds_read_b64_tr_b16 a[240:241], v4 offset:35840
v_mfma_f32_16x16x32_bf16 v[84:87], a[248:251], v[204:207], v[84:87]
ds_read_b64_tr_b16 a[242:243], v4 offset:36352
v_mfma_f32_16x16x32_bf16 v[28:31], a[252:255], v[200:203], v[28:31]
ds_read_b64_tr_b16 a[244:245], v4 offset:35872
v_mfma_f32_16x16x32_bf16 v[88:91], a[252:255], v[204:207], v[88:91]
ds_read_b64_tr_b16 a[246:247], v4 offset:36384
s_waitcnt lgkmcnt(4)
v_mfma_f32_16x16x32_bf16 v[16:19], a[224:227], v[208:211], v[16:19]
ds_read_b64_tr_b16 a[248:249], v4 offset:35904
v_accvgpr_read_b32 v42, a209
v_mfma_f32_16x16x32_bf16 v[76:79], a[224:227], v[212:215], v[76:79]
ds_read_b64_tr_b16 a[250:251], v4 offset:36416
s_mov_b32 s65, 20447232
v_mfma_f32_16x16x32_bf16 v[20:23], a[228:231], v[208:211], v[20:23]
ds_read_b64_tr_b16 a[252:253], v4 offset:35936
s_barrier
v_mfma_f32_16x16x32_bf16 v[80:83], a[228:231], v[212:215], v[80:83]
ds_read_b64_tr_b16 a[254:255], v4 offset:36448
buffer_load_dword v43, v42, s[44:47], s65 offen sc1
v_mfma_f32_16x16x32_bf16 v[24:27], a[232:235], v[208:211], v[24:27]
ds_read_b64_tr_b16 a[224:225], v4 offset:40960
s_add_u32 s68, s22, 21516288
v_mfma_f32_16x16x32_bf16 v[84:87], a[232:235], v[212:215], v[84:87]
ds_read_b64_tr_b16 a[226:227], v4 offset:41472
s_add_u32 m0, s61, 0
v_mfma_f32_16x16x32_bf16 v[28:31], a[236:239], v[208:211], v[28:31]
ds_read_b64_tr_b16 a[228:229], v4 offset:40992
buffer_load_dwordx4 v0, s[44:47], s68 offen sc1 lds
v_mfma_f32_16x16x32_bf16 v[88:91], a[236:239], v[212:215], v[88:91]
ds_read_b64_tr_b16 a[230:231], v4 offset:41504
s_waitcnt lgkmcnt(4)
v_mfma_f32_16x16x32_bf16 v[16:19], a[240:243], v[216:219], v[16:19]
ds_read_b64_tr_b16 a[232:233], v4 offset:41024
buffer_load_dwordx4 v0, s[44:47], s68 offen offset:1024 sc1 lds
)ASM"
R"ASM(
v_mfma_f32_16x16x32_bf16 v[76:79], a[240:243], v[220:223], v[76:79]
ds_read_b64_tr_b16 a[234:235], v4 offset:41536
buffer_load_dwordx4 v0, s[44:47], s68 offen offset:2048 sc1 lds
v_mfma_f32_16x16x32_bf16 v[20:23], a[244:247], v[216:219], v[20:23]
ds_read_b64_tr_b16 a[236:237], v4 offset:41056
v_mfma_f32_16x16x32_bf16 v[80:83], a[244:247], v[220:223], v[80:83]
ds_read_b64_tr_b16 a[238:239], v4 offset:41568
v_mfma_f32_16x16x32_bf16 v[24:27], a[248:251], v[216:219], v[24:27]
ds_read_b64_tr_b16 a[240:241], v4 offset:46080
buffer_load_dwordx4 v0, s[44:47], s68 offen offset:3072 sc1 lds
v_mfma_f32_16x16x32_bf16 v[84:87], a[248:251], v[220:223], v[84:87]
ds_read_b64_tr_b16 a[242:243], v4 offset:46592
v_mfma_f32_16x16x32_bf16 v[28:31], a[252:255], v[216:219], v[28:31]
ds_read_b64_tr_b16 a[244:245], v4 offset:46112
v_mfma_f32_16x16x32_bf16 v[88:91], a[252:255], v[220:223], v[88:91]
ds_read_b64_tr_b16 a[246:247], v4 offset:46624
s_waitcnt lgkmcnt(4)
v_mfma_f32_16x16x32_bf16 v[16:19], a[224:227], v[224:227], v[16:19]
ds_read_b64_tr_b16 a[248:249], v4 offset:46144
s_add_u32 s68, s22, 21520384
v_mfma_f32_16x16x32_bf16 v[76:79], a[224:227], v[228:231], v[76:79]
ds_read_b64_tr_b16 a[250:251], v4 offset:46656
s_add_u32 m0, s61, 4096
v_mfma_f32_16x16x32_bf16 v[20:23], a[228:231], v[224:227], v[20:23]
ds_read_b64_tr_b16 a[252:253], v4 offset:46176
buffer_load_dwordx4 v0, s[44:47], s68 offen sc1 lds
v_mfma_f32_16x16x32_bf16 v[80:83], a[228:231], v[228:231], v[80:83]
ds_read_b64_tr_b16 a[254:255], v4 offset:46688
v_mfma_f32_16x16x32_bf16 v[24:27], a[232:235], v[224:227], v[24:27]
ds_read_b64_tr_b16 a[224:225], v4 offset:51200
v_accvgpr_read_b32 v13, a193
v_mfma_f32_16x16x32_bf16 v[84:87], a[232:235], v[228:231], v[84:87]
ds_read_b64_tr_b16 a[226:227], v4 offset:51712
ds_read_b128 v[32:35], v13 offset:256
v_mfma_f32_16x16x32_bf16 v[28:31], a[236:239], v[224:227], v[28:31]
ds_read_b64_tr_b16 a[228:229], v4 offset:51232
ds_read_b128 v[92:95], v13 offset:320
v_mfma_f32_16x16x32_bf16 v[88:91], a[236:239], v[228:231], v[88:91]
ds_read_b64_tr_b16 a[230:231], v4 offset:51744
s_waitcnt lgkmcnt(6)
v_mfma_f32_16x16x32_bf16 v[16:19], a[240:243], v[232:235], v[16:19]
ds_read_b64_tr_b16 a[232:233], v4 offset:51264
s_waitcnt vmcnt(5)
v_mfma_f32_16x16x32_bf16 v[76:79], a[240:243], v[236:239], v[76:79]
ds_read_b64_tr_b16 a[234:235], v4 offset:51776
v_cmp_ne_u32 vcc, s62, v43
v_mfma_f32_16x16x32_bf16 v[20:23], a[244:247], v[232:235], v[20:23]
ds_read_b64_tr_b16 a[236:237], v4 offset:51296
s_cbranch_vccnz .Lpq_9s
.Lpq_9:
v_mfma_f32_16x16x32_bf16 v[80:83], a[244:247], v[236:239], v[80:83]
ds_read_b64_tr_b16 a[238:239], v4 offset:51808
s_add_u32 s67, s15, 4096
v_mfma_f32_16x16x32_bf16 v[24:27], a[248:251], v[232:235], v[24:27]
ds_read_b64_tr_b16 a[240:241], v4 offset:56320
buffer_load_dwordx4 a[48:51], v0, s[44:47], s67 offen sc1
v_mfma_f32_16x16x32_bf16 v[84:87], a[248:251], v[236:239], v[84:87]
ds_read_b64_tr_b16 a[242:243], v4 offset:56832
buffer_load_dwordx4 a[52:55], v0, s[44:47], s67 offen offset:1024 sc1
v_mfma_f32_16x16x32_bf16 v[28:31], a[252:255], v[232:235], v[28:31]
ds_read_b64_tr_b16 a[244:245], v4 offset:56352
v_mfma_f32_16x16x32_bf16 v[88:91], a[252:255], v[236:239], v[88:91]
ds_read_b64_tr_b16 a[246:247], v4 offset:56864
s_waitcnt lgkmcnt(4)
v_mfma_f32_16x16x32_bf16 v[16:19], a[224:227], v[240:243], v[16:19]
ds_read_b64_tr_b16 a[248:249], v4 offset:56384
buffer_load_dwordx4 a[56:59], v0, s[44:47], s67 offen offset:2048 sc1
v_mfma_f32_16x16x32_bf16 v[76:79], a[224:227], v[244:247], v[76:79]
ds_read_b64_tr_b16 a[250:251], v4 offset:56896
v_mfma_f32_16x16x32_bf16 v[20:23], a[228:231], v[240:243], v[20:23]
ds_read_b64_tr_b16 a[252:253], v4 offset:56416
v_mfma_f32_16x16x32_bf16 v[80:83], a[228:231], v[244:247], v[80:83]
ds_read_b64_tr_b16 a[254:255], v4 offset:56928
buffer_load_dwordx4 a[60:63], v0, s[44:47], s67 offen offset:3072 sc1
v_mfma_f32_16x16x32_bf16 v[24:27], a[232:235], v[240:243], v[24:27]
v_mfma_f32_16x16x32_bf16 v[84:87], a[232:235], v[244:247], v[84:87]
v_mfma_f32_16x16x32_bf16 v[28:31], a[236:239], v[240:243], v[28:31]
v_mfma_f32_16x16x32_bf16 v[88:91], a[236:239], v[244:247], v[88:91]
s_waitcnt lgkmcnt(0)
v_mfma_f32_16x16x32_bf16 v[16:19], a[240:243], v[248:251], v[16:19]
v_mfma_f32_16x16x32_bf16 v[76:79], a[240:243], v[252:255], v[76:79]
v_mfma_f32_16x16x32_bf16 v[20:23], a[244:247], v[248:251], v[20:23]
v_mfma_f32_16x16x32_bf16 v[80:83], a[244:247], v[252:255], v[80:83]
v_mfma_f32_16x16x32_bf16 v[24:27], a[248:251], v[248:251], v[24:27]
v_mfma_f32_16x16x32_bf16 v[84:87], a[248:251], v[252:255], v[84:87]
v_mfma_f32_16x16x32_bf16 v[28:31], a[252:255], v[248:251], v[28:31]
v_mfma_f32_16x16x32_bf16 v[88:91], a[252:255], v[252:255], v[88:91]
v_mov_b32 v224, s62
v_lshrrev_b32 v225, 2, v0
v_lshrrev_b32 v12, 1, v0
v_accvgpr_read_b32 v62, a208
s_mov_b32 s65, 19398656
buffer_load_dword v63, v62, s[44:47], s65 offen sc1
v_pk_fma_f32 v[16:17], v[16:17], v[14:15], v[32:33] op_sel:[0,0,0] op_sel_hi:[1,0,1]
v_pk_fma_f32 v[18:19], v[18:19], v[14:15], v[34:35] op_sel:[0,0,0] op_sel_hi:[1,0,1]
v_pk_fma_f32 v[76:77], v[76:77], v[14:15], v[32:33] op_sel:[0,1,0] op_sel_hi:[1,1,1]
v_pk_fma_f32 v[78:79], v[78:79], v[14:15], v[34:35] op_sel:[0,1,0] op_sel_hi:[1,1,1]
ds_read_b128 v[32:35], v13 offset:384
v_max_f32 v16, 0, v16
v_max_f32 v17, 0, v17
v_max_f32 v18, 0, v18
v_max_f32 v19, 0, v19
v_pk_mul_f32 v[16:17], v[16:17], v[16:17]
v_pk_mul_f32 v[18:19], v[18:19], v[18:19]
v_cvt_pk_bf16_f32 v176, v16, v17
v_cvt_pk_bf16_f32 v177, v18, v19
v_max_f32 v76, 0, v76
v_max_f32 v77, 0, v77
v_max_f32 v78, 0, v78
v_max_f32 v79, 0, v79
v_pk_mul_f32 v[76:77], v[76:77], v[76:77]
v_pk_mul_f32 v[78:79], v[78:79], v[78:79]
v_cvt_pk_bf16_f32 v180, v76, v77
v_cvt_pk_bf16_f32 v181, v78, v79
v_pk_fma_f32 v[20:21], v[20:21], v[14:15], v[92:93] op_sel:[0,0,0] op_sel_hi:[1,0,1]
v_pk_fma_f32 v[22:23], v[22:23], v[14:15], v[94:95] op_sel:[0,0,0] op_sel_hi:[1,0,1]
v_pk_fma_f32 v[80:81], v[80:81], v[14:15], v[92:93] op_sel:[0,1,0] op_sel_hi:[1,1,1]
v_pk_fma_f32 v[82:83], v[82:83], v[14:15], v[94:95] op_sel:[0,1,0] op_sel_hi:[1,1,1]
ds_read_b128 v[92:95], v13 offset:448
v_max_f32 v20, 0, v20
v_max_f32 v21, 0, v21
v_max_f32 v22, 0, v22
v_max_f32 v23, 0, v23
v_pk_mul_f32 v[20:21], v[20:21], v[20:21]
v_pk_mul_f32 v[22:23], v[22:23], v[22:23]
v_cvt_pk_bf16_f32 v178, v20, v21
v_cvt_pk_bf16_f32 v179, v22, v23
v_max_f32 v80, 0, v80
v_max_f32 v81, 0, v81
v_max_f32 v82, 0, v82
v_max_f32 v83, 0, v83
v_pk_mul_f32 v[80:81], v[80:81], v[80:81]
v_pk_mul_f32 v[82:83], v[82:83], v[82:83]
v_cvt_pk_bf16_f32 v182, v80, v81
v_cvt_pk_bf16_f32 v183, v82, v83
s_add_u32 s66, s14, 8192
buffer_store_dwordx4 v[176:179], v0, s[44:47], s66 offen sc1
buffer_store_dwordx4 v[180:183], v0, s[44:47], s66 offen offset:1024 sc1
s_barrier
ds_read_b64_tr_b16 a[224:225], v2
ds_read_b64_tr_b16 a[226:227], v2 offset:2560
ds_read_b64_tr_b16 a[228:229], v2 offset:8
ds_read_b64_tr_b16 a[230:231], v2 offset:2568
ds_read_b64_tr_b16 a[232:233], v2 offset:16
ds_read_b64_tr_b16 a[234:235], v2 offset:2576
ds_read_b64_tr_b16 a[236:237], v2 offset:24
ds_read_b64_tr_b16 a[238:239], v2 offset:2584
ds_read_b64_tr_b16 a[240:241], v2 offset:32
ds_read_b64_tr_b16 a[242:243], v2 offset:2592
ds_read_b64_tr_b16 a[244:245], v2 offset:5120
ds_read_b64_tr_b16 a[246:247], v2 offset:7680
s_waitcnt lgkmcnt(13)
v_pk_fma_f32 v[24:25], v[24:25], v[14:15], v[32:33] op_sel:[0,0,0] op_sel_hi:[1,0,1]
v_pk_fma_f32 v[26:27], v[26:27], v[14:15], v[34:35] op_sel:[0,0,0] op_sel_hi:[1,0,1]
v_pk_fma_f32 v[84:85], v[84:85], v[14:15], v[32:33] op_sel:[0,1,0] op_sel_hi:[1,1,1]
v_pk_fma_f32 v[86:87], v[86:87], v[14:15], v[34:35] op_sel:[0,1,0] op_sel_hi:[1,1,1]
v_max_f32 v24, 0, v24
v_max_f32 v25, 0, v25
v_max_f32 v26, 0, v26
v_max_f32 v27, 0, v27
v_pk_mul_f32 v[24:25], v[24:25], v[24:25]
v_pk_mul_f32 v[26:27], v[26:27], v[26:27]
v_cvt_pk_bf16_f32 v184, v24, v25
v_cvt_pk_bf16_f32 v185, v26, v27
v_max_f32 v84, 0, v84
v_max_f32 v85, 0, v85
v_max_f32 v86, 0, v86
v_max_f32 v87, 0, v87
v_pk_mul_f32 v[84:85], v[84:85], v[84:85]
v_pk_mul_f32 v[86:87], v[86:87], v[86:87]
v_cvt_pk_bf16_f32 v188, v84, v85
v_cvt_pk_bf16_f32 v189, v86, v87
s_waitcnt lgkmcnt(12)
v_pk_fma_f32 v[28:29], v[28:29], v[14:15], v[92:93] op_sel:[0,0,0] op_sel_hi:[1,0,1]
v_pk_fma_f32 v[30:31], v[30:31], v[14:15], v[94:95] op_sel:[0,0,0] op_sel_hi:[1,0,1]
v_pk_fma_f32 v[88:89], v[88:89], v[14:15], v[92:93] op_sel:[0,1,0] op_sel_hi:[1,1,1]
v_pk_fma_f32 v[90:91], v[90:91], v[14:15], v[94:95] op_sel:[0,1,0] op_sel_hi:[1,1,1]
v_max_f32 v28, 0, v28
v_max_f32 v29, 0, v29
v_max_f32 v30, 0, v30
v_max_f32 v31, 0, v31
v_pk_mul_f32 v[28:29], v[28:29], v[28:29]
v_pk_mul_f32 v[30:31], v[30:31], v[30:31]
v_cvt_pk_bf16_f32 v186, v28, v29
v_cvt_pk_bf16_f32 v187, v30, v31
v_max_f32 v88, 0, v88
v_max_f32 v89, 0, v89
v_max_f32 v90, 0, v90
v_max_f32 v91, 0, v91
v_pk_mul_f32 v[88:89], v[88:89], v[88:89]
v_pk_mul_f32 v[90:91], v[90:91], v[90:91]
v_cvt_pk_bf16_f32 v190, v88, v89
v_cvt_pk_bf16_f32 v191, v90, v91
buffer_store_dwordx4 v[184:187], v0, s[44:47], s66 offen offset:2048 sc1
buffer_store_dwordx4 v[188:191], v0, s[44:47], s66 offen offset:3072 sc1
s_waitcnt lgkmcnt(8)
v_mfma_f32_16x16x32_bf16 v[136:139], a[224:227], a[32:35], 0
ds_read_b64_tr_b16 a[248:249], v2 offset:5128
s_waitcnt vmcnt(4)
v_mfma_f32_16x16x32_bf16 v[156:159], a[224:227], a[36:39], 0
ds_read_b64_tr_b16 a[250:251], v2 offset:7688
v_cmp_ne_u32 vcc, s62, v63
v_mfma_f32_16x16x32_bf16 v[140:143], a[228:231], a[32:35], 0
ds_read_b64_tr_b16 a[252:253], v2 offset:5136
s_cbranch_vccnz .Lpq_10s
.Lpq_10:
v_mfma_f32_16x16x32_bf16 v[160:163], a[228:231], a[36:39], 0
ds_read_b64_tr_b16 a[254:255], v2 offset:7696
s_waitcnt lgkmcnt(8)
v_mfma_f32_16x16x32_bf16 v[144:147], a[232:235], a[32:35], 0
ds_read_b64_tr_b16 a[224:225], v2 offset:5144
buffer_load_dwordx4 a[0:3], v60, s[44:47], s16 offen sc1
v_mfma_f32_16x16x32_bf16 v[164:167], a[232:235], a[36:39], 0
ds_read_b64_tr_b16 a[226:227], v2 offset:7704
buffer_load_dwordx4 a[4:7], v61, s[44:47], s16 offen sc1
v_mfma_f32_16x16x32_bf16 v[148:151], a[236:239], a[32:35], 0
ds_read_b64_tr_b16 a[228:229], v2 offset:5152
buffer_load_dwordx4 a[8:11], v60, s[44:47], s16 offen offset:2048 sc1
v_mfma_f32_16x16x32_bf16 v[168:171], a[236:239], a[36:39], 0
ds_read_b64_tr_b16 a[230:231], v2 offset:7712
s_waitcnt lgkmcnt(4)
v_mfma_f32_16x16x32_bf16 v[152:155], a[240:243], a[32:35], 0
ds_read_b64_tr_b16 a[232:233], v2 offset:10240
v_mfma_f32_16x16x32_bf16 v[172:175], a[240:243], a[36:39], 0
ds_read_b64_tr_b16 a[234:235], v2 offset:12800
buffer_load_dwordx4 a[12:15], v61, s[44:47], s16 offen offset:2048 sc1
v_mfma_f32_16x16x32_bf16 v[136:139], a[244:247], a[40:43], v[136:139]
ds_read_b64_tr_b16 a[236:237], v2 offset:10248
v_mfma_f32_16x16x32_bf16 v[156:159], a[244:247], a[44:47], v[156:159]
ds_read_b64_tr_b16 a[238:239], v2 offset:12808
s_add_u32 s16, s16, 4096
v_mfma_f32_16x16x32_bf16 v[140:143], a[248:251], a[40:43], v[140:143]
ds_read_b64_tr_b16 a[240:241], v2 offset:10256
s_and_b32 s16, s16, 32767
v_mfma_f32_16x16x32_bf16 v[160:163], a[248:251], a[44:47], v[160:163]
ds_read_b64_tr_b16 a[242:243], v2 offset:12816
buffer_load_dwordx4 a[16:19], v60, s[44:47], s16 offen sc1
v_mfma_f32_16x16x32_bf16 v[144:147], a[252:255], a[40:43], v[144:147]
ds_read_b64_tr_b16 a[244:245], v2 offset:10264
buffer_load_dwordx4 a[20:23], v61, s[44:47], s16 offen sc1
v_mfma_f32_16x16x32_bf16 v[164:167], a[252:255], a[44:47], v[164:167]
ds_read_b64_tr_b16 a[246:247], v2 offset:12824
s_waitcnt lgkmcnt(4)
v_mfma_f32_16x16x32_bf16 v[148:151], a[224:227], a[40:43], v[148:151]
ds_read_b64_tr_b16 a[248:249], v2 offset:10272
v_mfma_f32_16x16x32_bf16 v[168:171], a[224:227], a[44:47], v[168:171]
ds_read_b64_tr_b16 a[250:251], v2 offset:12832
buffer_load_dwordx4 a[24:27], v60, s[44:47], s16 offen offset:2048 sc1
v_mfma_f32_16x16x32_bf16 v[152:155], a[228:231], a[40:43], v[152:155]
ds_read_b64_tr_b16 a[252:253], v2 offset:15360
v_mfma_f32_16x16x32_bf16 v[172:175], a[228:231], a[44:47], v[172:175]
ds_read_b64_tr_b16 a[254:255], v2 offset:17920
v_mfma_f32_16x16x32_bf16 v[136:139], a[232:235], a[48:51], v[136:139]
ds_read_b64_tr_b16 a[224:225], v2 offset:15368
buffer_load_dwordx4 a[28:31], v61, s[44:47], s16 offen offset:2048 sc1
v_mfma_f32_16x16x32_bf16 v[156:159], a[232:235], a[52:55], v[156:159]
ds_read_b64_tr_b16 a[226:227], v2 offset:17928
s_waitcnt vmcnt(7)
v_mfma_f32_16x16x16_bf16 v[240:243], a[222:223], a[0:1], 0
s_add_u32 s16, s16, 4096
s_and_b32 s16, s16, 32767
v_mfma_f32_16x16x16_bf16 v[244:247], a[222:223], a[2:3], 0
buffer_load_dwordx4 a[64:67], v60, s[44:47], s16 offen sc1
v_mfma_f32_16x16x32_bf16 v[140:143], a[236:239], a[48:51], v[140:143]
ds_read_b64_tr_b16 a[228:229], v2 offset:15376
v_mfma_f32_16x16x32_bf16 v[160:163], a[236:239], a[52:55], v[160:163]
ds_read_b64_tr_b16 a[230:231], v2 offset:17936
s_waitcnt vmcnt(7) lgkmcnt(4)
v_mfma_f32_16x16x16_bf16 v[248:251], a[222:223], a[4:5], 0
buffer_load_dwordx4 a[68:71], v61, s[44:47], s16 offen sc1
v_mfma_f32_16x16x16_bf16 v[252:255], a[222:223], a[6:7], 0
v_mfma_f32_16x16x32_bf16 v[144:147], a[240:243], a[48:51], v[144:147]
ds_read_b64_tr_b16 a[232:233], v2 offset:15384
buffer_load_dwordx4 a[72:75], v60, s[44:47], s16 offen offset:2048 sc1
v_mfma_f32_16x16x32_bf16 v[164:167], a[240:243], a[52:55], v[164:167]
ds_read_b64_tr_b16 a[234:235], v2 offset:17944
s_waitcnt vmcnt(8)
v_mfma_f32_16x16x16_bf16 v[240:243], a[222:223], a[8:9], v[240:243]
v_mfma_f32_16x16x16_bf16 v[244:247], a[222:223], a[10:11], v[244:247]
buffer_load_dwordx4 a[76:79], v61, s[44:47], s16 offen offset:2048 sc1
v_mfma_f32_16x16x32_bf16 v[148:151], a[244:247], a[48:51], v[148:151]
ds_read_b64_tr_b16 a[236:237], v2 offset:15392
v_mfma_f32_16x16x32_bf16 v[168:171], a[244:247], a[52:55], v[168:171]
ds_read_b64_tr_b16 a[238:239], v2 offset:17952
s_add_u32 s16, s16, 4096
s_waitcnt vmcnt(8)
v_mfma_f32_16x16x16_bf16 v[248:251], a[222:223], a[12:13], v[248:251]
s_and_b32 s16, s16, 32767
buffer_load_dwordx4 a[80:83], v60, s[44:47], s16 offen sc1
v_mfma_f32_16x16x16_bf16 v[252:255], a[222:223], a[14:15], v[252:255]
v_mfma_f32_16x16x32_bf16 v[152:155], a[248:251], a[48:51], v[152:155]
buffer_load_dwordx4 a[84:87], v61, s[44:47], s16 offen sc1
v_mfma_f32_16x16x32_bf16 v[172:175], a[248:251], a[52:55], v[172:175]
s_waitcnt vmcnt(9)
v_mfma_f32_16x16x16_bf16 v[240:243], a[222:223], a[16:17], v[240:243]
v_mfma_f32_16x16x16_bf16 v[244:247], a[222:223], a[18:19], v[244:247]
buffer_load_dwordx4 a[88:91], v60, s[44:47], s16 offen offset:2048 sc1
v_mfma_f32_16x16x32_bf16 v[136:139], a[252:255], a[56:59], v[136:139]
v_mfma_f32_16x16x32_bf16 v[156:159], a[252:255], a[60:63], v[156:159]
s_waitcnt vmcnt(9) lgkmcnt(0)
v_mfma_f32_16x16x16_bf16 v[248:251], a[222:223], a[20:21], v[248:251]
buffer_load_dwordx4 a[92:95], v61, s[44:47], s16 offen offset:2048 sc1
v_mfma_f32_16x16x16_bf16 v[252:255], a[222:223], a[22:23], v[252:255]
v_mfma_f32_16x16x32_bf16 v[140:143], a[224:227], a[56:59], v[140:143]
s_add_u32 s65, s64, 262144
buffer_store_dword v224, v225, s[44:47], s65 offen sc1
v_mfma_f32_16x16x32_bf16 v[160:163], a[224:227], a[60:63], v[160:163]
s_add_u32 s16, s16, 4096
s_and_b32 s16, s16, 32767
s_waitcnt vmcnt(10)
v_mfma_f32_16x16x16_bf16 v[240:243], a[222:223], a[24:25], v[240:243]
buffer_load_dwordx4 a[96:99], v60, s[44:47], s16 offen sc1
v_mfma_f32_16x16x16_bf16 v[244:247], a[222:223], a[26:27], v[244:247]
buffer_load_dwordx4 a[100:103], v61, s[44:47], s16 offen sc1
v_mfma_f32_16x16x32_bf16 v[144:147], a[228:231], a[56:59], v[144:147]
v_mfma_f32_16x16x32_bf16 v[164:167], a[228:231], a[60:63], v[164:167]
s_waitcnt vmcnt(11)
v_mfma_f32_16x16x16_bf16 v[248:251], a[222:223], a[28:29], v[248:251]
buffer_load_dwordx4 a[104:107], v60, s[44:47], s16 offen offset:2048 sc1
v_mfma_f32_16x16x16_bf16 v[252:255], a[222:223], a[30:31], v[252:255]
v_mfma_f32_16x16x32_bf16 v[148:151], a[232:235], a[56:59], v[148:151]
v_mfma_f32_16x16x32_bf16 v[168:171], a[232:235], a[60:63], v[168:171]
buffer_load_dwordx4 a[108:111], v61, s[44:47], s16 offen offset:2048 sc1
s_waitcnt vmcnt(12)
v_mfma_f32_16x16x16_bf16 v[240:243], a[222:223], a[64:65], v[240:243]
v_mfma_f32_16x16x16_bf16 v[244:247], a[222:223], a[66:67], v[244:247]
s_add_u32 s16, s16, 4096
s_and_b32 s16, s16, 32767
v_mfma_f32_16x16x32_bf16 v[152:155], a[236:239], a[56:59], v[152:155]
buffer_load_dwordx4 a[112:115], v60, s[44:47], s16 offen sc1
v_mfma_f32_16x16x32_bf16 v[172:175], a[236:239], a[60:63], v[172:175]
s_waitcnt vmcnt(12)
v_mfma_f32_16x16x16_bf16 v[248:251], a[222:223], a[68:69], v[248:251]
buffer_load_dwordx4 a[116:119], v61, s[44:47], s16 offen sc1
v_mfma_f32_16x16x16_bf16 v[252:255], a[222:223], a[70:71], v[252:255]
s_waitcnt vmcnt(12)
v_mfma_f32_16x16x16_bf16 v[240:243], a[222:223], a[72:73], v[240:243]
v_mfma_f32_16x16x16_bf16 v[244:247], a[222:223], a[74:75], v[244:247]
buffer_load_dwordx4 v[16:19], v60, s[44:47], s16 offen offset:2048 sc1
s_waitcnt vmcnt(12)
v_mfma_f32_16x16x16_bf16 v[248:251], a[222:223], a[76:77], v[248:251]
v_mfma_f32_16x16x16_bf16 v[252:255], a[222:223], a[78:79], v[252:255]
s_waitcnt vmcnt(11)
v_mfma_f32_16x16x16_bf16 v[240:243], a[222:223], a[80:81], v[240:243]
buffer_load_dwordx4 v[20:23], v61, s[44:47], s16 offen offset:2048 sc1
v_mfma_f32_16x16x16_bf16 v[244:247], a[222:223], a[82:83], v[244:247]
s_waitcnt vmcnt(11)
v_mfma_f32_16x16x16_bf16 v[248:251], a[222:223], a[84:85], v[248:251]
s_add_u32 s16, s16, 4096
s_and_b32 s16, s16, 32767
v_mfma_f32_16x16x16_bf16 v[252:255], a[222:223], a[86:87], v[252:255]
buffer_load_dwordx4 v[24:27], v60, s[44:47], s16 offen sc1
s_waitcnt vmcnt(11)
v_mfma_f32_16x16x16_bf16 v[240:243], a[222:223], a[88:89], v[240:243]
v_mfma_f32_16x16x16_bf16 v[244:247], a[222:223], a[90:91], v[244:247]
buffer_load_dwordx4 v[28:31], v61, s[44:47], s16 offen sc1
s_waitcnt vmcnt(11)
v_mfma_f32_16x16x16_bf16 v[248:251], a[222:223], a[92:93], v[248:251]
v_mfma_f32_16x16x16_bf16 v[252:255], a[222:223], a[94:95], v[252:255]
s_waitcnt vmcnt(9)
v_mfma_f32_16x16x16_bf16 v[240:243], a[222:223], a[96:97], v[240:243]
buffer_load_dwordx4 v[32:35], v60, s[44:47], s16 offen offset:2048 sc1
v_mfma_f32_16x16x16_bf16 v[244:247], a[222:223], a[98:99], v[244:247]
s_waitcnt vmcnt(9)
v_mfma_f32_16x16x16_bf16 v[248:251], a[222:223], a[100:101], v[248:251]
v_mfma_f32_16x16x16_bf16 v[252:255], a[222:223], a[102:103], v[252:255]
buffer_load_dwordx4 v[36:39], v61, s[44:47], s16 offen offset:2048 sc1
s_waitcnt vmcnt(9)
v_mfma_f32_16x16x16_bf16 v[240:243], a[222:223], a[104:105], v[240:243]
v_mfma_f32_16x16x16_bf16 v[244:247], a[222:223], a[106:107], v[244:247]
s_add_u32 s16, s16, 4096
s_and_b32 s16, s16, 32767
s_waitcnt vmcnt(8)
v_mfma_f32_16x16x16_bf16 v[248:251], a[222:223], a[108:109], v[248:251]
buffer_load_dwordx4 v[40:43], v60, s[44:47], s16 offen sc1
v_mfma_f32_16x16x16_bf16 v[252:255], a[222:223], a[110:111], v[252:255]
s_waitcnt vmcnt(8)
v_mfma_f32_16x16x16_bf16 v[240:243], a[222:223], a[112:113], v[240:243]
buffer_load_dwordx4 v[44:47], v61, s[44:47], s16 offen sc1
v_mfma_f32_16x16x16_bf16 v[244:247], a[222:223], a[114:115], v[244:247]
s_waitcnt vmcnt(8)
v_mfma_f32_16x16x16_bf16 v[248:251], a[222:223], a[116:117], v[248:251]
v_mfma_f32_16x16x16_bf16 v[252:255], a[222:223], a[118:119], v[252:255]
buffer_load_dwordx4 v[48:51], v60, s[44:47], s16 offen offset:2048 sc1
s_waitcnt vmcnt(8)
v_mfma_f32_16x16x16_bf16 v[240:243], a[222:223], v[16:17], v[240:243]
v_mfma_f32_16x16x16_bf16 v[244:247], a[222:223], v[18:19], v[244:247]
s_waitcnt vmcnt(7)
v_mfma_f32_16x16x16_bf16 v[248:251], a[222:223], v[20:21], v[248:251]
buffer_load_dwordx4 v[52:55], v61, s[44:47], s16 offen offset:2048 sc1
v_mfma_f32_16x16x16_bf16 v[252:255], a[222:223], v[22:23], v[252:255]
s_waitcnt vmcnt(7)
v_mfma_f32_16x16x16_bf16 v[240:243], a[222:223], v[24:25], v[240:243]
s_add_u32 s68, s22, 21495808
s_add_u32 m0, s61, 20480
v_mfma_f32_16x16x16_bf16 v[244:247], a[222:223], v[26:27], v[244:247]
buffer_load_dwordx4 v0, s[44:47], s68 offen sc1 lds
s_waitcnt vmcnt(7)
v_mfma_f32_16x16x16_bf16 v[248:251], a[222:223], v[28:29], v[248:251]
v_mfma_f32_16x16x16_bf16 v[252:255], a[222:223], v[30:31], v[252:255]
)ASM"
R"ASM(
s_waitcnt vmcnt(6)
v_mfma_f32_16x16x16_bf16 v[240:243], a[222:223], v[32:33], v[240:243]
buffer_load_dwordx4 v0, s[44:47], s68 offen offset:1024 sc1 lds
v_mfma_f32_16x16x16_bf16 v[244:247], a[222:223], v[34:35], v[244:247]
s_waitcnt vmcnt(6)
v_mfma_f32_16x16x16_bf16 v[248:251], a[222:223], v[36:37], v[248:251]
v_mfma_f32_16x16x16_bf16 v[252:255], a[222:223], v[38:39], v[252:255]
s_waitcnt vmcnt(5)
v_mfma_f32_16x16x16_bf16 v[240:243], a[222:223], v[40:41], v[240:243]
buffer_load_dwordx4 v0, s[44:47], s68 offen offset:2048 sc1 lds
v_mfma_f32_16x16x16_bf16 v[244:247], a[222:223], v[42:43], v[244:247]
s_waitcnt vmcnt(5)
v_mfma_f32_16x16x16_bf16 v[248:251], a[222:223], v[44:45], v[248:251]
v_mfma_f32_16x16x16_bf16 v[252:255], a[222:223], v[46:47], v[252:255]
s_waitcnt vmcnt(4)
v_mfma_f32_16x16x16_bf16 v[240:243], a[222:223], v[48:49], v[240:243]
buffer_load_dwordx4 v0, s[44:47], s68 offen offset:3072 sc1 lds
v_mfma_f32_16x16x16_bf16 v[244:247], a[222:223], v[50:51], v[244:247]
s_waitcnt vmcnt(4)
v_mfma_f32_16x16x16_bf16 v[248:251], a[222:223], v[52:53], v[248:251]
v_mfma_f32_16x16x16_bf16 v[252:255], a[222:223], v[54:55], v[252:255]
v_accvgpr_read_b32 v228, a209
s_mov_b32 s65, 19660800
buffer_load_dword v229, v228, s[44:47], s65 offen sc1
v_accvgpr_read_b32 v4, a202
v_accvgpr_read_b32 v5, a203
v_accvgpr_read_b32 v6, a204
v_cvt_pk_bf16_f32 v232, v240, v241
v_cvt_pk_bf16_f32 v233, v242, v243
v_cvt_pk_bf16_f32 v234, v244, v245
v_cvt_pk_bf16_f32 v235, v246, v247
v_cvt_pk_bf16_f32 v236, v248, v249
v_cvt_pk_bf16_f32 v237, v250, v251
v_cvt_pk_bf16_f32 v238, v252, v253
v_cvt_pk_bf16_f32 v239, v254, v255
ds_write_b64 v4, v[232:233]
ds_write_b64 v4, v[234:235] offset:32
ds_write_b64 v5, v[236:237]
ds_write_b64 v6, v[238:239]
v_accvgpr_read_b32 v4, a191
v_accvgpr_read_b32 v5, a192
s_waitcnt lgkmcnt(0)
s_barrier
ds_read_b128 a[224:227], v5
ds_read_b128 a[228:231], v5 offset:16
ds_read_b128 a[232:235], v5 offset:32
ds_read_b128 a[236:239], v5 offset:48
ds_read_b128 a[240:243], v5 offset:64
ds_read_b128 a[244:247], v4
s_waitcnt lgkmcnt(5)
v_mfma_f32_16x16x32_bf16 v[192:195], a[224:227], a[120:123], 0
ds_read_b128 a[248:251], v4 offset:16
s_add_u32 s68, s22, 21499904
v_mfma_f32_16x16x32_bf16 v[196:199], a[224:227], a[140:143], 0
s_add_u32 m0, s61, 24576
s_nop 0
buffer_load_dwordx4 v0, s[44:47], s68 offen sc1 lds
s_waitcnt lgkmcnt(5)
v_mfma_f32_16x16x32_bf16 v[192:195], a[228:231], a[124:127], v[192:195]
ds_read_b128 a[252:255], v4 offset:32
s_add_u32 s68, s40, 23461888
v_mfma_f32_16x16x32_bf16 v[196:199], a[228:231], a[144:147], v[196:199]
s_add_u32 m0, s61, 40960
s_nop 0
buffer_load_dwordx4 v0, s[44:47], s68 offen sc1 lds
s_waitcnt lgkmcnt(5)
v_mfma_f32_16x16x32_bf16 v[192:195], a[232:235], a[128:131], v[192:195]
ds_read_b128 a[224:227], v4 offset:48
buffer_load_dwordx4 v0, s[44:47], s68 offen offset:1024 sc1 lds
v_mfma_f32_16x16x32_bf16 v[196:199], a[232:235], a[148:151], v[196:199]
s_waitcnt lgkmcnt(3)
v_mfma_f32_16x16x32_bf16 v[192:195], a[236:239], a[132:135], v[192:195]
ds_read_b128 a[228:231], v4 offset:64
s_waitcnt vmcnt(3)
v_mfma_f32_16x16x32_bf16 v[196:199], a[236:239], a[152:155], v[196:199]
v_cmp_ne_u32 vcc, s62, v229
s_cbranch_vccnz .Lpq_11s
.Lpq_11:
v_mfma_f32_16x16x32_bf16 v[192:195], a[240:243], a[136:139], v[192:195]
ds_read_b128 a[232:235], v4 offset:320
s_add_u32 s67, s15, 8192
v_mfma_f32_16x16x32_bf16 v[196:199], a[240:243], a[156:159], v[196:199]
buffer_load_dwordx4 a[80:83], v0, s[44:47], s67 offen sc1
v_mfma_f32_16x16x32_bf16 v[36:39], a[244:247], a[120:123], 0
ds_read_b128 a[236:239], v4 offset:336
v_mfma_f32_16x16x32_bf16 v[56:59], a[244:247], a[140:143], 0
s_waitcnt lgkmcnt(3)
buffer_load_dwordx4 a[84:87], v0, s[44:47], s67 offen offset:1024 sc1
v_mfma_f32_16x16x32_bf16 v[36:39], a[248:251], a[124:127], v[36:39]
ds_read_b128 a[240:243], v4 offset:352
v_mfma_f32_16x16x32_bf16 v[56:59], a[248:251], a[144:147], v[56:59]
v_add_f32 v200, 897988541, v192
v_add_f32 v201, 897988541, v196
v_mfma_f32_16x16x32_bf16 v[36:39], a[252:255], a[128:131], v[36:39]
ds_read_b128 a[244:247], v4 offset:368
v_rcp_f32 v200, v200
v_mfma_f32_16x16x32_bf16 v[56:59], a[252:255], a[148:151], v[56:59]
buffer_load_dwordx4 a[88:91], v0, s[44:47], s67 offen offset:2048 sc1
v_rcp_f32 v201, v201
v_mfma_f32_16x16x32_bf16 v[36:39], a[224:227], a[132:135], v[36:39]
ds_read_b128 a[248:251], v4 offset:384
v_mfma_f32_16x16x32_bf16 v[56:59], a[224:227], a[152:155], v[56:59]
s_waitcnt lgkmcnt(3)
v_mfma_f32_16x16x32_bf16 v[36:39], a[228:231], a[136:139], v[36:39]
ds_read_b128 a[252:255], v4 offset:640
v_mfma_f32_16x16x32_bf16 v[56:59], a[228:231], a[156:159], v[56:59]
buffer_load_dwordx4 a[92:95], v0, s[44:47], s67 offen offset:3072 sc1
v_mfma_f32_16x16x32_bf16 v[40:43], a[232:235], a[120:123], 0
ds_read_b128 a[224:227], v4 offset:656
v_mfma_f32_16x16x32_bf16 v[60:63], a[232:235], a[140:143], 0
v_mfma_f32_16x16x32_bf16 v[40:43], a[236:239], a[124:127], v[40:43]
ds_read_b128 a[228:231], v4 offset:672
v_mfma_f32_16x16x32_bf16 v[60:63], a[236:239], a[144:147], v[60:63]
s_waitcnt lgkmcnt(3)
buffer_load_dwordx4 v0, s[44:47], s68 offen offset:2048 sc1 lds
v_mfma_f32_16x16x32_bf16 v[40:43], a[240:243], a[128:131], v[40:43]
ds_read_b128 a[232:235], v4 offset:688
v_mul_f32 v36, v200, v36
v_mfma_f32_16x16x32_bf16 v[60:63], a[240:243], a[148:151], v[60:63]
v_mul_f32 v37, v200, v37
v_mul_f32 v38, v200, v38
v_mfma_f32_16x16x32_bf16 v[40:43], a[244:247], a[132:135], v[40:43]
ds_read_b128 a[236:239], v4 offset:704
v_mul_f32 v39, v200, v39
v_mfma_f32_16x16x32_bf16 v[60:63], a[244:247], a[152:155], v[60:63]
buffer_load_dwordx4 v0, s[44:47], s68 offen offset:3072 sc1 lds
v_cvt_pk_bf16_f32 v204, v36, v37
v_mfma_f32_16x16x32_bf16 v[40:43], a[248:251], a[136:139], v[40:43]
ds_read_b128 a[240:243], v4 offset:960
v_cvt_pk_bf16_f32 v205, v38, v39
v_mfma_f32_16x16x32_bf16 v[60:63], a[248:251], a[156:159], v[60:63]
s_waitcnt lgkmcnt(3)
v_mul_f32 v56, v201, v56
v_mfma_f32_16x16x32_bf16 v[44:47], a[252:255], a[120:123], 0
ds_read_b128 a[244:247], v4 offset:976
v_accvgpr_read_b32 v226, a210
v_mfma_f32_16x16x32_bf16 v[64:67], a[252:255], a[140:143], 0
s_mov_b32 s65, 19660800
buffer_load_dword v227, v226, s[44:47], s65 offen sc1
v_mfma_f32_16x16x32_bf16 v[44:47], a[224:227], a[124:127], v[44:47]
ds_read_b128 a[248:251], v4 offset:992
v_mul_f32 v57, v201, v57
v_mfma_f32_16x16x32_bf16 v[64:67], a[224:227], a[144:147], v[64:67]
v_mul_f32 v58, v201, v58
v_mul_f32 v59, v201, v59
v_mfma_f32_16x16x32_bf16 v[44:47], a[228:231], a[128:131], v[44:47]
ds_read_b128 a[252:255], v4 offset:1008
v_cvt_pk_bf16_f32 v214, v56, v57
v_mfma_f32_16x16x32_bf16 v[64:67], a[228:231], a[148:151], v[64:67]
s_waitcnt lgkmcnt(3)
v_cvt_pk_bf16_f32 v215, v58, v59
v_mfma_f32_16x16x32_bf16 v[44:47], a[232:235], a[132:135], v[44:47]
ds_read_b128 a[224:227], v4 offset:1024
v_mul_f32 v40, v200, v40
v_mfma_f32_16x16x32_bf16 v[64:67], a[232:235], a[152:155], v[64:67]
v_mul_f32 v41, v200, v41
v_mul_f32 v42, v200, v42
v_mfma_f32_16x16x32_bf16 v[44:47], a[236:239], a[136:139], v[44:47]
ds_read_b128 a[228:231], v4 offset:1280
v_mul_f32 v43, v200, v43
v_mfma_f32_16x16x32_bf16 v[64:67], a[236:239], a[156:159], v[64:67]
v_cvt_pk_bf16_f32 v206, v40, v41
v_cvt_pk_bf16_f32 v207, v42, v43
v_mfma_f32_16x16x32_bf16 v[48:51], a[240:243], a[120:123], 0
ds_read_b128 a[232:235], v4 offset:1296
buffer_store_dwordx4 v[204:207], v0, s[44:47], s17 offen sc1
v_mfma_f32_16x16x32_bf16 v[68:71], a[240:243], a[140:143], 0
s_waitcnt lgkmcnt(3)
v_mul_f32 v60, v201, v60
v_mfma_f32_16x16x32_bf16 v[48:51], a[244:247], a[124:127], v[48:51]
ds_read_b128 a[236:239], v4 offset:1312
v_mul_f32 v61, v201, v61
v_mfma_f32_16x16x32_bf16 v[68:71], a[244:247], a[144:147], v[68:71]
v_mul_f32 v62, v201, v62
v_mul_f32 v63, v201, v63
v_mfma_f32_16x16x32_bf16 v[48:51], a[248:251], a[128:131], v[48:51]
ds_read_b128 a[240:243], v4 offset:1328
v_cvt_pk_bf16_f32 v216, v60, v61
v_mfma_f32_16x16x32_bf16 v[68:71], a[248:251], a[148:151], v[68:71]
v_cvt_pk_bf16_f32 v217, v62, v63
buffer_store_dwordx4 v[214:217], v0, s[44:47], s18 offen sc1
v_mfma_f32_16x16x32_bf16 v[48:51], a[252:255], a[132:135], v[48:51]
ds_read_b128 a[244:247], v4 offset:1344
v_mul_f32 v44, v200, v44
v_mfma_f32_16x16x32_bf16 v[68:71], a[252:255], a[152:155], v[68:71]
s_waitcnt lgkmcnt(3)
v_mul_f32 v45, v200, v45
v_mfma_f32_16x16x32_bf16 v[48:51], a[224:227], a[136:139], v[48:51]
v_mul_f32 v46, v200, v46
v_mul_f32 v47, v200, v47
v_mfma_f32_16x16x32_bf16 v[68:71], a[224:227], a[156:159], v[68:71]
v_cvt_pk_bf16_f32 v208, v44, v45
v_cvt_pk_bf16_f32 v209, v46, v47
v_mfma_f32_16x16x32_bf16 v[52:55], a[228:231], a[120:123], 0
v_mul_f32 v64, v201, v64
v_mul_f32 v65, v201, v65
v_mfma_f32_16x16x32_bf16 v[72:75], a[228:231], a[140:143], 0
v_mul_f32 v66, v201, v66
v_mul_f32 v67, v201, v67
v_mfma_f32_16x16x32_bf16 v[52:55], a[232:235], a[124:127], v[52:55]
v_cvt_pk_bf16_f32 v218, v64, v65
v_cvt_pk_bf16_f32 v219, v66, v67
v_mfma_f32_16x16x32_bf16 v[72:75], a[232:235], a[144:147], v[72:75]
s_waitcnt lgkmcnt(0)
v_mul_f32 v48, v200, v48
v_mfma_f32_16x16x32_bf16 v[52:55], a[236:239], a[128:131], v[52:55]
v_mul_f32 v49, v200, v49
v_mul_f32 v50, v200, v50
v_mfma_f32_16x16x32_bf16 v[72:75], a[236:239], a[148:151], v[72:75]
v_mul_f32 v51, v200, v51
v_cvt_pk_bf16_f32 v210, v48, v49
v_mfma_f32_16x16x32_bf16 v[52:55], a[240:243], a[132:135], v[52:55]
v_cvt_pk_bf16_f32 v211, v50, v51
buffer_store_dwordx4 v[208:211], v0, s[44:47], s17 offen offset:1024 sc1
v_mfma_f32_16x16x32_bf16 v[72:75], a[240:243], a[152:155], v[72:75]
v_mul_f32 v68, v201, v68
v_mul_f32 v69, v201, v69
v_mfma_f32_16x16x32_bf16 v[52:55], a[244:247], a[136:139], v[52:55]
v_mul_f32 v70, v201, v70
v_mul_f32 v71, v201, v71
v_mfma_f32_16x16x32_bf16 v[72:75], a[244:247], a[156:159], v[72:75]
v_cvt_pk_bf16_f32 v220, v68, v69
v_cvt_pk_bf16_f32 v221, v70, v71
buffer_store_dwordx4 v[218:221], v0, s[44:47], s18 offen offset:1024 sc1
s_barrier
ds_read_b64_tr_b16 a[224:225], v2 offset:20480
ds_read_b64_tr_b16 a[226:227], v2 offset:23040
ds_read_b64_tr_b16 a[228:229], v2 offset:20488
ds_read_b64_tr_b16 a[230:231], v2 offset:23048
ds_read_b64_tr_b16 a[232:233], v2 offset:20496
ds_read_b64_tr_b16 a[234:235], v2 offset:23056
ds_read_b64_tr_b16 a[236:237], v2 offset:20504
ds_read_b64_tr_b16 a[238:239], v2 offset:23064
ds_read_b64_tr_b16 a[240:241], v2 offset:20512
ds_read_b64_tr_b16 a[242:243], v2 offset:23072
ds_read_b64_tr_b16 a[244:245], v2 offset:25600
ds_read_b64_tr_b16 a[246:247], v2 offset:28160
v_pk_mul_f32 v[52:53], v[52:53], v[200:201] op_sel:[0,0] op_sel_hi:[1,0]
v_pk_mul_f32 v[54:55], v[54:55], v[200:201] op_sel:[0,0] op_sel_hi:[1,0]
v_cvt_pk_bf16_f32 v212, v52, v53
v_cvt_pk_bf16_f32 v213, v54, v55
buffer_store_dwordx2 v[212:213], v12, s[44:47], s17 offen offset:2048 sc1
v_pk_mul_f32 v[72:73], v[72:73], v[200:201] op_sel:[0,1] op_sel_hi:[1,1]
v_pk_mul_f32 v[74:75], v[74:75], v[200:201] op_sel:[0,1] op_sel_hi:[1,1]
v_cvt_pk_bf16_f32 v222, v72, v73
v_cvt_pk_bf16_f32 v223, v74, v75
buffer_store_dwordx2 v[222:223], v12, s[44:47], s18 offen offset:2048 sc1
s_waitcnt lgkmcnt(8)
v_mfma_f32_16x16x32_bf16 v[96:99], a[224:227], a[32:35], 0
ds_read_b64_tr_b16 a[248:249], v2 offset:25608
s_waitcnt vmcnt(6)
v_mfma_f32_16x16x32_bf16 v[116:119], a[224:227], a[36:39], 0
ds_read_b64_tr_b16 a[250:251], v2 offset:28168
v_cmp_ne_u32 vcc, s62, v227
v_mfma_f32_16x16x32_bf16 v[100:103], a[228:231], a[32:35], 0
ds_read_b64_tr_b16 a[252:253], v2 offset:25616
s_cbranch_vccnz .Lpq_12s
.Lpq_12:
v_mfma_f32_16x16x32_bf16 v[120:123], a[228:231], a[36:39], 0
ds_read_b64_tr_b16 a[254:255], v2 offset:28176
s_waitcnt lgkmcnt(8)
v_mfma_f32_16x16x32_bf16 v[104:107], a[232:235], a[32:35], 0
ds_read_b64_tr_b16 a[224:225], v2 offset:25624
buffer_load_dwordx4 a[0:3], v0, s[44:47], s14 offen sc1
v_mfma_f32_16x16x32_bf16 v[124:127], a[232:235], a[36:39], 0
ds_read_b64_tr_b16 a[226:227], v2 offset:28184
buffer_load_dwordx4 a[4:7], v0, s[44:47], s14 offen offset:1024 sc1
v_mfma_f32_16x16x32_bf16 v[108:111], a[236:239], a[32:35], 0
ds_read_b64_tr_b16 a[228:229], v2 offset:25632
v_mfma_f32_16x16x32_bf16 v[128:131], a[236:239], a[36:39], 0
ds_read_b64_tr_b16 a[230:231], v2 offset:28192
s_waitcnt lgkmcnt(4)
v_mfma_f32_16x16x32_bf16 v[112:115], a[240:243], a[32:35], 0
ds_read_b64_tr_b16 a[232:233], v2 offset:30720
buffer_load_dwordx4 a[8:11], v0, s[44:47], s14 offen offset:2048 sc1
v_mfma_f32_16x16x32_bf16 v[132:135], a[240:243], a[36:39], 0
ds_read_b64_tr_b16 a[234:235], v2 offset:33280
v_mfma_f32_16x16x32_bf16 v[96:99], a[244:247], a[40:43], v[96:99]
ds_read_b64_tr_b16 a[236:237], v2 offset:30728
v_mfma_f32_16x16x32_bf16 v[116:119], a[244:247], a[44:47], v[116:119]
ds_read_b64_tr_b16 a[238:239], v2 offset:33288
v_mfma_f32_16x16x32_bf16 v[100:103], a[248:251], a[40:43], v[100:103]
ds_read_b64_tr_b16 a[240:241], v2 offset:30736
buffer_load_dwordx4 a[12:15], v0, s[44:47], s14 offen offset:3072 sc1
v_mfma_f32_16x16x32_bf16 v[120:123], a[248:251], a[44:47], v[120:123]
ds_read_b64_tr_b16 a[242:243], v2 offset:33296
v_mfma_f32_16x16x32_bf16 v[104:107], a[252:255], a[40:43], v[104:107]
ds_read_b64_tr_b16 a[244:245], v2 offset:30744
v_mfma_f32_16x16x32_bf16 v[124:127], a[252:255], a[44:47], v[124:127]
ds_read_b64_tr_b16 a[246:247], v2 offset:33304
s_waitcnt lgkmcnt(4)
v_mfma_f32_16x16x32_bf16 v[108:111], a[224:227], a[40:43], v[108:111]
ds_read_b64_tr_b16 a[248:249], v2 offset:30752
buffer_load_dwordx4 a[16:19], v0, s[44:47], s15 offen sc1
v_mfma_f32_16x16x32_bf16 v[128:131], a[224:227], a[44:47], v[128:131]
ds_read_b64_tr_b16 a[250:251], v2 offset:33312
v_mfma_f32_16x16x32_bf16 v[112:115], a[228:231], a[40:43], v[112:115]
ds_read_b64_tr_b16 a[252:253], v2 offset:35840
v_mfma_f32_16x16x32_bf16 v[132:135], a[228:231], a[44:47], v[132:135]
ds_read_b64_tr_b16 a[254:255], v2 offset:38400
v_mfma_f32_16x16x32_bf16 v[96:99], a[232:235], a[48:51], v[96:99]
ds_read_b64_tr_b16 a[224:225], v2 offset:35848
buffer_load_dwordx4 a[20:23], v0, s[44:47], s15 offen offset:1024 sc1
v_mfma_f32_16x16x32_bf16 v[116:119], a[232:235], a[52:55], v[116:119]
ds_read_b64_tr_b16 a[226:227], v2 offset:38408
v_mfma_f32_16x16x32_bf16 v[100:103], a[236:239], a[48:51], v[100:103]
ds_read_b64_tr_b16 a[228:229], v2 offset:35856
v_mfma_f32_16x16x32_bf16 v[120:123], a[236:239], a[52:55], v[120:123]
ds_read_b64_tr_b16 a[230:231], v2 offset:38416
s_waitcnt lgkmcnt(4)
v_mfma_f32_16x16x32_bf16 v[104:107], a[240:243], a[48:51], v[104:107]
ds_read_b64_tr_b16 a[232:233], v2 offset:35864
buffer_load_dwordx4 a[24:27], v0, s[44:47], s15 offen offset:2048 sc1
v_mfma_f32_16x16x32_bf16 v[124:127], a[240:243], a[52:55], v[124:127]
ds_read_b64_tr_b16 a[234:235], v2 offset:38424
v_mfma_f32_16x16x32_bf16 v[108:111], a[244:247], a[48:51], v[108:111]
ds_read_b64_tr_b16 a[236:237], v2 offset:35872
s_barrier
v_mfma_f32_16x16x32_bf16 v[128:131], a[244:247], a[52:55], v[128:131]
ds_read_b64_tr_b16 a[238:239], v2 offset:38432
v_mfma_f32_16x16x32_bf16 v[112:115], a[248:251], a[48:51], v[112:115]
ds_read_b64_tr_b16 a[240:241], v2 offset:40960
s_add_u32 s65, s64, 524288
v_mfma_f32_16x16x32_bf16 v[132:135], a[248:251], a[52:55], v[132:135]
ds_read_b64_tr_b16 a[242:243], v2 offset:43520
s_waitcnt vmcnt(7)
v_mfma_f32_16x16x32_bf16 v[96:99], a[252:255], a[56:59], v[96:99]
ds_read_b64_tr_b16 a[244:245], v2 offset:40968
buffer_store_dword v224, v225, s[44:47], s65 offen sc1
v_mfma_f32_16x16x32_bf16 v[116:119], a[252:255], a[60:63], v[116:119]
ds_read_b64_tr_b16 a[246:247], v2 offset:43528
s_waitcnt lgkmcnt(4)
v_mfma_f32_16x16x32_bf16 v[100:103], a[224:227], a[56:59], v[100:103]
ds_read_b64_tr_b16 a[248:249], v2 offset:40976
buffer_load_dwordx4 a[28:31], v0, s[44:47], s15 offen offset:3072 sc1
v_mfma_f32_16x16x32_bf16 v[120:123], a[224:227], a[60:63], v[120:123]
ds_read_b64_tr_b16 a[250:251], v2 offset:43536
s_add_u32 s68, s40, 23465984
v_mfma_f32_16x16x32_bf16 v[104:107], a[228:231], a[56:59], v[104:107]
ds_read_b64_tr_b16 a[252:253], v2 offset:40984
s_add_u32 m0, s61, 45056
v_mfma_f32_16x16x32_bf16 v[124:127], a[228:231], a[60:63], v[124:127]
ds_read_b64_tr_b16 a[254:255], v2 offset:43544
buffer_load_dwordx4 v0, s[44:47], s68 offen sc1 lds
v_mfma_f32_16x16x32_bf16 v[108:111], a[232:235], a[56:59], v[108:111]
ds_read_b64_tr_b16 a[224:225], v2 offset:40992
s_add_u32 s68, s40, 23482368
v_mfma_f32_16x16x32_bf16 v[128:131], a[232:235], a[60:63], v[128:131]
ds_read_b64_tr_b16 a[226:227], v2 offset:43552
s_add_u32 m0, s61, 61440
v_mfma_f32_16x16x32_bf16 v[112:115], a[236:239], a[56:59], v[112:115]
ds_read_b64_tr_b16 a[228:229], v2 offset:46080
buffer_load_dwordx4 v0, s[44:47], s68 offen sc1 lds
v_mfma_f32_16x16x32_bf16 v[132:135], a[236:239], a[60:63], v[132:135]
ds_read_b64_tr_b16 a[230:231], v2 offset:48640
s_waitcnt lgkmcnt(4)
v_mfma_f32_16x16x32_bf16 v[136:139], a[240:243], v[176:179], v[136:139]
ds_read_b64_tr_b16 a[232:233], v2 offset:46088
buffer_load_dwordx4 v0, s[44:47], s68 offen offset:1024 sc1 lds
v_mfma_f32_16x16x32_bf16 v[156:159], a[240:243], v[180:183], v[156:159]
ds_read_b64_tr_b16 a[234:235], v2 offset:48648
v_mfma_f32_16x16x32_bf16 v[140:143], a[244:247], v[176:179], v[140:143]
ds_read_b64_tr_b16 a[236:237], v2 offset:46096
v_mfma_f32_16x16x32_bf16 v[160:163], a[244:247], v[180:183], v[160:163]
ds_read_b64_tr_b16 a[238:239], v2 offset:48656
v_mfma_f32_16x16x32_bf16 v[144:147], a[248:251], v[176:179], v[144:147]
ds_read_b64_tr_b16 a[240:241], v2 offset:46104
buffer_load_dwordx4 v0, s[44:47], s68 offen offset:2048 sc1 lds
v_mfma_f32_16x16x32_bf16 v[164:167], a[248:251], v[180:183], v[164:167]
ds_read_b64_tr_b16 a[242:243], v2 offset:48664
v_mfma_f32_16x16x32_bf16 v[148:151], a[252:255], v[176:179], v[148:151]
ds_read_b64_tr_b16 a[244:245], v2 offset:46112
v_mfma_f32_16x16x32_bf16 v[168:171], a[252:255], v[180:183], v[168:171]
ds_read_b64_tr_b16 a[246:247], v2 offset:48672
s_waitcnt lgkmcnt(4)
v_mfma_f32_16x16x32_bf16 v[152:155], a[224:227], v[176:179], v[152:155]
ds_read_b64_tr_b16 a[248:249], v2 offset:51200
buffer_load_dwordx4 v0, s[44:47], s68 offen offset:3072 sc1 lds
v_mfma_f32_16x16x32_bf16 v[172:175], a[224:227], v[180:183], v[172:175]
ds_read_b64_tr_b16 a[250:251], v2 offset:53760
v_mfma_f32_16x16x32_bf16 v[136:139], a[228:231], v[184:187], v[136:139]
ds_read_b64_tr_b16 a[252:253], v2 offset:51208
v_mfma_f32_16x16x32_bf16 v[156:159], a[228:231], v[188:191], v[156:159]
ds_read_b64_tr_b16 a[254:255], v2 offset:53768
v_mfma_f32_16x16x32_bf16 v[140:143], a[232:235], v[184:187], v[140:143]
ds_read_b64_tr_b16 a[224:225], v2 offset:51216
s_add_u32 s68, s40, 23486464
v_mfma_f32_16x16x32_bf16 v[160:163], a[232:235], v[188:191], v[160:163]
ds_read_b64_tr_b16 a[226:227], v2 offset:53776
s_add_u32 m0, s61, 65536
v_mfma_f32_16x16x32_bf16 v[144:147], a[236:239], v[184:187], v[144:147]
ds_read_b64_tr_b16 a[228:229], v2 offset:51224
buffer_load_dwordx4 v0, s[44:47], s68 offen sc1 lds
v_mfma_f32_16x16x32_bf16 v[164:167], a[236:239], v[188:191], v[164:167]
ds_read_b64_tr_b16 a[230:231], v2 offset:53784
s_waitcnt lgkmcnt(4)
v_mfma_f32_16x16x32_bf16 v[148:151], a[240:243], v[184:187], v[148:151]
ds_read_b64_tr_b16 a[232:233], v2 offset:51232
v_mfma_f32_16x16x32_bf16 v[168:171], a[240:243], v[188:191], v[168:171]
ds_read_b64_tr_b16 a[234:235], v2 offset:53792
)ASM"
R"ASM(
v_mfma_f32_16x16x32_bf16 v[152:155], a[244:247], v[184:187], v[152:155]
ds_read_b64_tr_b16 a[236:237], v2 offset:56320
v_mfma_f32_16x16x32_bf16 v[172:175], a[244:247], v[188:191], v[172:175]
ds_read_b64_tr_b16 a[238:239], v2 offset:58880
v_mfma_f32_16x16x32_bf16 v[136:139], a[248:251], a[80:83], v[136:139]
ds_read_b64_tr_b16 a[240:241], v2 offset:56328
v_mfma_f32_16x16x32_bf16 v[156:159], a[248:251], a[84:87], v[156:159]
ds_read_b64_tr_b16 a[242:243], v2 offset:58888
v_mfma_f32_16x16x32_bf16 v[140:143], a[252:255], a[80:83], v[140:143]
ds_read_b64_tr_b16 a[244:245], v2 offset:56336
v_mfma_f32_16x16x32_bf16 v[160:163], a[252:255], a[84:87], v[160:163]
ds_read_b64_tr_b16 a[246:247], v2 offset:58896
s_waitcnt lgkmcnt(4)
v_mfma_f32_16x16x32_bf16 v[144:147], a[224:227], a[80:83], v[144:147]
ds_read_b64_tr_b16 a[248:249], v2 offset:56344
v_mfma_f32_16x16x32_bf16 v[164:167], a[224:227], a[84:87], v[164:167]
ds_read_b64_tr_b16 a[250:251], v2 offset:58904
v_mfma_f32_16x16x32_bf16 v[148:151], a[228:231], a[80:83], v[148:151]
ds_read_b64_tr_b16 a[252:253], v2 offset:56352
s_barrier
v_mfma_f32_16x16x32_bf16 v[168:171], a[228:231], a[84:87], v[168:171]
ds_read_b64_tr_b16 a[254:255], v2 offset:58912
s_add_u32 s68, s40, 23502848
v_mfma_f32_16x16x32_bf16 v[152:155], a[232:235], a[80:83], v[152:155]
ds_read_b64_tr_b16 a[224:225], v3
s_add_u32 m0, s61, 81920
v_mfma_f32_16x16x32_bf16 v[172:175], a[232:235], a[84:87], v[172:175]
ds_read_b64_tr_b16 a[226:227], v3 offset:2560
buffer_load_dwordx4 v0, s[44:47], s68 offen sc1 lds
v_mfma_f32_16x16x32_bf16 v[136:139], a[236:239], a[88:91], v[136:139]
ds_read_b64_tr_b16 a[228:229], v3 offset:8
buffer_load_dwordx4 v0, s[44:47], s68 offen offset:1024 sc1 lds
v_mfma_f32_16x16x32_bf16 v[156:159], a[236:239], a[92:95], v[156:159]
ds_read_b64_tr_b16 a[230:231], v3 offset:2568
s_waitcnt lgkmcnt(4)
v_mfma_f32_16x16x32_bf16 v[140:143], a[240:243], a[88:91], v[140:143]
ds_read_b64_tr_b16 a[232:233], v3 offset:16
buffer_load_dwordx4 v0, s[44:47], s68 offen offset:2048 sc1 lds
v_mfma_f32_16x16x32_bf16 v[160:163], a[240:243], a[92:95], v[160:163]
ds_read_b64_tr_b16 a[234:235], v3 offset:2576
v_mfma_f32_16x16x32_bf16 v[144:147], a[244:247], a[88:91], v[144:147]
ds_read_b64_tr_b16 a[236:237], v3 offset:24
v_mfma_f32_16x16x32_bf16 v[164:167], a[244:247], a[92:95], v[164:167]
ds_read_b64_tr_b16 a[238:239], v3 offset:2584
buffer_load_dwordx4 v0, s[44:47], s68 offen offset:3072 sc1 lds
v_mfma_f32_16x16x32_bf16 v[148:151], a[248:251], a[88:91], v[148:151]
ds_read_b64_tr_b16 a[240:241], v3 offset:32
v_mfma_f32_16x16x32_bf16 v[168:171], a[248:251], a[92:95], v[168:171]
ds_read_b64_tr_b16 a[242:243], v3 offset:2592
v_mfma_f32_16x16x32_bf16 v[152:155], a[252:255], a[88:91], v[152:155]
ds_read_b64_tr_b16 a[244:245], v3 offset:5120
v_mfma_f32_16x16x32_bf16 v[172:175], a[252:255], a[92:95], v[172:175]
ds_read_b64_tr_b16 a[246:247], v3 offset:7680
s_waitcnt vmcnt(17) lgkmcnt(4)
v_mfma_f32_16x16x32_bf16 v[136:139], a[224:227], a[0:3], v[136:139]
ds_read_b64_tr_b16 a[248:249], v3 offset:5128
s_add_u32 s68, s40, 23506944
v_mfma_f32_16x16x32_bf16 v[156:159], a[224:227], a[4:7], v[156:159]
ds_read_b64_tr_b16 a[250:251], v3 offset:7688
s_add_u32 m0, s61, 86016
v_mfma_f32_16x16x32_bf16 v[140:143], a[228:231], a[0:3], v[140:143]
ds_read_b64_tr_b16 a[252:253], v3 offset:5136
buffer_load_dwordx4 v0, s[44:47], s68 offen sc1 lds
v_mfma_f32_16x16x32_bf16 v[160:163], a[228:231], a[4:7], v[160:163]
ds_read_b64_tr_b16 a[254:255], v3 offset:7696
v_mfma_f32_16x16x32_bf16 v[144:147], a[232:235], a[0:3], v[144:147]
ds_read_b64_tr_b16 a[224:225], v3 offset:5144
v_mfma_f32_16x16x32_bf16 v[164:167], a[232:235], a[4:7], v[164:167]
ds_read_b64_tr_b16 a[226:227], v3 offset:7704
v_mfma_f32_16x16x32_bf16 v[148:151], a[236:239], a[0:3], v[148:151]
ds_read_b64_tr_b16 a[228:229], v3 offset:5152
v_mfma_f32_16x16x32_bf16 v[168:171], a[236:239], a[4:7], v[168:171]
ds_read_b64_tr_b16 a[230:231], v3 offset:7712
s_waitcnt lgkmcnt(4)
v_mfma_f32_16x16x32_bf16 v[152:155], a[240:243], a[0:3], v[152:155]
ds_read_b64_tr_b16 a[232:233], v3 offset:10240
v_mfma_f32_16x16x32_bf16 v[172:175], a[240:243], a[4:7], v[172:175]
ds_read_b64_tr_b16 a[234:235], v3 offset:12800
s_waitcnt vmcnt(16)
v_mfma_f32_16x16x32_bf16 v[136:139], a[244:247], a[8:11], v[136:139]
ds_read_b64_tr_b16 a[236:237], v3 offset:10248
v_mfma_f32_16x16x32_bf16 v[156:159], a[244:247], a[12:15], v[156:159]
ds_read_b64_tr_b16 a[238:239], v3 offset:12808
v_mfma_f32_16x16x32_bf16 v[140:143], a[248:251], a[8:11], v[140:143]
ds_read_b64_tr_b16 a[240:241], v3 offset:10256
v_mfma_f32_16x16x32_bf16 v[160:163], a[248:251], a[12:15], v[160:163]
ds_read_b64_tr_b16 a[242:243], v3 offset:12816
v_mfma_f32_16x16x32_bf16 v[144:147], a[252:255], a[8:11], v[144:147]
ds_read_b64_tr_b16 a[244:245], v3 offset:10264
v_mfma_f32_16x16x32_bf16 v[164:167], a[252:255], a[12:15], v[164:167]
ds_read_b64_tr_b16 a[246:247], v3 offset:12824
s_waitcnt lgkmcnt(4)
v_mfma_f32_16x16x32_bf16 v[148:151], a[224:227], a[8:11], v[148:151]
ds_read_b64_tr_b16 a[248:249], v3 offset:10272
v_mfma_f32_16x16x32_bf16 v[168:171], a[224:227], a[12:15], v[168:171]
ds_read_b64_tr_b16 a[250:251], v3 offset:12832
v_mfma_f32_16x16x32_bf16 v[152:155], a[228:231], a[8:11], v[152:155]
ds_read_b64_tr_b16 a[252:253], v3 offset:15360
v_mfma_f32_16x16x32_bf16 v[172:175], a[228:231], a[12:15], v[172:175]
ds_read_b64_tr_b16 a[254:255], v3 offset:17920
s_waitcnt vmcnt(14)
v_mfma_f32_16x16x32_bf16 v[136:139], a[232:235], a[16:19], v[136:139]
ds_read_b64_tr_b16 a[224:225], v3 offset:15368
v_mfma_f32_16x16x32_bf16 v[156:159], a[232:235], a[20:23], v[156:159]
ds_read_b64_tr_b16 a[226:227], v3 offset:17928
v_mfma_f32_16x16x32_bf16 v[140:143], a[236:239], a[16:19], v[140:143]
ds_read_b64_tr_b16 a[228:229], v3 offset:15376
v_mfma_f32_16x16x32_bf16 v[160:163], a[236:239], a[20:23], v[160:163]
ds_read_b64_tr_b16 a[230:231], v3 offset:17936
s_waitcnt lgkmcnt(4)
v_mfma_f32_16x16x32_bf16 v[144:147], a[240:243], a[16:19], v[144:147]
ds_read_b64_tr_b16 a[232:233], v3 offset:15384
v_mfma_f32_16x16x32_bf16 v[164:167], a[240:243], a[20:23], v[164:167]
ds_read_b64_tr_b16 a[234:235], v3 offset:17944
v_mfma_f32_16x16x32_bf16 v[148:151], a[244:247], a[16:19], v[148:151]
ds_read_b64_tr_b16 a[236:237], v3 offset:15392
s_barrier
v_mfma_f32_16x16x32_bf16 v[168:171], a[244:247], a[20:23], v[168:171]
ds_read_b64_tr_b16 a[238:239], v3 offset:17952
s_add_u32 s68, s40, 23523328
v_mfma_f32_16x16x32_bf16 v[152:155], a[248:251], a[16:19], v[152:155]
ds_read_b64_tr_b16 a[240:241], v1
s_add_u32 m0, s61, 102400
v_mfma_f32_16x16x32_bf16 v[172:175], a[248:251], a[20:23], v[172:175]
ds_read_b64_tr_b16 a[242:243], v1 offset:2560
s_waitcnt vmcnt(11)
v_mfma_f32_16x16x32_bf16 v[136:139], a[252:255], a[24:27], v[136:139]
ds_read_b64_tr_b16 a[244:245], v1 offset:8
buffer_load_dwordx4 v0, s[44:47], s68 offen sc1 lds
v_mfma_f32_16x16x32_bf16 v[156:159], a[252:255], a[28:31], v[156:159]
ds_read_b64_tr_b16 a[246:247], v1 offset:2568
s_waitcnt lgkmcnt(4)
v_mfma_f32_16x16x32_bf16 v[140:143], a[224:227], a[24:27], v[140:143]
ds_read_b64_tr_b16 a[248:249], v1 offset:16
buffer_load_dwordx4 v0, s[44:47], s68 offen offset:1024 sc1 lds
v_mfma_f32_16x16x32_bf16 v[160:163], a[224:227], a[28:31], v[160:163]
ds_read_b64_tr_b16 a[250:251], v1 offset:2576
buffer_load_dwordx4 v0, s[44:47], s68 offen offset:2048 sc1 lds
v_mfma_f32_16x16x32_bf16 v[144:147], a[228:231], a[24:27], v[144:147]
ds_read_b64_tr_b16 a[252:253], v1 offset:24
v_mfma_f32_16x16x32_bf16 v[164:167], a[228:231], a[28:31], v[164:167]
ds_read_b64_tr_b16 a[254:255], v1 offset:2584
v_mfma_f32_16x16x32_bf16 v[148:151], a[232:235], a[24:27], v[148:151]
ds_read_b64_tr_b16 a[224:225], v1 offset:32
buffer_load_dwordx4 v0, s[44:47], s68 offen offset:3072 sc1 lds
v_mfma_f32_16x16x32_bf16 v[168:171], a[232:235], a[28:31], v[168:171]
ds_read_b64_tr_b16 a[226:227], v1 offset:2592
v_mfma_f32_16x16x32_bf16 v[152:155], a[236:239], a[24:27], v[152:155]
ds_read_b64_tr_b16 a[228:229], v1 offset:5120
v_mfma_f32_16x16x32_bf16 v[172:175], a[236:239], a[28:31], v[172:175]
ds_read_b64_tr_b16 a[230:231], v1 offset:7680
s_waitcnt lgkmcnt(4)
v_mfma_f32_16x16x32_bf16 v[96:99], a[240:243], v[176:179], v[96:99]
ds_read_b64_tr_b16 a[232:233], v1 offset:5128
s_add_u32 s68, s40, 23527424
v_mfma_f32_16x16x32_bf16 v[116:119], a[240:243], v[180:183], v[116:119]
ds_read_b64_tr_b16 a[234:235], v1 offset:7688
s_add_u32 m0, s61, 106496
v_mfma_f32_16x16x32_bf16 v[100:103], a[244:247], v[176:179], v[100:103]
ds_read_b64_tr_b16 a[236:237], v1 offset:5136
buffer_load_dwordx4 v0, s[44:47], s68 offen sc1 lds
v_mfma_f32_16x16x32_bf16 v[120:123], a[244:247], v[180:183], v[120:123]
ds_read_b64_tr_b16 a[238:239], v1 offset:7696
v_mfma_f32_16x16x32_bf16 v[104:107], a[248:251], v[176:179], v[104:107]
ds_read_b64_tr_b16 a[240:241], v1 offset:5144
v_cvt_pk_bf16_f32 v46, v136, v137
v_mfma_f32_16x16x32_bf16 v[124:127], a[248:251], v[180:183], v[124:127]
ds_read_b64_tr_b16 a[242:243], v1 offset:7704
v_cvt_pk_bf16_f32 v47, v138, v139
v_mfma_f32_16x16x32_bf16 v[108:111], a[252:255], v[176:179], v[108:111]
ds_read_b64_tr_b16 a[244:245], v1 offset:5152
v_cvt_pk_bf16_f32 v48, v140, v141
v_mfma_f32_16x16x32_bf16 v[128:131], a[252:255], v[180:183], v[128:131]
ds_read_b64_tr_b16 a[246:247], v1 offset:7712
s_waitcnt lgkmcnt(4)
v_mfma_f32_16x16x32_bf16 v[112:115], a[224:227], v[176:179], v[112:115]
ds_read_b64_tr_b16 a[248:249], v1 offset:10240
v_cvt_pk_bf16_f32 v49, v142, v143
v_mfma_f32_16x16x32_bf16 v[132:135], a[224:227], v[180:183], v[132:135]
ds_read_b64_tr_b16 a[250:251], v1 offset:12800
buffer_store_dwordx4 v[46:49], v0, s[44:47], s53 offen sc1
v_mfma_f32_16x16x32_bf16 v[96:99], a[228:231], v[184:187], v[96:99]
ds_read_b64_tr_b16 a[252:253], v1 offset:10248
v_cvt_pk_bf16_f32 v50, v144, v145
v_mfma_f32_16x16x32_bf16 v[116:119], a[228:231], v[188:191], v[116:119]
ds_read_b64_tr_b16 a[254:255], v1 offset:12808
v_cvt_pk_bf16_f32 v51, v146, v147
v_mfma_f32_16x16x32_bf16 v[100:103], a[232:235], v[184:187], v[100:103]
ds_read_b64_tr_b16 a[224:225], v1 offset:10256
v_cvt_pk_bf16_f32 v52, v148, v149
v_mfma_f32_16x16x32_bf16 v[120:123], a[232:235], v[188:191], v[120:123]
ds_read_b64_tr_b16 a[226:227], v1 offset:12816
v_cvt_pk_bf16_f32 v53, v150, v151
v_mfma_f32_16x16x32_bf16 v[104:107], a[236:239], v[184:187], v[104:107]
ds_read_b64_tr_b16 a[228:229], v1 offset:10264
buffer_store_dwordx4 v[50:53], v0, s[44:47], s53 offen offset:1024 sc1
v_mfma_f32_16x16x32_bf16 v[124:127], a[236:239], v[188:191], v[124:127]
ds_read_b64_tr_b16 a[230:231], v1 offset:12824
s_waitcnt lgkmcnt(4)
v_mfma_f32_16x16x32_bf16 v[108:111], a[240:243], v[184:187], v[108:111]
ds_read_b64_tr_b16 a[232:233], v1 offset:10272
v_cvt_pk_bf16_f32 v54, v152, v153
v_mfma_f32_16x16x32_bf16 v[128:131], a[240:243], v[188:191], v[128:131]
ds_read_b64_tr_b16 a[234:235], v1 offset:12832
v_cvt_pk_bf16_f32 v55, v154, v155
v_mfma_f32_16x16x32_bf16 v[112:115], a[244:247], v[184:187], v[112:115]
ds_read_b64_tr_b16 a[236:237], v1 offset:15360
buffer_store_dwordx2 v[54:55], v12, s[44:47], s53 offen offset:2048 sc1
v_mfma_f32_16x16x32_bf16 v[132:135], a[244:247], v[188:191], v[132:135]
ds_read_b64_tr_b16 a[238:239], v1 offset:17920
v_cvt_pk_bf16_f32 v56, v156, v157
v_mfma_f32_16x16x32_bf16 v[96:99], a[248:251], a[80:83], v[96:99]
ds_read_b64_tr_b16 a[240:241], v1 offset:15368
v_cvt_pk_bf16_f32 v57, v158, v159
v_mfma_f32_16x16x32_bf16 v[116:119], a[248:251], a[84:87], v[116:119]
ds_read_b64_tr_b16 a[242:243], v1 offset:17928
v_cvt_pk_bf16_f32 v58, v160, v161
v_mfma_f32_16x16x32_bf16 v[100:103], a[252:255], a[80:83], v[100:103]
ds_read_b64_tr_b16 a[244:245], v1 offset:15376
v_cvt_pk_bf16_f32 v59, v162, v163
v_mfma_f32_16x16x32_bf16 v[120:123], a[252:255], a[84:87], v[120:123]
ds_read_b64_tr_b16 a[246:247], v1 offset:17936
s_waitcnt lgkmcnt(4)
v_mfma_f32_16x16x32_bf16 v[104:107], a[224:227], a[80:83], v[104:107]
ds_read_b64_tr_b16 a[248:249], v1 offset:15384
buffer_store_dwordx4 v[56:59], v0, s[44:47], s54 offen sc1
v_mfma_f32_16x16x32_bf16 v[124:127], a[224:227], a[84:87], v[124:127]
ds_read_b64_tr_b16 a[250:251], v1 offset:17944
v_cvt_pk_bf16_f32 v60, v164, v165
v_mfma_f32_16x16x32_bf16 v[108:111], a[228:231], a[80:83], v[108:111]
ds_read_b64_tr_b16 a[252:253], v1 offset:15392
s_barrier
v_mfma_f32_16x16x32_bf16 v[128:131], a[228:231], a[84:87], v[128:131]
ds_read_b64_tr_b16 a[254:255], v1 offset:17952
s_add_u32 s68, s40, 23543808
v_mfma_f32_16x16x32_bf16 v[112:115], a[232:235], a[80:83], v[112:115]
ds_read_b64_tr_b16 a[224:225], v1 offset:20480
v_accvgpr_read_b32 v230, a212
v_mfma_f32_16x16x32_bf16 v[132:135], a[232:235], a[84:87], v[132:135]
ds_read_b64_tr_b16 a[226:227], v1 offset:23040
s_mov_b32 s65, 19922944
v_mfma_f32_16x16x32_bf16 v[96:99], a[236:239], a[88:91], v[96:99]
ds_read_b64_tr_b16 a[228:229], v1 offset:20488
buffer_load_dword v231, v230, s[44:47], s65 offen sc1
v_mfma_f32_16x16x32_bf16 v[116:119], a[236:239], a[92:95], v[116:119]
ds_read_b64_tr_b16 a[230:231], v1 offset:23048
s_waitcnt lgkmcnt(4)
v_mfma_f32_16x16x32_bf16 v[100:103], a[240:243], a[88:91], v[100:103]
ds_read_b64_tr_b16 a[232:233], v1 offset:20496
s_add_u32 m0, s61, 122880
v_mfma_f32_16x16x32_bf16 v[120:123], a[240:243], a[92:95], v[120:123]
ds_read_b64_tr_b16 a[234:235], v1 offset:23056
buffer_load_dwordx4 v0, s[44:47], s68 offen sc1 lds
v_mfma_f32_16x16x32_bf16 v[104:107], a[244:247], a[88:91], v[104:107]
ds_read_b64_tr_b16 a[236:237], v1 offset:20504
buffer_load_dwordx4 v0, s[44:47], s68 offen offset:1024 sc1 lds
v_mfma_f32_16x16x32_bf16 v[124:127], a[244:247], a[92:95], v[124:127]
ds_read_b64_tr_b16 a[238:239], v1 offset:23064
buffer_load_dwordx4 v0, s[44:47], s68 offen offset:2048 sc1 lds
v_mfma_f32_16x16x32_bf16 v[108:111], a[248:251], a[88:91], v[108:111]
ds_read_b64_tr_b16 a[240:241], v1 offset:20512
v_cvt_pk_bf16_f32 v61, v166, v167
v_mfma_f32_16x16x32_bf16 v[128:131], a[248:251], a[92:95], v[128:131]
ds_read_b64_tr_b16 a[242:243], v1 offset:23072
v_cvt_pk_bf16_f32 v62, v168, v169
v_mfma_f32_16x16x32_bf16 v[112:115], a[252:255], a[88:91], v[112:115]
ds_read_b64_tr_b16 a[244:245], v1 offset:25600
buffer_load_dwordx4 v0, s[44:47], s68 offen offset:3072 sc1 lds
v_mfma_f32_16x16x32_bf16 v[132:135], a[252:255], a[92:95], v[132:135]
ds_read_b64_tr_b16 a[246:247], v1 offset:28160
s_waitcnt lgkmcnt(4)
v_mfma_f32_16x16x32_bf16 v[96:99], a[224:227], a[0:3], v[96:99]
ds_read_b64_tr_b16 a[248:249], v1 offset:25608
v_cvt_pk_bf16_f32 v63, v170, v171
v_mfma_f32_16x16x32_bf16 v[116:119], a[224:227], a[4:7], v[116:119]
ds_read_b64_tr_b16 a[250:251], v1 offset:28168
buffer_store_dwordx4 v[60:63], v0, s[44:47], s54 offen offset:1024 sc1
v_mfma_f32_16x16x32_bf16 v[100:103], a[228:231], a[0:3], v[100:103]
ds_read_b64_tr_b16 a[252:253], v1 offset:25616
s_add_u32 s68, s40, 23547904
v_mfma_f32_16x16x32_bf16 v[120:123], a[228:231], a[4:7], v[120:123]
ds_read_b64_tr_b16 a[254:255], v1 offset:28176
s_add_u32 m0, s61, 126976
v_mfma_f32_16x16x32_bf16 v[104:107], a[232:235], a[0:3], v[104:107]
ds_read_b64_tr_b16 a[224:225], v1 offset:25624
buffer_load_dwordx4 v0, s[44:47], s68 offen sc1 lds
v_mfma_f32_16x16x32_bf16 v[124:127], a[232:235], a[4:7], v[124:127]
ds_read_b64_tr_b16 a[226:227], v1 offset:28184
v_cvt_pk_bf16_f32 v64, v172, v173
v_mfma_f32_16x16x32_bf16 v[108:111], a[236:239], a[0:3], v[108:111]
ds_read_b64_tr_b16 a[228:229], v1 offset:25632
v_cvt_pk_bf16_f32 v65, v174, v175
v_mfma_f32_16x16x32_bf16 v[128:131], a[236:239], a[4:7], v[128:131]
ds_read_b64_tr_b16 a[230:231], v1 offset:28192
s_waitcnt lgkmcnt(4)
v_mfma_f32_16x16x32_bf16 v[112:115], a[240:243], a[0:3], v[112:115]
ds_read_b64_tr_b16 a[232:233], v1 offset:30720
s_waitcnt vmcnt(6)
v_mfma_f32_16x16x32_bf16 v[132:135], a[240:243], a[4:7], v[132:135]
ds_read_b64_tr_b16 a[234:235], v1 offset:33280
v_cmp_ne_u32 vcc, s62, v231
v_mfma_f32_16x16x32_bf16 v[96:99], a[244:247], a[8:11], v[96:99]
ds_read_b64_tr_b16 a[236:237], v1 offset:30728
s_cbranch_vccnz .Lpq_13s
.Lpq_13:
v_mfma_f32_16x16x32_bf16 v[116:119], a[244:247], a[12:15], v[116:119]
ds_read_b64_tr_b16 a[238:239], v1 offset:33288
s_add_u32 s17, s19, 5120
s_add_u32 s18, s20, 5120
v_mfma_f32_16x16x32_bf16 v[100:103], a[248:251], a[8:11], v[100:103]
ds_read_b64_tr_b16 a[240:241], v1 offset:30736
buffer_load_dwordx4 a[96:99], v0, s[44:47], s19 offen sc1
v_mfma_f32_16x16x32_bf16 v[120:123], a[248:251], a[12:15], v[120:123]
ds_read_b64_tr_b16 a[242:243], v1 offset:33296
buffer_load_dwordx4 a[100:103], v0, s[44:47], s20 offen sc1
v_mfma_f32_16x16x32_bf16 v[104:107], a[252:255], a[8:11], v[104:107]
ds_read_b64_tr_b16 a[244:245], v1 offset:30744
buffer_load_dwordx4 a[104:107], v0, s[44:47], s17 offen sc1
v_mfma_f32_16x16x32_bf16 v[124:127], a[252:255], a[12:15], v[124:127]
ds_read_b64_tr_b16 a[246:247], v1 offset:33304
s_waitcnt lgkmcnt(4)
v_mfma_f32_16x16x32_bf16 v[108:111], a[224:227], a[8:11], v[108:111]
ds_read_b64_tr_b16 a[248:249], v1 offset:30752
buffer_load_dwordx4 a[108:111], v0, s[44:47], s18 offen sc1
v_mfma_f32_16x16x32_bf16 v[128:131], a[224:227], a[12:15], v[128:131]
ds_read_b64_tr_b16 a[250:251], v1 offset:33312
buffer_store_dwordx2 v[64:65], v12, s[44:47], s54 offen offset:2048 sc1
v_mfma_f32_16x16x32_bf16 v[112:115], a[228:231], a[8:11], v[112:115]
ds_read_b64_tr_b16 a[252:253], v1 offset:35840
buffer_load_dwordx4 a[112:115], v0, s[44:47], s19 offen offset:1024 sc1
v_mfma_f32_16x16x32_bf16 v[132:135], a[228:231], a[12:15], v[132:135]
ds_read_b64_tr_b16 a[254:255], v1 offset:38400
buffer_load_dwordx4 a[116:119], v0, s[44:47], s20 offen offset:1024 sc1
v_mfma_f32_16x16x32_bf16 v[96:99], a[232:235], a[16:19], v[96:99]
ds_read_b64_tr_b16 a[224:225], v1 offset:35848
v_mfma_f32_16x16x32_bf16 v[116:119], a[232:235], a[20:23], v[116:119]
ds_read_b64_tr_b16 a[226:227], v1 offset:38408
v_mfma_f32_16x16x32_bf16 v[100:103], a[236:239], a[16:19], v[100:103]
ds_read_b64_tr_b16 a[228:229], v1 offset:35856
buffer_load_dwordx4 a[120:123], v0, s[44:47], s17 offen offset:1024 sc1
v_mfma_f32_16x16x32_bf16 v[120:123], a[236:239], a[20:23], v[120:123]
ds_read_b64_tr_b16 a[230:231], v1 offset:38416
s_waitcnt lgkmcnt(4)
v_mfma_f32_16x16x32_bf16 v[104:107], a[240:243], a[16:19], v[104:107]
ds_read_b64_tr_b16 a[232:233], v1 offset:35864
buffer_load_dwordx4 a[124:127], v0, s[44:47], s18 offen offset:1024 sc1
v_mfma_f32_16x16x32_bf16 v[124:127], a[240:243], a[20:23], v[124:127]
ds_read_b64_tr_b16 a[234:235], v1 offset:38424
v_mfma_f32_16x16x32_bf16 v[108:111], a[244:247], a[16:19], v[108:111]
ds_read_b64_tr_b16 a[236:237], v1 offset:35872
buffer_load_dwordx4 a[128:131], v0, s[44:47], s19 offen offset:2560 sc1
v_mfma_f32_16x16x32_bf16 v[128:131], a[244:247], a[20:23], v[128:131]
ds_read_b64_tr_b16 a[238:239], v1 offset:38432
v_mfma_f32_16x16x32_bf16 v[112:115], a[248:251], a[16:19], v[112:115]
v_mfma_f32_16x16x32_bf16 v[132:135], a[248:251], a[20:23], v[132:135]
buffer_load_dwordx4 a[132:135], v0, s[44:47], s20 offen offset:2560 sc1
v_mfma_f32_16x16x32_bf16 v[96:99], a[252:255], a[24:27], v[96:99]
v_mfma_f32_16x16x32_bf16 v[116:119], a[252:255], a[28:31], v[116:119]
s_waitcnt lgkmcnt(0)
buffer_load_dwordx4 a[136:139], v0, s[44:47], s17 offen offset:2560 sc1
v_mfma_f32_16x16x32_bf16 v[100:103], a[224:227], a[24:27], v[100:103]
v_mfma_f32_16x16x32_bf16 v[120:123], a[224:227], a[28:31], v[120:123]
buffer_load_dwordx4 a[140:143], v0, s[44:47], s18 offen offset:2560 sc1
v_mfma_f32_16x16x32_bf16 v[104:107], a[228:231], a[24:27], v[104:107]
v_mfma_f32_16x16x32_bf16 v[124:127], a[228:231], a[28:31], v[124:127]
v_mfma_f32_16x16x32_bf16 v[108:111], a[232:235], a[24:27], v[108:111]
buffer_load_dwordx4 a[144:147], v0, s[44:47], s19 offen offset:3584 sc1
v_mfma_f32_16x16x32_bf16 v[128:131], a[232:235], a[28:31], v[128:131]
v_mfma_f32_16x16x32_bf16 v[112:115], a[236:239], a[24:27], v[112:115]
buffer_load_dwordx4 a[148:151], v0, s[44:47], s20 offen offset:3584 sc1
v_mfma_f32_16x16x32_bf16 v[132:135], a[236:239], a[28:31], v[132:135]
buffer_load_dwordx4 a[152:155], v0, s[44:47], s17 offen offset:3584 sc1
buffer_load_dwordx4 a[156:159], v0, s[44:47], s18 offen offset:3584 sc1
buffer_load_dwordx2 v[192:193], v12, s[44:47], s19 offen offset:2048 sc1
s_add_u32 s67, s19, 4096
buffer_load_dwordx2 v[194:195], v12, s[44:47], s67 offen offset:512 sc1
buffer_load_dwordx2 v[196:197], v12, s[44:47], s20 offen offset:2048 sc1
s_add_u32 s67, s20, 4096
buffer_load_dwordx2 v[198:199], v12, s[44:47], s67 offen offset:512 sc1
buffer_load_dwordx2 v[200:201], v12, s[44:47], s17 offen offset:2048 sc1
s_add_u32 s67, s17, 4096
buffer_load_dwordx2 v[202:203], v12, s[44:47], s67 offen offset:512 sc1
buffer_load_dwordx2 v[204:205], v12, s[44:47], s18 offen offset:2048 sc1
s_add_u32 s67, s18, 4096
buffer_load_dwordx2 v[206:207], v12, s[44:47], s67 offen offset:512 sc1
v_mfma_f32_16x16x16_bf16 v[96:99], a[222:223], a[160:161], v[96:99]
v_cvt_pk_bf16_f32 v36, v116, v117
v_cvt_pk_bf16_f32 v37, v118, v119
v_mfma_f32_16x16x16_bf16 v[100:103], a[222:223], a[162:163], v[100:103]
v_cvt_pk_bf16_f32 v38, v120, v121
v_cvt_pk_bf16_f32 v39, v122, v123
v_mfma_f32_16x16x16_bf16 v[104:107], a[222:223], a[164:165], v[104:107]
buffer_store_dwordx4 v[36:39], v0, s[44:47], s52 offen sc1
v_cvt_pk_bf16_f32 v40, v124, v125
v_mfma_f32_16x16x16_bf16 v[108:111], a[222:223], a[166:167], v[108:111]
v_cvt_pk_bf16_f32 v41, v126, v127
v_cvt_pk_bf16_f32 v42, v128, v129
v_mfma_f32_16x16x16_bf16 v[112:115], a[222:223], a[168:169], v[112:115]
v_cvt_pk_bf16_f32 v43, v130, v131
buffer_store_dwordx4 v[40:43], v0, s[44:47], s52 offen offset:1024 sc1
)ASM"
R"ASM(
v_mfma_f32_16x16x16_bf16 v[96:99], a[222:223], a[170:171], v[96:99]
v_cvt_pk_bf16_f32 v44, v132, v133
v_cvt_pk_bf16_f32 v45, v134, v135
v_mfma_f32_16x16x16_bf16 v[100:103], a[222:223], a[172:173], v[100:103]
buffer_store_dwordx2 v[44:45], v12, s[44:47], s52 offen offset:2048 sc1
v_mfma_f32_16x16x16_bf16 v[104:107], a[222:223], a[174:175], v[104:107]
v_mfma_f32_16x16x16_bf16 v[108:111], a[222:223], a[176:177], v[108:111]
v_mfma_f32_16x16x16_bf16 v[112:115], a[222:223], a[178:179], v[112:115]
v_add_u32 v1, s70, v1
v_add_u32 v2, s70, v2
v_add_u32 v3, s70, v3
s_barrier
ds_read_b64_tr_b16 a[224:225], v1 offset:40960
ds_read_b64_tr_b16 a[226:227], v1 offset:43520
ds_read_b64_tr_b16 a[228:229], v1 offset:40968
ds_read_b64_tr_b16 a[230:231], v1 offset:43528
ds_read_b64_tr_b16 a[232:233], v1 offset:40976
ds_read_b64_tr_b16 a[234:235], v1 offset:43536
ds_read_b64_tr_b16 a[236:237], v1 offset:40984
ds_read_b64_tr_b16 a[238:239], v1 offset:43544
ds_read_b64_tr_b16 a[240:241], v1 offset:40992
ds_read_b64_tr_b16 a[242:243], v1 offset:43552
ds_read_b64_tr_b16 a[244:245], v1 offset:46080
ds_read_b64_tr_b16 a[246:247], v1 offset:48640
s_waitcnt vmcnt(26) lgkmcnt(8)
v_mfma_f32_16x16x32_bf16 v[96:99], a[224:227], a[96:99], v[96:99]
ds_read_b64_tr_b16 a[248:249], v1 offset:46088
v_mfma_f32_16x16x32_bf16 v[116:119], a[224:227], a[100:103], 0
ds_read_b64_tr_b16 a[250:251], v1 offset:48648
v_mfma_f32_16x16x32_bf16 v[100:103], a[228:231], a[96:99], v[100:103]
ds_read_b64_tr_b16 a[252:253], v1 offset:46096
v_mfma_f32_16x16x32_bf16 v[120:123], a[228:231], a[100:103], 0
ds_read_b64_tr_b16 a[254:255], v1 offset:48656
s_waitcnt lgkmcnt(8)
v_mfma_f32_16x16x32_bf16 v[104:107], a[232:235], a[96:99], v[104:107]
ds_read_b64_tr_b16 a[224:225], v1 offset:46104
v_mfma_f32_16x16x32_bf16 v[124:127], a[232:235], a[100:103], 0
ds_read_b64_tr_b16 a[226:227], v1 offset:48664
v_mfma_f32_16x16x32_bf16 v[108:111], a[236:239], a[96:99], v[108:111]
ds_read_b64_tr_b16 a[228:229], v1 offset:46112
s_barrier
v_mfma_f32_16x16x32_bf16 v[128:131], a[236:239], a[100:103], 0
ds_read_b64_tr_b16 a[230:231], v1 offset:48672
s_waitcnt lgkmcnt(4)
v_mfma_f32_16x16x32_bf16 v[112:115], a[240:243], a[96:99], v[112:115]
ds_read_b64_tr_b16 a[232:233], v2
v_mfma_f32_16x16x32_bf16 v[132:135], a[240:243], a[100:103], 0
ds_read_b64_tr_b16 a[234:235], v2 offset:2560
s_waitcnt vmcnt(24)
v_mfma_f32_16x16x32_bf16 v[96:99], a[244:247], a[104:107], v[96:99]
ds_read_b64_tr_b16 a[236:237], v2 offset:8
v_mfma_f32_16x16x32_bf16 v[116:119], a[244:247], a[108:111], v[116:119]
ds_read_b64_tr_b16 a[238:239], v2 offset:2568
v_mfma_f32_16x16x32_bf16 v[100:103], a[248:251], a[104:107], v[100:103]
ds_read_b64_tr_b16 a[240:241], v2 offset:16
v_mfma_f32_16x16x32_bf16 v[120:123], a[248:251], a[108:111], v[120:123]
ds_read_b64_tr_b16 a[242:243], v2 offset:2576
v_mfma_f32_16x16x32_bf16 v[104:107], a[252:255], a[104:107], v[104:107]
ds_read_b64_tr_b16 a[244:245], v2 offset:24
v_mfma_f32_16x16x32_bf16 v[124:127], a[252:255], a[108:111], v[124:127]
ds_read_b64_tr_b16 a[246:247], v2 offset:2584
s_waitcnt lgkmcnt(4)
v_mfma_f32_16x16x32_bf16 v[108:111], a[224:227], a[104:107], v[108:111]
ds_read_b64_tr_b16 a[248:249], v2 offset:32
v_mfma_f32_16x16x32_bf16 v[128:131], a[224:227], a[108:111], v[128:131]
ds_read_b64_tr_b16 a[250:251], v2 offset:2592
v_mfma_f32_16x16x32_bf16 v[112:115], a[228:231], a[104:107], v[112:115]
ds_read_b64_tr_b16 a[252:253], v2 offset:5120
v_mfma_f32_16x16x32_bf16 v[132:135], a[228:231], a[108:111], v[132:135]
ds_read_b64_tr_b16 a[254:255], v2 offset:7680
s_waitcnt vmcnt(21)
v_mfma_f32_16x16x32_bf16 v[96:99], a[232:235], a[112:115], v[96:99]
ds_read_b64_tr_b16 a[224:225], v2 offset:5128
v_mfma_f32_16x16x32_bf16 v[116:119], a[232:235], a[116:119], v[116:119]
ds_read_b64_tr_b16 a[226:227], v2 offset:7688
v_mfma_f32_16x16x32_bf16 v[100:103], a[236:239], a[112:115], v[100:103]
ds_read_b64_tr_b16 a[228:229], v2 offset:5136
v_mfma_f32_16x16x32_bf16 v[120:123], a[236:239], a[116:119], v[120:123]
ds_read_b64_tr_b16 a[230:231], v2 offset:7696
s_waitcnt lgkmcnt(4)
v_mfma_f32_16x16x32_bf16 v[104:107], a[240:243], a[112:115], v[104:107]
ds_read_b64_tr_b16 a[232:233], v2 offset:5144
v_mfma_f32_16x16x32_bf16 v[124:127], a[240:243], a[116:119], v[124:127]
ds_read_b64_tr_b16 a[234:235], v2 offset:7704
v_mfma_f32_16x16x32_bf16 v[108:111], a[244:247], a[112:115], v[108:111]
ds_read_b64_tr_b16 a[236:237], v2 offset:5152
s_barrier
v_mfma_f32_16x16x32_bf16 v[128:131], a[244:247], a[116:119], v[128:131]
ds_read_b64_tr_b16 a[238:239], v2 offset:7712
v_mfma_f32_16x16x32_bf16 v[112:115], a[248:251], a[112:115], v[112:115]
ds_read_b64_tr_b16 a[240:241], v2 offset:20480
s_add_u32 s65, s64, 786432
v_mfma_f32_16x16x32_bf16 v[132:135], a[248:251], a[116:119], v[132:135]
ds_read_b64_tr_b16 a[242:243], v2 offset:23040
s_waitcnt vmcnt(19)
v_mfma_f32_16x16x32_bf16 v[96:99], a[252:255], a[120:123], v[96:99]
ds_read_b64_tr_b16 a[244:245], v2 offset:20488
s_waitcnt vmcnt(0)
v_mfma_f32_16x16x32_bf16 v[116:119], a[252:255], a[124:127], v[116:119]
ds_read_b64_tr_b16 a[246:247], v2 offset:23048
s_waitcnt lgkmcnt(4)
v_mfma_f32_16x16x32_bf16 v[100:103], a[224:227], a[120:123], v[100:103]
ds_read_b64_tr_b16 a[248:249], v2 offset:20496
buffer_store_dword v224, v225, s[44:47], s65 offen sc1
v_mfma_f32_16x16x32_bf16 v[120:123], a[224:227], a[124:127], v[120:123]
ds_read_b64_tr_b16 a[250:251], v2 offset:23056
v_mfma_f32_16x16x32_bf16 v[104:107], a[228:231], a[120:123], v[104:107]
ds_read_b64_tr_b16 a[252:253], v2 offset:20504
v_mfma_f32_16x16x32_bf16 v[124:127], a[228:231], a[124:127], v[124:127]
ds_read_b64_tr_b16 a[254:255], v2 offset:23064
v_mfma_f32_16x16x32_bf16 v[108:111], a[232:235], a[120:123], v[108:111]
ds_read_b64_tr_b16 a[224:225], v2 offset:20512
v_mfma_f32_16x16x32_bf16 v[128:131], a[232:235], a[124:127], v[128:131]
ds_read_b64_tr_b16 a[226:227], v2 offset:23072
v_mfma_f32_16x16x32_bf16 v[112:115], a[236:239], a[120:123], v[112:115]
ds_read_b64_tr_b16 a[228:229], v2 offset:25600
v_mfma_f32_16x16x32_bf16 v[132:135], a[236:239], a[124:127], v[132:135]
ds_read_b64_tr_b16 a[230:231], v2 offset:28160
s_waitcnt lgkmcnt(4)
v_mfma_f32_16x16x32_bf16 v[96:99], a[240:243], a[128:131], v[96:99]
ds_read_b64_tr_b16 a[232:233], v2 offset:25608
v_mfma_f32_16x16x32_bf16 v[116:119], a[240:243], a[132:135], v[116:119]
ds_read_b64_tr_b16 a[234:235], v2 offset:28168
v_mfma_f32_16x16x32_bf16 v[100:103], a[244:247], a[128:131], v[100:103]
ds_read_b64_tr_b16 a[236:237], v2 offset:25616
v_mfma_f32_16x16x32_bf16 v[120:123], a[244:247], a[132:135], v[120:123]
ds_read_b64_tr_b16 a[238:239], v2 offset:28176
v_mfma_f32_16x16x32_bf16 v[104:107], a[248:251], a[128:131], v[104:107]
ds_read_b64_tr_b16 a[240:241], v2 offset:25624
v_mfma_f32_16x16x32_bf16 v[124:127], a[248:251], a[132:135], v[124:127]
ds_read_b64_tr_b16 a[242:243], v2 offset:28184
v_mfma_f32_16x16x32_bf16 v[108:111], a[252:255], a[128:131], v[108:111]
ds_read_b64_tr_b16 a[244:245], v2 offset:25632
s_barrier
v_mfma_f32_16x16x32_bf16 v[128:131], a[252:255], a[132:135], v[128:131]
ds_read_b64_tr_b16 a[246:247], v2 offset:28192
s_waitcnt lgkmcnt(4)
v_mfma_f32_16x16x32_bf16 v[112:115], a[224:227], a[128:131], v[112:115]
ds_read_b64_tr_b16 a[248:249], v2 offset:40960
v_mfma_f32_16x16x32_bf16 v[132:135], a[224:227], a[132:135], v[132:135]
ds_read_b64_tr_b16 a[250:251], v2 offset:43520
v_mfma_f32_16x16x32_bf16 v[96:99], a[228:231], a[136:139], v[96:99]
ds_read_b64_tr_b16 a[252:253], v2 offset:40968
v_mfma_f32_16x16x32_bf16 v[116:119], a[228:231], a[140:143], v[116:119]
ds_read_b64_tr_b16 a[254:255], v2 offset:43528
v_mfma_f32_16x16x32_bf16 v[100:103], a[232:235], a[136:139], v[100:103]
ds_read_b64_tr_b16 a[224:225], v2 offset:40976
v_mfma_f32_16x16x32_bf16 v[120:123], a[232:235], a[140:143], v[120:123]
ds_read_b64_tr_b16 a[226:227], v2 offset:43536
v_mfma_f32_16x16x32_bf16 v[104:107], a[236:239], a[136:139], v[104:107]
ds_read_b64_tr_b16 a[228:229], v2 offset:40984
v_mfma_f32_16x16x32_bf16 v[124:127], a[236:239], a[140:143], v[124:127]
ds_read_b64_tr_b16 a[230:231], v2 offset:43544
s_waitcnt lgkmcnt(4)
v_mfma_f32_16x16x32_bf16 v[108:111], a[240:243], a[136:139], v[108:111]
ds_read_b64_tr_b16 a[232:233], v2 offset:40992
v_mfma_f32_16x16x32_bf16 v[128:131], a[240:243], a[140:143], v[128:131]
ds_read_b64_tr_b16 a[234:235], v2 offset:43552
v_mfma_f32_16x16x32_bf16 v[112:115], a[244:247], a[136:139], v[112:115]
ds_read_b64_tr_b16 a[236:237], v2 offset:46080
v_mfma_f32_16x16x32_bf16 v[132:135], a[244:247], a[140:143], v[132:135]
ds_read_b64_tr_b16 a[238:239], v2 offset:48640
v_mfma_f32_16x16x32_bf16 v[96:99], a[248:251], a[144:147], v[96:99]
ds_read_b64_tr_b16 a[240:241], v2 offset:46088
v_mfma_f32_16x16x32_bf16 v[116:119], a[248:251], a[148:151], v[116:119]
ds_read_b64_tr_b16 a[242:243], v2 offset:48648
v_mfma_f32_16x16x32_bf16 v[100:103], a[252:255], a[144:147], v[100:103]
ds_read_b64_tr_b16 a[244:245], v2 offset:46096
v_mfma_f32_16x16x32_bf16 v[120:123], a[252:255], a[148:151], v[120:123]
ds_read_b64_tr_b16 a[246:247], v2 offset:48656
s_waitcnt lgkmcnt(4)
v_mfma_f32_16x16x32_bf16 v[104:107], a[224:227], a[144:147], v[104:107]
ds_read_b64_tr_b16 a[248:249], v2 offset:46104
v_mfma_f32_16x16x32_bf16 v[124:127], a[224:227], a[148:151], v[124:127]
ds_read_b64_tr_b16 a[250:251], v2 offset:48664
v_mfma_f32_16x16x32_bf16 v[108:111], a[228:231], a[144:147], v[108:111]
ds_read_b64_tr_b16 a[252:253], v2 offset:46112
s_barrier
v_mfma_f32_16x16x32_bf16 v[128:131], a[228:231], a[148:151], v[128:131]
ds_read_b64_tr_b16 a[254:255], v2 offset:48672
v_mfma_f32_16x16x32_bf16 v[112:115], a[232:235], a[144:147], v[112:115]
ds_read_b64_tr_b16 a[224:225], v3
v_mfma_f32_16x16x32_bf16 v[132:135], a[232:235], a[148:151], v[132:135]
ds_read_b64_tr_b16 a[226:227], v3 offset:2560
v_mfma_f32_16x16x32_bf16 v[96:99], a[236:239], a[152:155], v[96:99]
ds_read_b64_tr_b16 a[228:229], v3 offset:8
v_mfma_f32_16x16x32_bf16 v[116:119], a[236:239], a[156:159], v[116:119]
ds_read_b64_tr_b16 a[230:231], v3 offset:2568
s_waitcnt lgkmcnt(4)
v_mfma_f32_16x16x32_bf16 v[100:103], a[240:243], a[152:155], v[100:103]
ds_read_b64_tr_b16 a[232:233], v3 offset:16
v_mfma_f32_16x16x32_bf16 v[120:123], a[240:243], a[156:159], v[120:123]
ds_read_b64_tr_b16 a[234:235], v3 offset:2576
v_mfma_f32_16x16x32_bf16 v[104:107], a[244:247], a[152:155], v[104:107]
ds_read_b64_tr_b16 a[236:237], v3 offset:24
v_mfma_f32_16x16x32_bf16 v[124:127], a[244:247], a[156:159], v[124:127]
ds_read_b64_tr_b16 a[238:239], v3 offset:2584
v_mfma_f32_16x16x32_bf16 v[108:111], a[248:251], a[152:155], v[108:111]
ds_read_b64_tr_b16 a[240:241], v3 offset:32
v_mfma_f32_16x16x32_bf16 v[128:131], a[248:251], a[156:159], v[128:131]
ds_read_b64_tr_b16 a[242:243], v3 offset:2592
v_mfma_f32_16x16x32_bf16 v[112:115], a[252:255], a[152:155], v[112:115]
ds_read_b64_tr_b16 a[244:245], v3 offset:5120
v_mfma_f32_16x16x32_bf16 v[132:135], a[252:255], a[156:159], v[132:135]
ds_read_b64_tr_b16 a[246:247], v3 offset:7680
s_waitcnt lgkmcnt(4)
v_mfma_f32_16x16x32_bf16 v[96:99], a[224:227], v[192:195], v[96:99]
ds_read_b64_tr_b16 a[248:249], v3 offset:5128
v_add_u32 v16, s61, v0
v_mfma_f32_16x16x32_bf16 v[116:119], a[224:227], v[196:199], v[116:119]
ds_read_b64_tr_b16 a[250:251], v3 offset:7688
v_add_u32 v17, s72, v0
v_mfma_f32_16x16x32_bf16 v[100:103], a[228:231], v[192:195], v[100:103]
ds_read_b64_tr_b16 a[252:253], v3 offset:5136
v_accvgpr_read_b32 v7, a180
v_mfma_f32_16x16x32_bf16 v[120:123], a[228:231], v[196:199], v[120:123]
ds_read_b64_tr_b16 a[254:255], v3 offset:7696
v_accvgpr_read_b32 v8, a182
v_mfma_f32_16x16x32_bf16 v[104:107], a[232:235], v[192:195], v[104:107]
ds_read_b64_tr_b16 a[224:225], v3 offset:5144
v_accvgpr_read_b32 v228, a211
v_mfma_f32_16x16x32_bf16 v[124:127], a[232:235], v[196:199], v[124:127]
ds_read_b64_tr_b16 a[226:227], v3 offset:7704
s_mov_b32 s65, 20185088
v_mfma_f32_16x16x32_bf16 v[108:111], a[236:239], v[192:195], v[108:111]
ds_read_b64_tr_b16 a[228:229], v3 offset:5152
buffer_load_dword v229, v228, s[44:47], s65 offen sc1
v_mfma_f32_16x16x32_bf16 v[128:131], a[236:239], v[196:199], v[128:131]
ds_read_b64_tr_b16 a[230:231], v3 offset:7712
s_waitcnt lgkmcnt(4)
v_mfma_f32_16x16x32_bf16 v[112:115], a[240:243], v[192:195], v[112:115]
v_accvgpr_read_b32 v9, a184
v_add_u32 v18, 143360, v16
v_mfma_f32_16x16x32_bf16 v[132:135], a[240:243], v[196:199], v[132:135]
v_accvgpr_read_b32 v19, a206
v_add_u32 v19, 143360, v19
v_mfma_f32_16x16x32_bf16 v[96:99], a[244:247], v[200:203], v[96:99]
v_mfma_f32_16x16x32_bf16 v[116:119], a[244:247], v[204:207], v[116:119]
v_mfma_f32_16x16x32_bf16 v[100:103], a[248:251], v[200:203], v[100:103]
v_mfma_f32_16x16x32_bf16 v[120:123], a[248:251], v[204:207], v[120:123]
v_mfma_f32_16x16x32_bf16 v[104:107], a[252:255], v[200:203], v[104:107]
v_mfma_f32_16x16x32_bf16 v[124:127], a[252:255], v[204:207], v[124:127]
s_waitcnt lgkmcnt(0)
v_mfma_f32_16x16x32_bf16 v[108:111], a[224:227], v[200:203], v[108:111]
v_mfma_f32_16x16x32_bf16 v[128:131], a[224:227], v[204:207], v[128:131]
v_mfma_f32_16x16x32_bf16 v[112:115], a[228:231], v[200:203], v[112:115]
v_mfma_f32_16x16x32_bf16 v[132:135], a[228:231], v[204:207], v[132:135]
v_add_u32 v16, 0, v16
v_add_u32 v17, 0, v17
ds_write_b128 v16, v[116:119]
ds_write_b128 v16, v[120:123] offset:1024
ds_write_b128 v16, v[124:127] offset:2048
s_nop 0
ds_write_b128 v16, v[128:131] offset:3072
s_nop 0
ds_write_b128 v16, v[132:135] offset:4096
s_waitcnt vmcnt(0)
v_cmp_ne_u32 vcc, s62, v229
s_cbranch_vccnz .Lpq_14s
.Lpq_14:
buffer_load_dwordx4 v[36:39], v0, s[44:47], s21 offen sc1
buffer_load_dwordx4 v[40:43], v0, s[44:47], s21 offen offset:1024 sc1
buffer_load_dwordx2 v[44:45], v12, s[44:47], s21 offen offset:2048 sc1
buffer_load_dwordx4 v[46:49], v0, s[44:47], s21 offen offset:2560 sc1
buffer_load_dwordx4 v[50:53], v0, s[44:47], s21 offen offset:3584 sc1
s_add_u32 s67, s21, 4096
buffer_load_dwordx2 v[54:55], v12, s[44:47], s67 offen offset:512 sc1
buffer_load_dwordx4 v[56:59], v0, s[44:47], s67 offen offset:1024 sc1
buffer_load_dwordx4 v[60:63], v0, s[44:47], s67 offen offset:2048 sc1
buffer_load_dwordx2 v[64:65], v12, s[44:47], s67 offen offset:3072 sc1
s_waitcnt lgkmcnt(0)
s_barrier
ds_read_b128 v[136:139], v17
ds_read_b128 v[140:143], v17 offset:1024
ds_read_b128 v[144:147], v17 offset:2048
ds_read_b128 v[148:151], v17 offset:3072
ds_read_b128 v[152:155], v17 offset:4096
s_waitcnt vmcnt(8)
v_mfma_f32_16x16x16_bf16 v[96:99], a[222:223], v[36:37], v[96:99]
v_mfma_f32_16x16x16_bf16 v[100:103], a[222:223], v[38:39], v[100:103]
s_waitcnt vmcnt(7)
v_mfma_f32_16x16x16_bf16 v[104:107], a[222:223], v[40:41], v[104:107]
v_mfma_f32_16x16x16_bf16 v[108:111], a[222:223], v[42:43], v[108:111]
s_waitcnt vmcnt(6)
v_mfma_f32_16x16x16_bf16 v[112:115], a[222:223], v[44:45], v[112:115]
s_waitcnt vmcnt(5)
v_mfma_f32_16x16x16_bf16 v[96:99], a[222:223], v[46:47], v[96:99]
v_mfma_f32_16x16x16_bf16 v[100:103], a[222:223], v[48:49], v[100:103]
s_waitcnt vmcnt(4)
v_mfma_f32_16x16x16_bf16 v[104:107], a[222:223], v[50:51], v[104:107]
v_mfma_f32_16x16x16_bf16 v[108:111], a[222:223], v[52:53], v[108:111]
s_waitcnt vmcnt(3)
v_mfma_f32_16x16x16_bf16 v[112:115], a[222:223], v[54:55], v[112:115]
s_waitcnt vmcnt(2)
v_mfma_f32_16x16x16_bf16 v[96:99], a[222:223], v[56:57], v[96:99]
v_mfma_f32_16x16x16_bf16 v[100:103], a[222:223], v[58:59], v[100:103]
s_waitcnt vmcnt(1)
v_mfma_f32_16x16x16_bf16 v[104:107], a[222:223], v[60:61], v[104:107]
v_mfma_f32_16x16x16_bf16 v[108:111], a[222:223], v[62:63], v[108:111]
s_waitcnt vmcnt(0)
v_mfma_f32_16x16x16_bf16 v[112:115], a[222:223], v[64:65], v[112:115]
s_waitcnt lgkmcnt(4)
s_nop 0
v_pk_add_f32 v[96:97], v[96:97], v[136:137]
v_pk_add_f32 v[98:99], v[98:99], v[138:139]
v_cvt_pk_bf16_f32 v176, v96, v97
v_cvt_pk_bf16_f32 v177, v98, v99
ds_write_b64 v19, v[176:177]
s_waitcnt lgkmcnt(4)
v_pk_add_f32 v[100:101], v[100:101], v[140:141]
v_pk_add_f32 v[102:103], v[102:103], v[142:143]
v_cvt_pk_bf16_f32 v178, v100, v101
v_cvt_pk_bf16_f32 v179, v102, v103
ds_write_b64 v19, v[178:179] offset:8
s_waitcnt lgkmcnt(4)
v_pk_add_f32 v[104:105], v[104:105], v[144:145]
v_pk_add_f32 v[106:107], v[106:107], v[146:147]
v_cvt_pk_bf16_f32 v180, v104, v105
v_cvt_pk_bf16_f32 v181, v106, v107
ds_write_b64 v19, v[180:181] offset:16
s_waitcnt lgkmcnt(4)
v_pk_add_f32 v[108:109], v[108:109], v[148:149]
v_pk_add_f32 v[110:111], v[110:111], v[150:151]
v_cvt_pk_bf16_f32 v182, v108, v109
v_cvt_pk_bf16_f32 v183, v110, v111
ds_write_b64 v19, v[182:183] offset:24
s_waitcnt lgkmcnt(4)
v_pk_add_f32 v[112:113], v[112:113], v[152:153]
v_pk_add_f32 v[114:115], v[114:115], v[154:155]
v_cvt_pk_bf16_f32 v184, v112, v113
v_cvt_pk_bf16_f32 v185, v114, v115
ds_write_b64 v19, v[184:185] offset:32
s_waitcnt lgkmcnt(0)
ds_read_b128 v[188:191], v18
ds_read_b128 v[192:195], v18 offset:1024
ds_read_b128 v[196:199], v18 offset:2048
s_waitcnt lgkmcnt(2)
buffer_store_dwordx4 v[188:191], v7, s[48:51], 0 offen
s_waitcnt lgkmcnt(1)
buffer_store_dwordx4 v[192:195], v8, s[48:51], 0 offen offset:1024
s_waitcnt lgkmcnt(0)
buffer_store_dwordx4 v[196:199], v9, s[48:51], 0 offen offset:2048
s_endpgm
.Lpq_8s:
s_mov_b32 s69, 65536
s_mov_b32 s65, 20709376
.Lpq_8t:
buffer_load_dword v77, v76, s[44:47], s65 offen sc1
s_waitcnt vmcnt(0)
v_cmp_ne_u32 vcc, s62, v77
s_cbranch_vccz .Lpq_8
s_sub_u32 s69, s69, 1
s_cbranch_scc0 .Lpq_8t
s_branch .Lpq_8
.Lpq_9s:
s_mov_b32 s69, 65536
s_mov_b32 s65, 20447232
.Lpq_9t:
buffer_load_dword v43, v42, s[44:47], s65 offen sc1
s_waitcnt vmcnt(0)
v_cmp_ne_u32 vcc, s62, v43
s_cbranch_vccz .Lpq_9
s_sub_u32 s69, s69, 1
s_cbranch_scc0 .Lpq_9t
s_branch .Lpq_9
.Lpq_10s:
s_mov_b32 s69, 65536
s_mov_b32 s65, 19398656
.Lpq_10t:
buffer_load_dword v63, v62, s[44:47], s65 offen sc1
s_waitcnt vmcnt(0)
v_cmp_ne_u32 vcc, s62, v63
s_cbranch_vccz .Lpq_10
s_sub_u32 s69, s69, 1
s_cbranch_scc0 .Lpq_10t
s_branch .Lpq_10
.Lpq_11s:
s_mov_b32 s69, 65536
s_mov_b32 s65, 19660800
.Lpq_11t:
buffer_load_dword v229, v228, s[44:47], s65 offen sc1
s_waitcnt vmcnt(0)
v_cmp_ne_u32 vcc, s62, v229
s_cbranch_vccz .Lpq_11
s_sub_u32 s69, s69, 1
s_cbranch_scc0 .Lpq_11t
s_branch .Lpq_11
.Lpq_12s:
s_mov_b32 s69, 65536
s_mov_b32 s65, 19660800
.Lpq_12t:
buffer_load_dword v227, v226, s[44:47], s65 offen sc1
s_waitcnt vmcnt(0)
v_cmp_ne_u32 vcc, s62, v227
s_cbranch_vccz .Lpq_12
s_sub_u32 s69, s69, 1
s_cbranch_scc0 .Lpq_12t
s_branch .Lpq_12
.Lpq_13s:
s_mov_b32 s69, 65536
)ASM"
R"ASM(
s_mov_b32 s65, 19922944
.Lpq_13t:
buffer_load_dword v231, v230, s[44:47], s65 offen sc1
s_waitcnt vmcnt(0)
v_cmp_ne_u32 vcc, s62, v231
s_cbranch_vccz .Lpq_13
s_sub_u32 s69, s69, 1
s_cbranch_scc0 .Lpq_13t
s_branch .Lpq_13
.Lpq_14s:
s_mov_b32 s69, 65536
s_mov_b32 s65, 20185088
.Lpq_14t:
buffer_load_dword v229, v228, s[44:47], s65 offen sc1
s_waitcnt vmcnt(0)
v_cmp_ne_u32 vcc, s62, v229
s_cbranch_vccz .Lpq_14
s_sub_u32 s69, s69, 1
s_cbranch_scc0 .Lpq_14t
s_branch .Lpq_14
.Lend:
)ASM"
  :: "s"(__builtin_amdgcn_kernarg_segment_ptr()), "s"(blockIdx.x), "v"(threadIdx.x)
  : "memory", "scc", "vcc", "s3", "s4", "s5", "s6", "s7", "s8", "s9", "s10", "s11", "s12", "s13", "s14", "s15", "s16", "s17", "s18", "s19", "s20", "s21", "s22", "s23", "s24", "s25", "s26", "s27", "s28", "s29", "s30", "s31", "s33", "s34", "s35", "s36", "s37", "s38", "s39", "s40", "s41", "s42", "s43", "s44", "s45", "s46", "s47", "s48", "s49", "s50", "s51", "s52", "s53", "s54", "s55", "s56", "s57", "s58", "s59", "s60", "s61", "s62", "s63", "s64", "s65", "s66", "s67", "s68", "s69", "s70", "s71", "s72", "s73", "s74", "s75", "s76", "s77", "s78", "s79", "s80", "s81", "s82", "s83", "s84", "s85", "s86", "s87", "s88", "s89", "s90", "s91", "s92", "s93", "s94", "s95", "s96", "s97", "s98", "s99", "v1", "v2", "v3", "v4", "v5", "v6", "v7", "v8", "v9", "v10", "v11", "v12", "v13", "v14", "v15", "v16", "v17", "v18", "v19", "v20", "v21", "v22", "v23", "v24", "v25", "v26", "v27", "v28", "v29", "v30", "v31", "v32", "v33", "v34", "v35", "v36", "v37", "v38", "v39", "v40", "v41", "v42", "v43", "v44", "v45", "v46", "v47", "v48", "v49", "v50", "v51", "v52", "v53", "v54", "v55", "v56", "v57", "v58", "v59", "v60", "v61", "v62", "v63", "v64", "v65", "v66", "v67", "v68", "v69", "v70", "v71", "v72", "v73", "v74", "v75", "v76", "v77", "v78", "v79", "v80", "v81", "v82", "v83", "v84", "v85", "v86", "v87", "v88", "v89", "v90", "v91", "v92", "v93", "v94", "v95", "v96", "v97", "v98", "v99", "v100", "v101", "v102", "v103", "v104", "v105", "v106", "v107", "v108", "v109", "v110", "v111", "v112", "v113", "v114", "v115", "v116", "v117", "v118", "v119", "v120", "v121", "v122", "v123", "v124", "v125", "v126", "v127", "v128", "v129", "v130", "v131", "v132", "v133", "v134", "v135", "v136", "v137", "v138", "v139", "v140", "v141", "v142", "v143", "v144", "v145", "v146", "v147", "v148", "v149", "v150", "v151", "v152", "v153", "v154", "v155", "v156", "v157", "v158", "v159", "v160", "v161", "v162", "v163", "v164", "v165", "v166", "v167", "v168", "v169", "v170", "v171", "v172", "v173", "v174", "v175", "v176", "v177", "v178", "v179", "v180", "v181", "v182", "v183", "v184", "v185", "v186", "v187", "v188", "v189", "v190", "v191", "v192", "v193", "v194", "v195", "v196", "v197", "v198", "v199", "v200", "v201", "v202", "v203", "v204", "v205", "v206", "v207", "v208", "v209", "v210", "v211", "v212", "v213", "v214", "v215", "v216", "v217", "v218", "v219", "v220", "v221", "v222", "v223", "v224", "v225", "v226", "v227", "v228", "v229", "v230", "v231", "v232", "v233", "v234", "v235", "v236", "v237", "v238", "v239", "v240", "v241", "v242", "v243", "v244", "v245", "v246", "v247", "v248", "v249", "v250", "v251", "v252", "v253", "v254", "v255", "a0", "a1", "a2", "a3", "a4", "a5", "a6", "a7", "a8", "a9", "a10", "a11", "a12", "a13", "a14", "a15", "a16", "a17", "a18", "a19", "a20", "a21", "a22", "a23", "a24", "a25", "a26", "a27", "a28", "a29", "a30", "a31", "a32", "a33", "a34", "a35", "a36", "a37", "a38", "a39", "a40", "a41", "a42", "a43", "a44", "a45", "a46", "a47", "a48", "a49", "a50", "a51", "a52", "a53", "a54", "a55", "a56", "a57", "a58", "a59", "a60", "a61", "a62", "a63", "a64", "a65", "a66", "a67", "a68", "a69", "a70", "a71", "a72", "a73", "a74", "a75", "a76", "a77", "a78", "a79", "a80", "a81", "a82", "a83", "a84", "a85", "a86", "a87", "a88", "a89", "a90", "a91", "a92", "a93", "a94", "a95", "a96", "a97", "a98", "a99", "a100", "a101", "a102", "a103", "a104", "a105", "a106", "a107", "a108", "a109", "a110", "a111", "a112", "a113", "a114", "a115", "a116", "a117", "a118", "a119", "a120", "a121", "a122", "a123", "a124", "a125", "a126", "a127", "a128", "a129", "a130", "a131", "a132", "a133", "a134", "a135", "a136", "a137", "a138", "a139", "a140", "a141", "a142", "a143", "a144", "a145", "a146", "a147", "a148", "a149", "a150", "a151", "a152", "a153", "a154", "a155", "a156", "a157", "a158", "a159", "a160", "a161", "a162", "a163", "a164", "a165", "a166", "a167", "a168", "a169", "a170", "a171", "a172", "a173", "a174", "a175", "a176", "a177", "a178", "a179", "a180", "a181", "a182", "a183", "a184", "a185", "a186", "a187", "a188", "a189", "a190", "a191", "a192", "a193", "a194", "a195", "a196", "a197", "a198", "a199", "a200", "a201", "a202", "a203", "a204", "a205", "a206", "a207", "a208", "a209", "a210", "a211", "a212", "a213", "a214", "a215", "a216", "a217", "a218", "a219", "a220", "a221", "a222", "a223", "a224", "a225", "a226", "a227", "a228", "a229", "a230", "a231", "a232", "a233", "a234", "a235", "a236", "a237", "a238", "a239", "a240", "a241", "a242", "a243", "a244", "a245", "a246", "a247", "a248", "a249", "a250", "a251", "a252", "a253", "a254", "a255");
}

extern "C" void kernel_launch(void* const* d_in, const int* in_sizes, int n_in, void* d_out, int out_size, void* d_ws, size_t ws_size,
                              hipStream_t stream) {
  fused_block_kernel<<<256, 256, 0, stream>>>(d_in[0], d_in[1], d_in[2], d_in[3], d_in[5], d_in[6], d_in[7], d_in[8], d_out, d_ws);
}
```
